```python
import torch

def _softmax(x, axis=-1):
    return torch.softmax(x, dim=axis)
def _gelu(x):
    return torch.nn.functional.gelu(x, approximate="tanh")
def _silu(x):
    return torch.nn.functional.silu(x)
def _sigmoid(x):
    return torch.sigmoid(x)
def _layernorm(x, axis=-1, eps=1e-5):
    m = x.mean(dim=axis, keepdim=True)
    v = x.var(dim=axis, keepdim=True, unbiased=False)
    return (x - m) / torch.sqrt(v + eps)
def _scatter_add(tgt, idx, src, axis=0):
    return tgt.clone().index_add_(axis, idx, src)
def _segment_sum(src, seg, n):
    out = torch.zeros((n,) + src.shape[1:], dtype=src.dtype)
    return out.index_add_(0, seg, src)
def _pad(x, pw):
    flat = tuple(v for pair in reversed(pw) for v in pair)
    return torch.nn.functional.pad(x, flat)

kernel_name = 'standalone__mha_softmax__near_ridge'

def setup_inputs():

    _w = (lambda a: (lambda t: t.to(torch.bfloat16) if t.is_floating_point() else t)(torch.as_tensor(a)))
    B, H, Sq, Sk, D = 16, 16, 848, 512, 256
    return {
        "q": _w((lambda *s: torch.randn(*s, dtype=torch.bfloat16) * 0.8)(B, H, Sq, D)),
        "k": _w((lambda *s: torch.randn(*s, dtype=torch.bfloat16) * 0.8)(B, H, Sk, D)),
        "v": _w((lambda *s: torch.randn(*s, dtype=torch.bfloat16) * 0.05)(B, H, Sk, D)),
    }

def reference(q, k, v):
    scale = q.shape[-1] ** -0.5
    scores = torch.einsum("bhqd,bhkd->bhqk", q, k) * scale
    p = _softmax(scores, axis=-1)
    return torch.einsum("bhqk,bhkd->bhqd", p, v)
```

```cpp
#include <hip/hip_runtime.h>
#include <hip/hip_bf16.h>

constexpr int NB = 16, NH = 16, SQ = 848, SK = 512, DD = 256;
typedef float f32x4 __attribute__((ext_vector_type(4)));
typedef __bf16 bf16x8 __attribute__((ext_vector_type(8)));
typedef unsigned u32x4 __attribute__((ext_vector_type(4)));
typedef unsigned u32x2 __attribute__((ext_vector_type(2)));
typedef float f32x2 __attribute__((ext_vector_type(2)));
typedef __bf16 bf16x2 __attribute__((ext_vector_type(2)));

__device__ __forceinline__ unsigned cvtpk(float lo, float hi) {
  f32x2 v = {lo, hi};
  bf16x2 b = __builtin_convertvector(v, bf16x2);
  return __builtin_bit_cast(unsigned, b);
}

__device__ __forceinline__ void safe_tile16(const unsigned short* __restrict__ Qh, const unsigned short* __restrict__ Kh,
                                            const unsigned short* __restrict__ Vh, unsigned short* __restrict__ Oh, int row0, int lane) {
  const int r = lane & 15, g = lane >> 4;
  const float C = 0.0625f * 1.4426950408889634f;
  bf16x8 qf[8];
#pragma unroll
  for (int s = 0; s < 8; ++s) qf[s] = *(const bf16x8*)(Qh + (size_t)(row0 + r) * DD + 32 * s + 8 * g);
  f32x4 o[16];
#pragma unroll
  for (int c = 0; c < 16; ++c) o[c] = f32x4{0.f, 0.f, 0.f, 0.f};
  float m = -INFINITY, l = 0.f;
  for (int k0 = 0; k0 < SK; k0 += 32) {
    f32x4 s0 = {0.f, 0.f, 0.f, 0.f}, s1 = {0.f, 0.f, 0.f, 0.f};
#pragma unroll
    for (int s = 0; s < 8; ++s) {
      const bf16x8 ka = *(const bf16x8*)(Kh + (size_t)(k0 + r) * DD + 32 * s + 8 * g);
      const bf16x8 kb = *(const bf16x8*)(Kh + (size_t)(k0 + 16 + r) * DD + 32 * s + 8 * g);
      s0 = __builtin_amdgcn_mfma_f32_16x16x32_bf16(ka, qf[s], s0, 0, 0, 0);
      s1 = __builtin_amdgcn_mfma_f32_16x16x32_bf16(kb, qf[s], s1, 0, 0, 0);
    }
    float mx = fmaxf(fmaxf(fmaxf(s0[0], s0[1]), fmaxf(s0[2], s0[3])), fmaxf(fmaxf(s1[0], s1[1]), fmaxf(s1[2], s1[3])));
    mx = fmaxf(mx, __shfl_xor(mx, 16));
    mx = fmaxf(mx, __shfl_xor(mx, 32));
    const float mn = fmaxf(m, mx);
    const float al = __builtin_amdgcn_exp2f((m - mn) * C);
    m = mn;
    float p[8], ps = 0.f;
#pragma unroll
    for (int j = 0; j < 4; ++j) {
      p[j] = __builtin_amdgcn_exp2f((s0[j] - mn) * C);
      p[4 + j] = __builtin_amdgcn_exp2f((s1[j] - mn) * C);
      ps += p[j] + p[4 + j];
    }
    l = l * al + ps;
    u32x4 pw = {cvtpk(p[0], p[1]), cvtpk(p[2], p[3]), cvtpk(p[4], p[5]), cvtpk(p[6], p[7])};
    const bf16x8 pb = __builtin_bit_cast(bf16x8, pw);
#pragma unroll
    for (int c = 0; c < 16; ++c) {
      unsigned short e[8];
#pragma unroll
      for (int j = 0; j < 8; ++j) e[j] = Vh[(size_t)(k0 + (j >> 2) * 16 + 4 * g + (j & 3)) * DD + 16 * c + r];
      u32x4 vw = {e[0] | ((unsigned)e[1] << 16), e[2] | ((unsigned)e[3] << 16), e[4] | ((unsigned)e[5] << 16), e[6] | ((unsigned)e[7] << 16)};
      o[c] *= al;
      o[c] = __builtin_amdgcn_mfma_f32_16x16x32_bf16(__builtin_bit_cast(bf16x8, vw), pb, o[c], 0, 0, 0);
    }
  }
  l += __shfl_xor(l, 16);
  l += __shfl_xor(l, 32);
  const float il = 1.f / l;
#pragma unroll
  for (int c = 0; c < 16; ++c) {
    u32x2 w = {cvtpk(o[c][0] * il, o[c][1] * il), cvtpk(o[c][2] * il, o[c][3] * il)};
    *(u32x2*)(Oh + (size_t)(row0 + r) * DD + 16 * c + 4 * g) = w;
  }
}

__global__ __launch_bounds__(256) void attn_base(const unsigned short* __restrict__ Q, const unsigned short* __restrict__ K,
                                                 const unsigned short* __restrict__ V, unsigned short* __restrict__ O) {
  const int wave = blockIdx.x * 4 + (threadIdx.x >> 6), lane = threadIdx.x & 63;
  const int head = wave / 53, tile = wave % 53;
  if (head >= NB * NH) return;
  safe_tile16(Q + (size_t)head * SQ * DD, K + (size_t)head * SK * DD, V + (size_t)head * SK * DD, O + (size_t)head * SQ * DD, tile * 16, lane);
}

extern "C" void kernel_launch(void* const* d_in, const int* in_sizes, int n_in, void* d_out, int out_size, void* d_ws, size_t ws_size,
                              hipStream_t stream) {
  attn_base<<<NB * NH * 53 / 4, 256, 0, stream>>>((const unsigned short*)d_in[0], (const unsigned short*)d_in[1], (const unsigned short*)d_in[2],
                                                  (unsigned short*)d_out);
}
```

```cpp
#include <hip/hip_runtime.h>
#include <hip/hip_bf16.h>

constexpr int NB = 16, NH = 16, SQ = 848, SK = 512, DD = 256;
typedef float f32x4 __attribute__((ext_vector_type(4)));
typedef __bf16 bf16x8 __attribute__((ext_vector_type(8)));
typedef unsigned u32x4 __attribute__((ext_vector_type(4)));
typedef unsigned u32x2 __attribute__((ext_vector_type(2)));
typedef float f32x2 __attribute__((ext_vector_type(2)));
typedef __bf16 bf16x2 __attribute__((ext_vector_type(2)));

__device__ __forceinline__ unsigned cvtpk(float lo, float hi) {
  f32x2 v = {lo, hi};
  bf16x2 b = __builtin_convertvector(v, bf16x2);
  return __builtin_bit_cast(unsigned, b);
}

__device__ __forceinline__ void safe_tile16(const unsigned short* __restrict__ Qr, const unsigned short* __restrict__ Kh,
                                            const unsigned short* __restrict__ Vh, unsigned short* __restrict__ Or, int lane) {
  const int r = lane & 15, g = lane >> 4;
  const float C = 0.0625f * 1.4426950408889634f;
  bf16x8 qf[8];
#pragma unroll
  for (int s = 0; s < 8; ++s) qf[s] = *(const bf16x8*)(Qr + (size_t)r * DD + 32 * s + 8 * g);
  f32x4 o[16];
#pragma unroll
  for (int c = 0; c < 16; ++c) o[c] = f32x4{0.f, 0.f, 0.f, 0.f};
  float m = -INFINITY, l = 0.f;
  for (int k0 = 0; k0 < SK; k0 += 32) {
    f32x4 s0 = {0.f, 0.f, 0.f, 0.f}, s1 = {0.f, 0.f, 0.f, 0.f};
#pragma unroll
    for (int s = 0; s < 8; ++s) {
      const bf16x8 ka = *(const bf16x8*)(Kh + (size_t)(k0 + r) * DD + 32 * s + 8 * g);
      const bf16x8 kb = *(const bf16x8*)(Kh + (size_t)(k0 + 16 + r) * DD + 32 * s + 8 * g);
      s0 = __builtin_amdgcn_mfma_f32_16x16x32_bf16(ka, qf[s], s0, 0, 0, 0);
      s1 = __builtin_amdgcn_mfma_f32_16x16x32_bf16(kb, qf[s], s1, 0, 0, 0);
    }
    float mx = fmaxf(fmaxf(fmaxf(s0[0], s0[1]), fmaxf(s0[2], s0[3])), fmaxf(fmaxf(s1[0], s1[1]), fmaxf(s1[2], s1[3])));
    mx = fmaxf(mx, __shfl_xor(mx, 16));
    mx = fmaxf(mx, __shfl_xor(mx, 32));
    const float mn = fmaxf(m, mx);
    const float al = __builtin_amdgcn_exp2f((m - mn) * C);
    m = mn;
    float p[8], ps = 0.f;
#pragma unroll
    for (int j = 0; j < 4; ++j) {
      p[j] = __builtin_amdgcn_exp2f((s0[j] - mn) * C);
      p[4 + j] = __builtin_amdgcn_exp2f((s1[j] - mn) * C);
      ps += p[j] + p[4 + j];
    }
    l = l * al + ps;
    u32x4 pw = {cvtpk(p[0], p[1]), cvtpk(p[2], p[3]), cvtpk(p[4], p[5]), cvtpk(p[6], p[7])};
    const bf16x8 pb = __builtin_bit_cast(bf16x8, pw);
#pragma unroll
    for (int c = 0; c < 16; ++c) {
      unsigned short e[8];
#pragma unroll
      for (int j = 0; j < 8; ++j) e[j] = Vh[(size_t)(k0 + (j >> 2) * 16 + 4 * g + (j & 3)) * DD + 16 * c + r];
      u32x4 vw = {e[0] | ((unsigned)e[1] << 16), e[2] | ((unsigned)e[3] << 16), e[4] | ((unsigned)e[5] << 16), e[6] | ((unsigned)e[7] << 16)};
      o[c] *= al;
      o[c] = __builtin_amdgcn_mfma_f32_16x16x32_bf16(__builtin_bit_cast(bf16x8, vw), pb, o[c], 0, 0, 0);
    }
  }
  l += __shfl_xor(l, 16);
  l += __shfl_xor(l, 32);
  const float il = 1.f / l;
#pragma unroll
  for (int c = 0; c < 16; ++c) {
    u32x2 w = {cvtpk(o[c][0] * il, o[c][1] * il), cvtpk(o[c][2] * il, o[c][3] * il)};
    *(u32x2*)(Or + (size_t)r * DD + 16 * c + 4 * g) = w;
  }
}

__device__ __forceinline__ unsigned pass_desc(unsigned head0, unsigned r, unsigned p) {
  const unsigned type = (0x27728dd8u >> (8 * r + 2 * p)) & 3u;
  const unsigned tile0 = (0x2c201000u >> (8 * type)) & 63u, ntile = (0x090c1010u >> (8 * type)) & 31u;
  return (((head0 + p) * 53u + tile0) << 13) | (ntile << 3) | (type < 2 ? 4u : 3u);
}

__global__ __launch_bounds__(256, 1) void mha_fwd(const unsigned short* __restrict__ Q, const unsigned short* __restrict__ K,
                                                  const unsigned short* __restrict__ V, unsigned short* __restrict__ O) {
  __shared__ __attribute__((aligned(1024))) char lds[163840];
  const unsigned wg = blockIdx.x;
  unsigned x = threadIdx.x;
  asm volatile(
      "s_load_dwordx4 s[80:83], %1, 0x0\n"
      "s_load_dwordx4 s[84:87], %1, 0x10\n"
      "v_lshrrev_b32 v16, 6, %0\n"
      "v_and_b32 v17, 63, %0\n"
      "v_readfirstlane_b32 s78, v16\n"
      "s_and_b32 s76, %2, 24\n"
      "s_and_b32 s77, %2, 7\n"
      "s_lshl_b32 s77, s77, 5\n"
      "s_lshr_b32 s79, %2, 5\n"
      "s_lshl_b32 s79, s79, 2\n"
      "s_add_u32 s77, s77, s79\n"
      "s_add_u32 s79, s76, 0\n"
      "s_lshr_b32 s79, 0x27728dd8, s79\n"
      "s_and_b32 s79, s79, 3\n"
      "s_cmp_lt_u32 s79, 2\n"
      "s_cselect_b32 s63, 4, 3\n"
      "s_lshl_b32 s79, s79, 3\n"
      "s_lshr_b32 s75, 0x48608080, s79\n"
      "s_and_b32 s75, s75, 0xf8\n"
      "s_or_b32 s63, s63, s75\n"
      "s_lshr_b32 s75, 0x2c201000, s79\n"
      "s_and_b32 s75, s75, 63\n"
      "s_add_u32 s79, s77, 0\n"
      "s_mul_i32 s79, s79, 53\n"
      "s_add_u32 s79, s79, s75\n"
      "s_lshl_b32 s79, s79, 13\n"
      "s_or_b32 s63, s63, s79\n"
      "v_bfe_u32 v18, v17, 2, 3\n"
      "v_lshrrev_b32 v19, 2, v18\n"
      "v_lshlrev_b32 v19, 1, v19\n"
      "v_and_b32 v20, 3, v17\n"
      "v_xor_b32 v20, v20, v19\n"
      "v_lshrrev_b32 v21, 5, v17\n"
      "v_lshl_add_u32 v20, v21, 2, v20\n"
      "v_lshlrev_b32 v20, 4, v20\n"
      "v_lshrrev_b32 v21, 1, v18\n"
      "v_and_b32 v22, 1, v18\n"
      "v_lshl_add_u32 v23, v21, 3, v22\n"
      "v_lshl_add_u32 v4, v23, 9, v20\n"
      "v_lshl_add_u32 v23, v21, 2, v22\n"
      "v_lshl_add_u32 v5, v23, 9, v20\n"
      "s_mov_b32 s93, s78\n"
      "s_lshl_b32 s52, s78, 7\n"
      "s_lshl_b32 s79, s78, 12\n"
      "s_add_u32 s54, s79, %3\n"
      "s_mul_i32 s79, s78, 0x6000\n"
      "s_add_u32 s79, s79, %3\n"
      "s_add_u32 s58, s79, 0x10000\n"
      "s_mov_b32 s38, 0x100000\n"
      "s_mov_b32 s39, 0x20000\n"
      "s_mov_b32 s47, 0x20000\n"
      "s_lshl_b32 s74, s77, 18\n"
      "s_waitcnt lgkmcnt(0)\n"
      "s_add_u32 s36, s82, s74\n"
      "s_addc_u32 s37, s83, 0\n"
      "s_add_u32 s40, s84, s74\n"
      "s_addc_u32 s41, s85, 0\n"
      "s_and_b32 s79, s63, 0xffffe000\n"
      "s_add_u32 s44, s80, s79\n"
      "s_addc_u32 s45, s81, 0\n"
      "s_and_b32 s79, s63, 0xf8\n"
      "s_lshl_b32 s46, s79, 10\n"
      "s_and_b32 s79, s63, 7\n"
      "s_mul_i32 s79, s79, s93\n"
      "s_lshl_b32 s92, s79, 13\n"
      "s_mov_b32 m0, s54\n"
      "s_nop 0\n"
      "buffer_load_dwordx4 v4, s[36:39], s52 offen offset:0 sc1 lds\n"
      "buffer_load_dwordx4 v4, s[36:39], s52 offen offset:1024 sc1 lds\n"
      "buffer_load_dwordx4 v4, s[36:39], s52 offen offset:2048 sc1 lds\n"
      "buffer_load_dwordx4 v4, s[36:39], s52 offen offset:3072 sc1 lds\n"
      "s_add_u32 m0, s58, 0\n"
      "s_add_u32 s75, s92, 0\n"
      "buffer_load_dwordx4 v5, s[44:47], s75 offen offset:0 nt lds\n"
      "buffer_load_dwordx4 v5, s[44:47], s75 offen offset:1024 nt lds\n"
      "s_add_u32 m0, s58, 2048\n"
      "s_add_u32 s75, s92, 128\n"
      "buffer_load_dwordx4 v5, s[44:47], s75 offen offset:0 nt lds\n"
      "buffer_load_dwordx4 v5, s[44:47], s75 offen offset:1024 nt lds\n"
      "s_add_u32 m0, s58, 4096\n"
      "s_add_u32 s75, s92, 256\n"
      "buffer_load_dwordx4 v5, s[44:47], s75 offen offset:0 nt lds\n"
      "buffer_load_dwordx4 v5, s[44:47], s75 offen offset:1024 nt lds\n"
      "s_add_u32 m0, s58, 6144\n"
      "s_add_u32 s75, s92, 384\n"
      "buffer_load_dwordx4 v5, s[44:47], s75 offen offset:0 nt lds\n"
      "buffer_load_dwordx4 v5, s[44:47], s75 offen offset:1024 nt lds\n"
      "s_add_u32 m0, s58, 8192\n"
      "s_add_u32 s75, s92, 8192\n"
      "buffer_load_dwordx4 v5, s[44:47], s75 offen offset:0 nt lds\n"
      "buffer_load_dwordx4 v5, s[44:47], s75 offen offset:1024 nt lds\n"
      "s_add_u32 m0, s58, 10240\n"
      "s_add_u32 s75, s92, 8320\n"
      "buffer_load_dwordx4 v5, s[44:47], s75 offen offset:0 nt lds\n"
      "buffer_load_dwordx4 v5, s[44:47], s75 offen offset:1024 nt lds\n"
      "s_add_u32 m0, s58, 12288\n"
      "s_add_u32 s75, s92, 8448\n"
      "buffer_load_dwordx4 v5, s[44:47], s75 offen offset:0 nt lds\n"
      "buffer_load_dwordx4 v5, s[44:47], s75 offen offset:1024 nt lds\n"
      "s_add_u32 m0, s58, 14336\n"
      "s_add_u32 s75, s92, 8576\n"
      "buffer_load_dwordx4 v5, s[44:47], s75 offen offset:0 nt lds\n"
      "buffer_load_dwordx4 v5, s[44:47], s75 offen offset:1024 nt lds\n"
      "s_mov_b32 s78, s93\n"
      "s_add_u32 s79, s76, 2\n"
      "s_lshr_b32 s79, 0x27728dd8, s79\n"
      "s_and_b32 s79, s79, 3\n"
      "s_cmp_lt_u32 s79, 2\n"
      "s_cselect_b32 s64, 4, 3\n"
      "s_lshl_b32 s79, s79, 3\n"
      "s_lshr_b32 s75, 0x48608080, s79\n"
      "s_and_b32 s75, s75, 0xf8\n"
      "s_or_b32 s64, s64, s75\n"
      "s_lshr_b32 s75, 0x2c201000, s79\n"
      "s_and_b32 s75, s75, 63\n"
      "s_add_u32 s79, s77, 1\n"
      "s_mul_i32 s79, s79, 53\n"
      "s_add_u32 s79, s79, s75\n"
      "s_lshl_b32 s79, s79, 13\n"
      "s_or_b32 s64, s64, s79\n"
      "s_add_u32 s79, s76, 4\n"
      "s_lshr_b32 s79, 0x27728dd8, s79\n"
      "s_and_b32 s79, s79, 3\n"
      "s_cmp_lt_u32 s79, 2\n"
      "s_cselect_b32 s65, 4, 3\n"
      "s_lshl_b32 s79, s79, 3\n"
      "s_lshr_b32 s75, 0x48608080, s79\n"
      "s_and_b32 s75, s75, 0xf8\n"
      "s_or_b32 s65, s65, s75\n"
      "s_lshr_b32 s75, 0x2c201000, s79\n"
      "s_and_b32 s75, s75, 63\n"
      "s_add_u32 s79, s77, 2\n"
      "s_mul_i32 s79, s79, 53\n"
      "s_add_u32 s79, s79, s75\n"
      "s_lshl_b32 s79, s79, 13\n"
      "s_or_b32 s65, s65, s79\n"
      "s_add_u32 s79, s76, 6\n"
      "s_lshr_b32 s79, 0x27728dd8, s79\n"
      "s_and_b32 s79, s79, 3\n"
      "s_cmp_lt_u32 s79, 2\n"
      "s_cselect_b32 s66, 4, 3\n"
      "s_lshl_b32 s79, s79, 3\n"
      "s_lshr_b32 s75, 0x48608080, s79\n"
      "s_and_b32 s75, s75, 0xf8\n"
      "s_or_b32 s66, s66, s75\n"
      "s_lshr_b32 s75, 0x2c201000, s79\n"
      "s_and_b32 s75, s75, 63\n"
      "s_add_u32 s79, s77, 3\n"
      "s_mul_i32 s79, s79, 53\n"
      "s_add_u32 s79, s79, s75\n"
      "s_lshl_b32 s79, s79, 13\n"
      "s_or_b32 s66, s66, s79\n"
      "s_mov_b32 s42, 0x100000\n"
      "s_mov_b32 s43, 0x20000\n"
      "s_mov_b32 s82, s86\n"
      "s_mov_b32 s83, s87\n"
      "s_mov_b32 s51, 0x20000\n"
      "s_mov_b32 s48, s86\n"
      "s_mov_b32 s49, s87\n"
      "s_mov_b32 s50, 0\n"
      "s_mov_b32 s91, 0\n"
      "s_and_b32 s79, s78, 1\n"
      "s_lshl_b32 s53, s79, 7\n"
      "s_lshl_b32 s56, s79, 12\n"
      "s_lshr_b32 s79, s78, 1\n"
      "s_lshl_b32 s79, s79, 11\n"
      "s_add_u32 s53, s53, s79\n"
      "s_add_u32 s90, s53, 0x100\n"
      "s_add_u32 s56, s56, s79\n"
      "s_add_u32 s56, s56, %3\n"
      "s_add_u32 s56, s56, 0x8000\n"
      "s_add_u32 s57, s56, 0x4000\n"
      "s_add_u32 s55, s54, 0x4000\n"
      "s_mov_b32 s59, 0x3db8aa3b\n"
      "s_mov_b32 s69, 0xbdb8aa3b\n"
      "s_mov_b32 s60, 0x4e800000\n"
      "s_mov_b32 s62, 3\n"
      "s_mov_b32 s67, 0\n"
      "s_mov_b32 s68, 1\n"
      "s_mov_b64 s[70:71], 0\n"
      "v_and_b32 v18, 15, v17\n"
      "v_lshrrev_b32 v19, 4, v17\n"
      "v_lshrrev_b32 v20, 2, v18\n"
      "v_lshrrev_b32 v21, 3, v18\n"
      "v_xor_b32 v20, v20, v21\n"
      "v_and_b32 v20, 1, v20\n"
      "v_and_b32 v21, 7, v18\n"
      "v_lshrrev_b32 v22, 2, v21\n"
      "v_lshlrev_b32 v22, 1, v22\n"
      "v_xor_b32 v22, v22, v19\n"
      "v_lshlrev_b32 v22, 4, v22\n"
      "v_lshl_add_u32 v22, v21, 6, v22\n"
      "v_lshl_add_u32 v22, v20, 10, v22\n"
      "v_add_u32 v1, %3, v22\n"
      "v_add_u32 v12, s58, v22\n"
      "v_lshrrev_b32 v22, 1, v21\n"
      "v_lshlrev_b32 v22, 2, v22\n"
      "v_and_b32 v23, 1, v21\n"
      "v_add_u32 v22, v22, v23\n"
      "v_lshl_add_u32 v22, v20, 1, v22\n"
      "v_lshlrev_b32 v23, 4, v19\n"
      "v_lshl_add_u32 v13, v22, 9, v23\n"
      "v_and_b32 v23, 1, v19\n"
      "v_lshlrev_b32 v23, 1, v23\n"
      "v_lshrrev_b32 v24, 1, v19\n"
      "v_add_u32 v23, v23, v24\n"
      "v_lshlrev_b32 v23, 4, v23\n"
      "v_lshl_add_u32 v44, v22, 9, v23\n"
      "v_and_b32 v25, 7, v22\n"
      "v_xor_b32 v23, v25, v24\n"
      "v_lshlrev_b32 v23, 4, v23\n"
      "v_and_b32 v24, 1, v19\n"
      "v_lshl_add_u32 v23, v24, 3, v23\n"
      "v_lshl_add_u32 v23, v25, 7, v23\n"
      "v_lshrrev_b32 v25, 3, v22\n"
      "v_lshl_add_u32 v23, v25, 12, v23\n"
      "v_xor_b32 v24, 0, v23\n"
      "v_add_u32 v8, s58, v24\n"
      "v_xor_b32 v24, 32, v23\n"
      "v_add_u32 v9, s58, v24\n"
      "v_xor_b32 v24, 64, v23\n"
      "v_add_u32 v10, s58, v24\n"
      "v_xor_b32 v24, 96, v23\n"
      "v_add_u32 v11, s58, v24\n"
      "v_lshrrev_b32 v18, 2, v17\n"
      "v_lshrrev_b32 v20, 2, v18\n"
      "v_lshrrev_b32 v21, 3, v18\n"
      "v_xor_b32 v20, v20, v21\n"
      "v_and_b32 v20, 1, v20\n"
      "v_and_b32 v21, 7, v18\n"
      "v_lshrrev_b32 v22, 2, v21\n"
      "v_lshlrev_b32 v22, 1, v22\n"
      "v_bfe_u32 v23, v17, 1, 1\n"
      "v_xor_b32 v22, v22, v23\n"
      "v_lshlrev_b32 v22, 4, v22\n"
      "v_and_b32 v23, 1, v17\n"
      "v_lshl_add_u32 v22, v23, 3, v22\n"
      "v_lshl_add_u32 v22, v21, 6, v22\n"
      "v_lshl_add_u32 v22, v20, 10, v22\n"
      "v_add_u32 v2, %3, v22\n"
      "v_xor_b32 v22, 32, v22\n"
      "v_add_u32 v3, %3, v22\n"
      "v_lshrrev_b32 v18, 3, v17\n"
      "v_and_b32 v19, 7, v17\n"
      "v_lshlrev_b32 v20, 4, v19\n"
      "v_lshl_add_u32 v7, v18, 9, v20\n"
      "v_xor_b32 v19, v19, v18\n"
      "v_lshlrev_b32 v20, 4, v19\n"
      "v_lshl_add_u32 v20, v18, 7, v20\n"
      "v_add_u32 v6, s58, v20\n"
      "s_and_b32 s98, s93, 0\n"
      "s_nop 4\n"
      "s_and_b32 s78, s63, 7\n"
      "s_cmp_eq_u32 s78, 4\n"
      "s_cbranch_scc0 LSTART3_%=\n"
      "s_waitcnt vmcnt(16)\n"
      "s_barrier\n"
      "ds_read_b128 a[128:131], v1 offset:0\n"
      "ds_read_b128 a[132:135], v1 offset:512\n"
      "ds_read_b128 a[136:139], v1 offset:4096\n"
      "ds_read_b128 a[140:143], v1 offset:4608\n"
      "ds_read_b128 a[144:147], v1 offset:8192\n"
      "ds_read_b128 a[148:151], v1 offset:8704\n"
      "ds_read_b128 a[152:155], v1 offset:12288\n"
      "ds_read_b128 a[156:159], v1 offset:12800\n"
      "ds_read_b128 a[192:195], v1 offset:2048\n"
      "ds_read_b128 a[196:199], v1 offset:2560\n"
      "ds_read_b128 a[200:203], v1 offset:6144\n"
      "ds_read_b128 a[204:207], v1 offset:6656\n"
      "ds_read_b128 a[208:211], v1 offset:10240\n"
      "ds_read_b128 a[212:215], v1 offset:10752\n"
      "ds_read_b128 a[216:219], v1 offset:14336\n"
      "ds_read_b128 a[220:223], v1 offset:14848\n"
      "s_waitcnt lgkmcnt(0)\n"
      "s_barrier\n"
      "s_add_u32 m0, s58, 16384\n"
      "s_add_u32 s75, s92, 16384\n"
      "buffer_load_dwordx4 v5, s[44:47], s75 offen offset:0 nt lds\n"
      "buffer_load_dwordx4 v5, s[44:47], s75 offen offset:1024 nt lds\n"
      "s_add_u32 m0, s58, 18432\n"
      "s_add_u32 s75, s92, 16512\n"
      "buffer_load_dwordx4 v5, s[44:47], s75 offen offset:0 nt lds\n"
      "buffer_load_dwordx4 v5, s[44:47], s75 offen offset:1024 nt lds\n"
      "s_add_u32 m0, s58, 20480\n"
      "s_add_u32 s75, s92, 16640\n"
      "buffer_load_dwordx4 v5, s[44:47], s75 offen offset:0 nt lds\n"
      "buffer_load_dwordx4 v5, s[44:47], s75 offen offset:1024 nt lds\n"
      "s_add_u32 m0, s58, 22528\n"
      "s_add_u32 s75, s92, 16768\n"
      "buffer_load_dwordx4 v5, s[44:47], s75 offen offset:0 nt lds\n"
      "buffer_load_dwordx4 v5, s[44:47], s75 offen offset:1024 nt lds\n"
      "s_nop 0\n"
      "s_waitcnt vmcnt(22)\n"
      "ds_read_b128 a[0:3], v12 offset:0\n"
      "s_waitcnt lgkmcnt(0)\n"
      "v_mfma_f32_16x16x32_bf16 v[36:39], a[128:131], a[0:3], 0\n"
      "s_mov_b32 s61, 0\n"
      "ds_read_b128 a[4:7], v12 offset:512\n"
      "s_waitcnt vmcnt(20)\n"
      "ds_read_b128 a[8:11], v12 offset:2048\n"
      "ds_read_b128 a[12:15], v12 offset:2560\n"
      "s_waitcnt vmcnt(18)\n"
      "ds_read_b128 a[16:19], v12 offset:4096\n"
      "ds_read_b128 a[20:23], v12 offset:4608\n"
      "s_waitcnt vmcnt(16)\n"
      "ds_read_b128 a[24:27], v12 offset:6144\n"
      "ds_read_b128 a[28:31], v12 offset:6656\n"
      "s_waitcnt lgkmcnt(6)\n"
      "v_mfma_f32_16x16x32_bf16 v[36:39], a[132:135], a[4:7], v[36:39]\n"
      "s_waitcnt lgkmcnt(5)\n"
      "v_mfma_f32_16x16x32_bf16 v[36:39], a[136:139], a[8:11], v[36:39]\n"
      "s_waitcnt lgkmcnt(4)\n"
      "v_mfma_f32_16x16x32_bf16 v[36:39], a[140:143], a[12:15], v[36:39]\n"
      "s_waitcnt lgkmcnt(3)\n"
      "v_mfma_f32_16x16x32_bf16 v[36:39], a[144:147], a[16:19], v[36:39]\n"
      "s_waitcnt lgkmcnt(2)\n"
      "v_mfma_f32_16x16x32_bf16 v[36:39], a[148:151], a[20:23], v[36:39]\n"
      "s_waitcnt lgkmcnt(1)\n"
      "v_mfma_f32_16x16x32_bf16 v[36:39], a[152:155], a[24:27], v[36:39]\n"
      "s_waitcnt lgkmcnt(0)\n"
      "v_mfma_f32_16x16x32_bf16 v[36:39], a[156:159], a[28:31], v[36:39]\n"
      "s_mov_b32 s96, 0x30000\n"
      "s_nop 0\n"
      "s_nop 0\n"
      "v_mfma_f32_16x16x32_bf16 v[40:43], a[192:195], a[0:3], 0\n"
      "s_mov_b32 s97, 0x10000000\n"
      "s_nop 0\n"
      "s_nop 0\n"
      "v_mfma_f32_16x16x32_bf16 v[40:43], a[196:199], a[4:7], v[40:43]\n"
      "v_max3_f32 v28, v36, v37, v38\n"
      "v_max_f32 v28, v28, v39\n"
      "v_mov_b32 v0, v28\n"
      "v_mfma_f32_16x16x32_bf16 v[40:43], a[200:203], a[8:11], v[40:43]\n"
      "s_nop 0\n"
      "v_permlane32_swap_b32 v28, v0\n"
      "v_max_f32 v28, v28, v0\n"
      "v_mov_b32 v0, v28\n"
      "v_mfma_f32_16x16x32_bf16 v[40:43], a[204:207], a[12:15], v[40:43]\n"
      "s_nop 0\n"
      "v_permlane16_swap_b32 v28, v0\n"
      "v_max_f32 v28, v28, v0\n"
      "v_mul_f32 v28, s69, v28\n"
      "v_fma_f32 v36, v36, s59, v28\n"
      "v_fma_f32 v37, v37, s59, v28\n"
      "v_fma_f32 v38, v38, s59, v28\n"
      "v_fma_f32 v39, v39, s59, v28\n"
      "v_exp_f32 v36, v36\n"
      "v_exp_f32 v37, v37\n"
      "v_exp_f32 v38, v38\n"
      "v_exp_f32 v39, v39\n"
      "v_cvt_pk_bf16_f32 v48, v36, v37\n"
      "v_add_f32 v32, v36, v37\n"
      "v_cvt_pk_bf16_f32 v49, v38, v39\n"
      "v_add_f32 v32, v32, v38\n"
      "v_add_f32 v32, v32, v39\n"
      "v_mfma_f32_16x16x32_bf16 v[40:43], a[208:211], a[16:19], v[40:43]\n"
      "v_mfma_f32_16x16x32_bf16 v[40:43], a[212:215], a[20:23], v[40:43]\n"
      "v_mfma_f32_16x16x32_bf16 v[40:43], a[216:219], a[24:27], v[40:43]\n"
      "v_mfma_f32_16x16x32_bf16 v[40:43], a[220:223], a[28:31], v[40:43]\n"
      "s_nop 0\n"
      "s_nop 0\n"
      "s_nop 0\n"
      "s_nop 4\n"
      "v_fma_f32 v40, v40, s59, v28\n"
      "v_exp_f32 v40, v40\n"
      "v_fma_f32 v41, v41, s59, v28\n"
      "v_exp_f32 v41, v41\n"
      "s_nop 0\n"
      "v_cvt_pk_bf16_f32 v50, v40, v41\n"
      "v_fma_f32 v42, v42, s59, v28\n"
      "v_exp_f32 v42, v42\n"
      "v_fma_f32 v43, v43, s59, v28\n"
      "v_exp_f32 v43, v43\n"
      "s_nop 0\n"
      "v_cvt_pk_bf16_f32 v51, v42, v43\n"
      "v_add_f32 v32, v32, v40\n"
      "v_add_f32 v32, v32, v41\n"
      "v_add_f32 v32, v32, v42\n"
      "v_add_f32 v32, v32, v43\n"
      "s_nop 0\n"
      "s_add_u32 s94, s92, 0x6000\n"
      "buffer_load_dwordx4 a[96:99], v13, s[44:47], s94 offen offset:0\n"
      "buffer_load_dwordx4 a[100:103], v13, s[44:47], s94 offen offset:64\n"
      "buffer_load_dwordx4 a[104:107], v13, s[44:47], s94 offen offset:128\n"
      "buffer_load_dwordx4 a[108:111], v13, s[44:47], s94 offen offset:192\n"
      "buffer_load_dwordx4 a[112:115], v13, s[44:47], s94 offen offset:256\n"
      "buffer_load_dwordx4 a[116:119], v13, s[44:47], s94 offen offset:320\n"
      "buffer_load_dwordx4 a[120:123], v13, s[44:47], s94 offen offset:384\n"
      "buffer_load_dwordx4 a[124:127], v13, s[44:47], s94 offen offset:448\n"
      "s_nop 0\n"
      "s_waitcnt vmcnt(22)\n"
      "ds_read_b128 a[32:35], v12 offset:8192\n"
      "s_waitcnt lgkmcnt(0)\n"
      "v_mfma_f32_16x16x32_bf16 v[36:39], a[128:131], a[32:35], 0\n"
      "ds_read_b128 a[36:39], v12 offset:8704\n"
      "s_waitcnt vmcnt(20)\n"
      "ds_read_b128 a[40:43], v12 offset:10240\n"
      "ds_read_b128 a[44:47], v12 offset:10752\n"
      "s_waitcnt vmcnt(18)\n"
      "ds_read_b128 a[48:51], v12 offset:12288\n"
      "ds_read_b128 a[52:55], v12 offset:12800\n"
      "s_waitcnt vmcnt(16)\n"
      "ds_read_b128 a[56:59], v12 offset:14336\n"
      "ds_read_b128 a[60:63], v12 offset:14848\n"
      "s_waitcnt lgkmcnt(6)\n"
      "v_mfma_f32_16x16x32_bf16 v[36:39], a[132:135], a[36:39], v[36:39]\n"
      "s_waitcnt lgkmcnt(5)\n"
      "v_mfma_f32_16x16x32_bf16 v[36:39], a[136:139], a[40:43], v[36:39]\n"
      "s_waitcnt lgkmcnt(4)\n"
      "v_mfma_f32_16x16x32_bf16 v[36:39], a[140:143], a[44:47], v[36:39]\n"
      "s_waitcnt lgkmcnt(3)\n"
      "v_mfma_f32_16x16x32_bf16 v[36:39], a[144:147], a[48:51], v[36:39]\n"
      "s_waitcnt lgkmcnt(2)\n"
      "v_mfma_f32_16x16x32_bf16 v[36:39], a[148:151], a[52:55], v[36:39]\n"
      "s_waitcnt lgkmcnt(1)\n"
      "v_mfma_f32_16x16x32_bf16 v[36:39], a[152:155], a[56:59], v[36:39]\n"
      "s_waitcnt lgkmcnt(0)\n"
      "v_mfma_f32_16x16x32_bf16 v[36:39], a[156:159], a[60:63], v[36:39]\n"
      "s_nop 0\n"
      "s_nop 0\n"
      "s_nop 0\n"
      "v_mfma_f32_16x16x32_bf16 v[40:43], a[192:195], a[32:35], 0\n"
      "s_nop 0\n"
      "s_nop 0\n"
      "s_nop 0\n"
      "v_mfma_f32_16x16x32_bf16 v[40:43], a[196:199], a[36:39], v[40:43]\n"
      "v_max3_f32 v29, v36, v37, v38\n"
      "v_max_f32 v29, v29, v39\n"
      "v_mov_b32 v0, v29\n"
      "v_mfma_f32_16x16x32_bf16 v[40:43], a[200:203], a[40:43], v[40:43]\n"
      "s_nop 0\n"
      "v_permlane32_swap_b32 v29, v0\n"
      "v_max_f32 v29, v29, v0\n"
      "v_mov_b32 v0, v29\n"
      "v_mfma_f32_16x16x32_bf16 v[40:43], a[204:207], a[44:47], v[40:43]\n"
      "s_nop 0\n"
      "v_permlane16_swap_b32 v29, v0\n"
      "v_max_f32 v29, v29, v0\n"
      "v_mul_f32 v29, s69, v29\n"
      "v_fma_f32 v36, v36, s59, v29\n"
      "v_fma_f32 v37, v37, s59, v29\n"
      "v_fma_f32 v38, v38, s59, v29\n"
      "v_fma_f32 v39, v39, s59, v29\n"
      "v_exp_f32 v36, v36\n"
      "v_exp_f32 v37, v37\n"
      "v_exp_f32 v38, v38\n"
      "v_exp_f32 v39, v39\n"
      "v_cvt_pk_bf16_f32 v52, v36, v37\n"
      "v_add_f32 v33, v36, v37\n"
      "v_cvt_pk_bf16_f32 v53, v38, v39\n"
      "v_add_f32 v33, v33, v38\n"
      "v_add_f32 v33, v33, v39\n"
      "v_mfma_f32_16x16x32_bf16 v[40:43], a[208:211], a[48:51], v[40:43]\n"
      "v_mfma_f32_16x16x32_bf16 v[40:43], a[212:215], a[52:55], v[40:43]\n"
      "v_mfma_f32_16x16x32_bf16 v[40:43], a[216:219], a[56:59], v[40:43]\n"
      "v_mfma_f32_16x16x32_bf16 v[40:43], a[220:223], a[60:63], v[40:43]\n"
      "s_nop 0\n"
      "s_nop 0\n"
      "s_nop 0\n"
      "s_nop 4\n"
      "v_fma_f32 v40, v40, s59, v29\n"
      "v_exp_f32 v40, v40\n"
      "v_fma_f32 v41, v41, s59, v29\n"
      "v_exp_f32 v41, v41\n"
      "s_nop 0\n"
      "v_cvt_pk_bf16_f32 v54, v40, v41\n"
      "v_fma_f32 v42, v42, s59, v29\n"
      "v_exp_f32 v42, v42\n"
      "v_fma_f32 v43, v43, s59, v29\n"
      "v_exp_f32 v43, v43\n"
      "s_nop 0\n"
      "v_cvt_pk_bf16_f32 v55, v42, v43\n"
      "v_add_f32 v33, v33, v40\n"
      "v_add_f32 v33, v33, v41\n"
      "v_add_f32 v33, v33, v42\n"
      "v_add_f32 v33, v33, v43\n"
      "s_nop 0\n"
      "s_add_u32 s52, s52, 0x4000\n"
      "s_mov_b32 m0, s55\n"
      "s_nop 0\n"
      "buffer_load_dwordx4 v4, s[36:39], s52 offen offset:0 sc1 lds\n"
      "buffer_load_dwordx4 v4, s[36:39], s52 offen offset:1024 sc1 lds\n"
      "s_add_u32 m0, s56, 0x0\n"
      "s_nop 0\n"
      "buffer_load_dwordx4 v4, s[40:43], s53 offen offset:0 sc1 lds\n"
      "buffer_load_dwordx4 v4, s[40:43], s53 offen offset:1024 sc1 lds\n"
      "s_mov_b32 m0, s55\n"
      "s_nop 0\n"
      "buffer_load_dwordx4 v4, s[36:39], s52 offen offset:2048 sc1 lds\n"
      "buffer_load_dwordx4 v4, s[36:39], s52 offen offset:3072 sc1 lds\n"
      "s_add_u32 m0, s56, 0x2000\n"
      "s_nop 0\n"
      "buffer_load_dwordx4 v4, s[40:43], s90 offen offset:0 sc1 lds\n"
      "buffer_load_dwordx4 v4, s[40:43], s90 offen offset:1024 sc1 lds\n"
      "s_add_u32 s52, s52, 0x4000\n"
      "s_mov_b32 m0, s54\n"
      "s_nop 0\n"
      "buffer_load_dwordx4 v4, s[36:39], s52 offen offset:0 sc1 lds\n"
      "buffer_load_dwordx4 v4, s[36:39], s52 offen offset:1024 sc1 lds\n"
      "s_add_u32 s53, s53, 0x4000\n"
      "s_mov_b32 m0, s57\n"
      "s_nop 0\n"
      "buffer_load_dwordx4 v4, s[40:43], s53 offen offset:0 sc1 lds\n"
      "buffer_load_dwordx4 v4, s[40:43], s53 offen offset:1024 sc1 lds\n"
      "s_nop 0\n"
      "s_waitcnt vmcnt(26)\n"
      "ds_read_b128 a[64:67], v12 offset:16384\n"
      "s_waitcnt lgkmcnt(0)\n"
      "v_mfma_f32_16x16x32_bf16 v[36:39], a[128:131], a[64:67], 0\n"
      "s_add_u32 s90, s90, 0x4000\n"
      "ds_read_b128 a[68:71], v12 offset:16896\n"
      "s_waitcnt vmcnt(24)\n"
      "ds_read_b128 a[72:75], v12 offset:18432\n"
      "ds_read_b128 a[76:79], v12 offset:18944\n"
      "s_waitcnt vmcnt(22)\n"
      "ds_read_b128 a[80:83], v12 offset:20480\n"
      "ds_read_b128 a[84:87], v12 offset:20992\n"
      "s_waitcnt vmcnt(20)\n"
      "ds_read_b128 a[88:91], v12 offset:22528\n"
      "ds_read_b128 a[92:95], v12 offset:23040\n"
      "s_waitcnt lgkmcnt(6)\n"
      "v_mfma_f32_16x16x32_bf16 v[36:39], a[132:135], a[68:71], v[36:39]\n"
      "s_waitcnt lgkmcnt(5)\n"
      "v_mfma_f32_16x16x32_bf16 v[36:39], a[136:139], a[72:75], v[36:39]\n"
      "s_waitcnt lgkmcnt(4)\n"
      "v_mfma_f32_16x16x32_bf16 v[36:39], a[140:143], a[76:79], v[36:39]\n"
      "s_waitcnt lgkmcnt(3)\n"
      "v_mfma_f32_16x16x32_bf16 v[36:39], a[144:147], a[80:83], v[36:39]\n"
      "s_waitcnt lgkmcnt(2)\n"
      "v_mfma_f32_16x16x32_bf16 v[36:39], a[148:151], a[84:87], v[36:39]\n"
      "s_waitcnt lgkmcnt(1)\n"
      "v_mfma_f32_16x16x32_bf16 v[36:39], a[152:155], a[88:91], v[36:39]\n"
      "s_waitcnt lgkmcnt(0)\n"
      "v_mfma_f32_16x16x32_bf16 v[36:39], a[156:159], a[92:95], v[36:39]\n"
      "s_and_b32 s79, s64, 0xffffe000\n"
      "s_nop 0\n"
      "s_nop 0\n"
      "v_mfma_f32_16x16x32_bf16 v[40:43], a[192:195], a[64:67], 0\n"
      "s_add_u32 s44, s80, s79\n"
      "s_nop 0\n"
      "s_nop 0\n"
      "v_mfma_f32_16x16x32_bf16 v[40:43], a[196:199], a[68:71], v[40:43]\n"
      "s_addc_u32 s45, s81, 0\n"
      "v_max3_f32 v30, v36, v37, v38\n"
      "v_max_f32 v30, v30, v39\n"
      "v_mov_b32 v0, v30\n"
      "v_mfma_f32_16x16x32_bf16 v[40:43], a[200:203], a[72:75], v[40:43]\n"
      "s_and_b32 s79, s64, 0xf8\n"
      "v_permlane32_swap_b32 v30, v0\n"
      "v_max_f32 v30, v30, v0\n"
      "v_mov_b32 v0, v30\n"
      "v_mfma_f32_16x16x32_bf16 v[40:43], a[204:207], a[76:79], v[40:43]\n"
      "s_lshl_b32 s46, s79, 10\n"
      "v_permlane16_swap_b32 v30, v0\n"
      "v_max_f32 v30, v30, v0\n"
      "v_mul_f32 v30, s69, v30\n"
      "v_fma_f32 v36, v36, s59, v30\n"
      "v_mfma_f32_16x16x32_bf16 v[40:43], a[208:211], a[80:83], v[40:43]\n"
      "s_and_b32 s79, s64, 7\n"
      "v_fma_f32 v37, v37, s59, v30\n"
      "v_fma_f32 v38, v38, s59, v30\n"
      "v_fma_f32 v39, v39, s59, v30\n"
      "v_mfma_f32_16x16x32_bf16 v[40:43], a[212:215], a[84:87], v[40:43]\n"
      "s_mul_i32 s79, s79, s93\n"
      "v_exp_f32 v36, v36\n"
      "v_exp_f32 v37, v37\n"
      "v_mfma_f32_16x16x32_bf16 v[40:43], a[216:219], a[88:91], v[40:43]\n"
      "s_lshl_b32 s92, s79, 13\n"
      "v_exp_f32 v38, v38\n"
      "v_exp_f32 v39, v39\n"
      "v_cvt_pk_bf16_f32 v56, v36, v37\n"
      "v_mfma_f32_16x16x32_bf16 v[40:43], a[220:223], a[92:95], v[40:43]\n"
      "s_waitcnt vmcnt(19)\n"
      "v_add_f32 v34, v36, v37\n"
      "v_cvt_pk_bf16_f32 v57, v38, v39\n"
      "v_add_f32 v34, v34, v38\n"
      "v_add_f32 v34, v34, v39\n"
      "v_mfma_f32_16x16x32_bf16 v[36:39], a[128:131], a[96:99], 0\n"
      "s_waitcnt vmcnt(18)\n"
      "s_nop 0\n"
      "v_fma_f32 v40, v40, s59, v30\n"
      "v_fma_f32 v41, v41, s59, v30\n"
      "v_fma_f32 v42, v42, s59, v30\n"
      "v_mfma_f32_16x16x32_bf16 v[36:39], a[132:135], a[100:103], v[36:39]\n"
      "s_waitcnt vmcnt(17)\n"
      "v_fma_f32 v43, v43, s59, v30\n"
      "v_exp_f32 v40, v40\n"
      "v_mfma_f32_16x16x32_bf16 v[36:39], a[136:139], a[104:107], v[36:39]\n"
      "s_waitcnt vmcnt(16)\n"
      "v_exp_f32 v41, v41\n"
      "v_exp_f32 v42, v42\n"
      "v_mfma_f32_16x16x32_bf16 v[36:39], a[140:143], a[108:111], v[36:39]\n"
      "s_waitcnt vmcnt(15)\n"
      "v_exp_f32 v43, v43\n"
      "v_cvt_pk_bf16_f32 v58, v40, v41\n"
      "v_add_f32 v34, v34, v40\n"
      "v_mfma_f32_16x16x32_bf16 v[36:39], a[144:147], a[112:115], v[36:39]\n"
      "s_waitcnt vmcnt(14)\n"
      "v_add_f32 v34, v34, v41\n"
      "v_cvt_pk_bf16_f32 v59, v42, v43\n"
      "v_add_f32 v34, v34, v42\n"
      "v_add_f32 v34, v34, v43\n"
      "v_mfma_f32_16x16x32_bf16 v[36:39], a[148:151], a[116:119], v[36:39]\n"
      "s_waitcnt vmcnt(13)\n"
      "v_mfma_f32_16x16x32_bf16 v[36:39], a[152:155], a[120:123], v[36:39]\n"
      "s_waitcnt vmcnt(12)\n"
      "v_mfma_f32_16x16x32_bf16 v[36:39], a[156:159], a[124:127], v[36:39]\n"
      "s_nop 0\n"
      "s_nop 0\n"
      "s_nop 0\n"
      "v_mfma_f32_16x16x32_bf16 v[40:43], a[192:195], a[96:99], 0\n"
      "s_nop 0\n"
      "s_nop 0\n"
      "s_nop 0\n"
      "v_mfma_f32_16x16x32_bf16 v[40:43], a[196:199], a[100:103], v[40:43]\n"
      "v_max3_f32 v31, v36, v37, v38\n"
      "v_max_f32 v31, v31, v39\n"
      "v_mov_b32 v0, v31\n"
      "v_mfma_f32_16x16x32_bf16 v[40:43], a[200:203], a[104:107], v[40:43]\n"
      "s_nop 0\n"
      "v_permlane32_swap_b32 v31, v0\n"
      "v_max_f32 v31, v31, v0\n"
      "v_mov_b32 v0, v31\n"
      "v_mfma_f32_16x16x32_bf16 v[40:43], a[204:207], a[108:111], v[40:43]\n"
      "s_nop 0\n"
      "v_permlane16_swap_b32 v31, v0\n"
      "v_max_f32 v31, v31, v0\n"
      "v_mul_f32 v31, s69, v31\n"
      "v_fma_f32 v36, v36, s59, v31\n"
      "v_fma_f32 v37, v37, s59, v31\n"
      "v_mfma_f32_16x16x32_bf16 v[40:43], a[208:211], a[112:115], v[40:43]\n"
      "v_fma_f32 v38, v38, s59, v31\n"
      "v_fma_f32 v39, v39, s59, v31\n"
      "v_exp_f32 v36, v36\n"
      "v_exp_f32 v37, v37\n"
      "v_mfma_f32_16x16x32_bf16 v[40:43], a[212:215], a[116:119], v[40:43]\n"
      "v_exp_f32 v38, v38\n"
      "v_exp_f32 v39, v39\n"
      "v_cvt_pk_bf16_f32 v60, v36, v37\n"
      "v_add_f32 v35, v36, v37\n"
      "v_cvt_pk_bf16_f32 v61, v38, v39\n"
      "v_mfma_f32_16x16x32_bf16 v[40:43], a[216:219], a[120:123], v[40:43]\n"
      "v_add_f32 v35, v35, v38\n"
      "v_add_f32 v35, v35, v39\n"
      "v_mfma_f32_16x16x32_bf16 v[40:43], a[220:223], a[124:127], v[40:43]\n"
      "s_waitcnt vmcnt(8) lgkmcnt(0)\n"
      "s_barrier\n"
      "ds_read_b128 a[128:131], v1 offset:16384\n"
      "ds_read_b128 a[132:135], v1 offset:16896\n"
      "ds_read_b128 a[136:139], v1 offset:20480\n"
      "ds_read_b128 a[140:143], v1 offset:20992\n"
      "ds_read_b128 a[144:147], v1 offset:24576\n"
      "ds_read_b128 a[148:151], v1 offset:25088\n"
      "ds_read_b128 a[152:155], v1 offset:28672\n"
      "ds_read_b128 a[156:159], v1 offset:29184\n"
      "s_waitcnt lgkmcnt(7)\n"
      "v_mfma_f32_16x16x32_bf16 v[36:39], a[128:131], a[0:3], 0\n"
      "s_waitcnt lgkmcnt(6)\n"
      "ds_read_b64_tr_b16 a[160:161], v2 offset:32768\n"
      "v_fma_f32 v40, v40, s59, v31\n"
      "v_fma_f32 v41, v41, s59, v31\n"
      "v_mfma_f32_16x16x32_bf16 v[36:39], a[132:135], a[4:7], v[36:39]\n"
      "s_waitcnt lgkmcnt(6)\n"
      "ds_read_b64_tr_b16 a[162:163], v2 offset:34816\n"
      "v_fma_f32 v42, v42, s59, v31\n"
      "v_fma_f32 v43, v43, s59, v31\n"
      "v_mfma_f32_16x16x32_bf16 v[36:39], a[136:139], a[8:11], v[36:39]\n"
      "s_waitcnt lgkmcnt(6)\n"
      "v_exp_f32 v40, v40\n"
      "v_mfma_f32_16x16x32_bf16 v[36:39], a[140:143], a[12:15], v[36:39]\n"
      "s_waitcnt lgkmcnt(5)\n"
      "v_exp_f32 v41, v41\n"
      "v_add_f32 v35, v35, v40\n"
      "v_mfma_f32_16x16x32_bf16 v[36:39], a[144:147], a[16:19], v[36:39]\n"
      "s_waitcnt lgkmcnt(4)\n"
      "v_exp_f32 v42, v42\n"
      "v_cvt_pk_bf16_f32 v62, v40, v41\n"
      "v_mfma_f32_16x16x32_bf16 v[36:39], a[148:151], a[20:23], v[36:39]\n"
      "s_waitcnt lgkmcnt(3)\n"
      "v_exp_f32 v43, v43\n"
      "v_mfma_f32_16x16x32_bf16 v[36:39], a[152:155], a[24:27], v[36:39]\n"
      "s_waitcnt lgkmcnt(2)\n"
      "ds_read_b64_tr_b16 a[164:165], v3 offset:32768\n"
      "v_add_f32 v35, v35, v41\n"
      "v_mfma_f32_16x16x32_bf16 v[36:39], a[156:159], a[28:31], v[36:39]\n"
      "s_mov_b32 m0, s54\n"
      "ds_read_b64_tr_b16 a[166:167], v3 offset:34816\n"
      "v_cvt_pk_bf16_f32 v63, v42, v43\n"
      "v_add_f32 v35, v35, v42\n"
      "v_add_f32 v35, v35, v43\n"
      "v_mfma_f32_16x16x32_bf16 v[40:43], a[156:159], a[60:63], 0\n"
      "s_nop 0\n"
      "ds_read_b64_tr_b16 a[168:169], v2 offset:33280\n"
      "v_fma_f32 v36, v36, s59, v28\n"
      "v_mfma_f32_16x16x32_bf16 v[40:43], a[152:155], a[56:59], v[40:43]\n"
      "ds_read_b64_tr_b16 a[170:171], v2 offset:35328\n"
      "v_fma_f32 v37, v37, s59, v28\n"
      "v_mfma_f32_16x16x32_bf16 v[40:43], a[148:151], a[52:55], v[40:43]\n"
      "ds_read_b64_tr_b16 a[172:173], v3 offset:33280\n"
      "v_fma_f32 v38, v38, s59, v28\n"
      "v_mfma_f32_16x16x32_bf16 v[40:43], a[144:147], a[48:51], v[40:43]\n"
      "ds_read_b64_tr_b16 a[174:175], v3 offset:35328\n"
      "v_fma_f32 v39, v39, s59, v28\n"
      "v_mfma_f32_16x16x32_bf16 v[40:43], a[140:143], a[44:47], v[40:43]\n"
      "v_exp_f32 v36, v36\n"
      "v_mfma_f32_16x16x32_bf16 v[40:43], a[136:139], a[40:43], v[40:43]\n"
      "v_exp_f32 v37, v37\n"
      "v_mfma_f32_16x16x32_bf16 v[40:43], a[132:135], a[36:39], v[40:43]\n"
      "v_exp_f32 v38, v38\n"
      "v_mfma_f32_16x16x32_bf16 v[40:43], a[128:131], a[32:35], v[40:43]\n"
      "s_waitcnt lgkmcnt(6)\n"
      "v_exp_f32 v39, v39\n"
      "v_mfma_f32_16x16x32_bf16 v[128:131], a[160:163], v[48:51], 0\n"
      "s_nop 0\n"
      "buffer_load_dwordx4 v4, s[36:39], s52 offen offset:2048 sc1 lds\n"
      "s_nop 0\n"
      "v_mfma_f32_16x16x32_bf16 v[192:195], a[160:163], v[52:55], 0\n"
      "s_nop 0\n"
      "v_cvt_pk_bf16_f32 v64, v36, v37\n"
      "v_add_f32 v32, v32, v36\n"
      "v_mfma_f32_16x16x32_bf16 v[80:83], a[160:163], v[56:59], 0\n"
      "v_add_f32 v32, v32, v37\n"
      "v_cvt_pk_bf16_f32 v65, v38, v39\n"
      "v_mfma_f32_16x16x32_bf16 a[192:195], a[160:163], v[60:63], 0\n"
      "s_waitcnt lgkmcnt(4)\n"
      "ds_read_b64_tr_b16 a[160:161], v2 offset:36864\n"
      "v_add_f32 v32, v32, v38\n"
      "v_mfma_f32_16x16x32_bf16 a[196:199], a[164:167], v[60:63], 0\n"
      "buffer_load_dwordx4 v4, s[36:39], s52 offen offset:3072 sc1 lds\n"
      "ds_read_b64_tr_b16 a[162:163], v2 offset:38912\n"
      "v_mfma_f32_16x16x32_bf16 v[84:87], a[164:167], v[56:59], 0\n"
      "s_add_u32 m0, s57, 0x2000\n"
      "v_add_f32 v32, v32, v39\n"
      "v_fma_f32 v40, v40, s59, v29\n"
      "v_mfma_f32_16x16x32_bf16 v[196:199], a[164:167], v[52:55], 0\n"
      "v_fma_f32 v41, v41, s59, v29\n"
      "v_fma_f32 v42, v42, s59, v29\n"
      "v_mfma_f32_16x16x32_bf16 v[132:135], a[164:167], v[48:51], 0\n"
      "s_waitcnt lgkmcnt(4)\n"
      "ds_read_b64_tr_b16 a[164:165], v3 offset:36864\n"
      "v_fma_f32 v43, v43, s59, v29\n"
      "v_mfma_f32_16x16x32_bf16 v[136:139], a[168:171], v[48:51], 0\n"
      "buffer_load_dwordx4 v4, s[40:43], s90 offen offset:0 sc1 lds\n"
      "ds_read_b64_tr_b16 a[166:167], v3 offset:38912\n"
      "v_mfma_f32_16x16x32_bf16 v[200:203], a[168:171], v[52:55], 0\n"
      "v_exp_f32 v40, v40\n"
      "v_mfma_f32_16x16x32_bf16 v[88:91], a[168:171], v[56:59], 0\n"
      "v_exp_f32 v41, v41\n"
      "v_mfma_f32_16x16x32_bf16 a[200:203], a[168:171], v[60:63], 0\n"
      "s_waitcnt lgkmcnt(4)\n"
      "v_exp_f32 v42, v42\n"
      "v_mfma_f32_16x16x32_bf16 a[204:207], a[172:175], v[60:63], 0\n"
      "buffer_load_dwordx4 v4, s[40:43], s90 offen offset:1024 sc1 lds\n"
      "ds_read_b64_tr_b16 a[168:169], v2 offset:37376\n"
      "v_mfma_f32_16x16x32_bf16 v[92:95], a[172:175], v[56:59], 0\n"
      "s_mov_b32 m0, s96\n"
      "v_exp_f32 v43, v43\n"
      "v_mfma_f32_16x16x32_bf16 v[204:207], a[172:175], v[52:55], 0\n"
      "ds_read_b64_tr_b16 a[170:171], v2 offset:39424\n"
      "v_cvt_pk_bf16_f32 v68, v40, v41\n"
      "v_mfma_f32_16x16x32_bf16 v[140:143], a[172:175], v[48:51], 0\n"
      "ds_read_b64_tr_b16 a[172:173], v3 offset:37376\n"
      "v_add_f32 v33, v33, v40\n"
      "v_mfma_f32_16x16x32_bf16 v[36:39], a[128:131], a[64:67], 0\n"
      "ds_read_b64_tr_b16 a[174:175], v3 offset:39424\n"
      "v_add_f32 v33, v33, v41\n"
      "v_mfma_f32_16x16x32_bf16 v[36:39], a[132:135], a[68:71], v[36:39]\n"
      "v_cvt_pk_bf16_f32 v69, v42, v43\n"
      "v_add_f32 v33, v33, v42\n"
      "v_mfma_f32_16x16x32_bf16 v[36:39], a[136:139], a[72:75], v[36:39]\n"
      "v_add_f32 v33, v33, v43\n"
      "v_mfma_f32_16x16x32_bf16 v[36:39], a[140:143], a[76:79], v[36:39]\n"
      "buffer_load_dwordx4 v5, s[44:47], s97 offen offset:0 nt lds\n"
      "v_mfma_f32_16x16x32_bf16 v[36:39], a[144:147], a[80:83], v[36:39]\n"
      "v_mfma_f32_16x16x32_bf16 v[36:39], a[148:151], a[84:87], v[36:39]\n"
      "v_mfma_f32_16x16x32_bf16 v[36:39], a[152:155], a[88:91], v[36:39]\n"
      "v_mfma_f32_16x16x32_bf16 v[36:39], a[156:159], a[92:95], v[36:39]\n"
      "s_nop 0\n"
      "s_nop 0\n"
      "s_nop 0\n"
      "v_mfma_f32_16x16x32_bf16 v[40:43], a[156:159], a[124:127], 0\n"
      "s_nop 0\n"
      "s_nop 0\n"
      "s_nop 0\n"
      "v_mfma_f32_16x16x32_bf16 v[40:43], a[152:155], a[120:123], v[40:43]\n"
      "v_fma_f32 v36, v36, s59, v30\n"
      "v_fma_f32 v37, v37, s59, v30\n"
      "v_mfma_f32_16x16x32_bf16 v[40:43], a[148:151], a[116:119], v[40:43]\n"
      "v_fma_f32 v38, v38, s59, v30\n"
      "v_fma_f32 v39, v39, s59, v30\n"
      "v_mfma_f32_16x16x32_bf16 v[40:43], a[144:147], a[112:115], v[40:43]\n"
      "v_exp_f32 v36, v36\n"
      "v_mfma_f32_16x16x32_bf16 v[40:43], a[140:143], a[108:111], v[40:43]\n"
      "v_exp_f32 v37, v37\n"
      "v_mfma_f32_16x16x32_bf16 v[40:43], a[136:139], a[104:107], v[40:43]\n"
      "v_exp_f32 v38, v38\n"
      "v_mfma_f32_16x16x32_bf16 v[40:43], a[132:135], a[100:103], v[40:43]\n"
      "v_exp_f32 v39, v39\n"
      "v_mfma_f32_16x16x32_bf16 v[40:43], a[128:131], a[96:99], v[40:43]\n"
      "s_waitcnt vmcnt(9) lgkmcnt(0)\n"
      "s_barrier\n"
      "v_mfma_f32_16x16x32_bf16 v[144:147], a[160:163], v[48:51], 0\n"
      "s_add_u32 s52, s52, 0x4000\n"
      "ds_read_b128 a[128:131], v1 offset:18432\n"
      "v_cvt_pk_bf16_f32 v72, v36, v37\n"
      "v_mfma_f32_16x16x32_bf16 v[208:211], a[160:163], v[52:55], 0\n"
      "s_mov_b32 m0, s55\n"
      "ds_read_b128 a[132:135], v1 offset:18944\n"
      "v_add_f32 v34, v34, v36\n"
      "v_mfma_f32_16x16x32_bf16 v[96:99], a[160:163], v[56:59], 0\n"
      "s_add_u32 s53, s53, 0x4000\n"
      "ds_read_b128 a[136:139], v1 offset:22528\n"
      "v_add_f32 v34, v34, v37\n"
      "v_mfma_f32_16x16x32_bf16 a[208:211], a[160:163], v[60:63], 0\n"
      "s_add_u32 s90, s90, 0x4000\n"
      "ds_read_b128 a[140:143], v1 offset:23040\n"
      "v_cvt_pk_bf16_f32 v73, v38, v39\n"
      "v_mfma_f32_16x16x32_bf16 a[212:215], a[164:167], v[60:63], 0\n"
      "ds_read_b128 a[144:147], v1 offset:26624\n"
      "v_add_f32 v34, v34, v38\n"
      "v_mfma_f32_16x16x32_bf16 v[100:103], a[164:167], v[56:59], 0\n"
      "ds_read_b128 a[148:151], v1 offset:27136\n"
      "v_add_f32 v34, v34, v39\n"
      "v_mfma_f32_16x16x32_bf16 v[212:215], a[164:167], v[52:55], 0\n"
      "ds_read_b128 a[152:155], v1 offset:30720\n"
      "v_fma_f32 v40, v40, s59, v31\n"
      "v_mfma_f32_16x16x32_bf16 v[148:151], a[164:167], v[48:51], 0\n"
      "ds_read_b128 a[156:159], v1 offset:31232\n"
      "v_fma_f32 v41, v41, s59, v31\n"
      "v_mfma_f32_16x16x32_bf16 v[152:155], a[168:171], v[48:51], 0\n"
      "ds_read_b64_tr_b16 a[160:161], v2 offset:40960\n"
      "v_fma_f32 v42, v42, s59, v31\n"
      "v_mfma_f32_16x16x32_bf16 v[216:219], a[168:171], v[52:55], 0\n"
      "ds_read_b64_tr_b16 a[162:163], v2 offset:43008\n"
      "v_fma_f32 v43, v43, s59, v31\n"
      "v_mfma_f32_16x16x32_bf16 v[104:107], a[168:171], v[56:59], 0\n"
      "v_exp_f32 v40, v40\n"
      "v_mfma_f32_16x16x32_bf16 a[216:219], a[168:171], v[60:63], 0\n"
      "v_exp_f32 v41, v41\n"
      "v_mfma_f32_16x16x32_bf16 a[220:223], a[172:175], v[60:63], 0\n"
      "v_exp_f32 v42, v42\n"
      "v_mfma_f32_16x16x32_bf16 v[108:111], a[172:175], v[56:59], 0\n"
      "v_exp_f32 v43, v43\n"
      "v_mfma_f32_16x16x32_bf16 v[220:223], a[172:175], v[52:55], 0\n"
      "ds_read_b64_tr_b16 a[164:165], v3 offset:40960\n"
      "v_cvt_pk_bf16_f32 v76, v40, v41\n"
      "v_mfma_f32_16x16x32_bf16 v[156:159], a[172:175], v[48:51], 0\n"
      "s_waitcnt lgkmcnt(10)\n"
      "ds_read_b64_tr_b16 a[166:167], v3 offset:43008\n"
      "v_add_f32 v35, v35, v40\n"
      "v_mfma_f32_16x16x32_bf16 v[36:39], a[128:131], a[0:3], 0\n"
      "s_waitcnt lgkmcnt(10)\n"
      "buffer_load_dwordx4 v4, s[36:39], s52 offen offset:0 sc1 lds\n"
      "ds_read_b64_tr_b16 a[168:169], v2 offset:41472\n"
      "v_mfma_f32_16x16x32_bf16 v[36:39], a[132:135], a[4:7], v[36:39]\n"
      "s_waitcnt lgkmcnt(10)\n"
      "ds_read_b64_tr_b16 a[170:171], v2 offset:43520\n"
      "v_add_f32 v35, v35, v41\n"
      "v_mfma_f32_16x16x32_bf16 v[36:39], a[136:139], a[8:11], v[36:39]\n"
      "s_waitcnt lgkmcnt(10)\n"
      "ds_read_b64_tr_b16 a[172:173], v3 offset:41472\n"
      "v_cvt_pk_bf16_f32 v77, v42, v43\n"
      "v_mfma_f32_16x16x32_bf16 v[36:39], a[140:143], a[12:15], v[36:39]\n"
      "s_waitcnt lgkmcnt(10)\n"
      "ds_read_b64_tr_b16 a[174:175], v3 offset:43520\n"
      "v_add_f32 v35, v35, v42\n"
      "v_mfma_f32_16x16x32_bf16 v[36:39], a[144:147], a[16:19], v[36:39]\n"
      "s_waitcnt lgkmcnt(10)\n"
      "buffer_load_dwordx4 v4, s[36:39], s52 offen offset:1024 sc1 lds\n"
      "v_mfma_f32_16x16x32_bf16 v[36:39], a[148:151], a[20:23], v[36:39]\n"
      "s_waitcnt lgkmcnt(9)\n"
      "v_add_f32 v35, v35, v43\n"
      "v_mfma_f32_16x16x32_bf16 v[36:39], a[152:155], a[24:27], v[36:39]\n"
      "s_waitcnt lgkmcnt(8)\n"
      "v_mfma_f32_16x16x32_bf16 v[36:39], a[156:159], a[28:31], v[36:39]\n"
      "s_mov_b32 m0, s56\n"
      "s_nop 0\n"
      "s_nop 0\n"
      "v_mfma_f32_16x16x32_bf16 v[40:43], a[156:159], a[60:63], 0\n"
      "s_nop 0\n"
      "buffer_load_dwordx4 v4, s[40:43], s53 offen offset:0 sc1 lds\n"
      "s_nop 0\n"
      "v_mfma_f32_16x16x32_bf16 v[40:43], a[152:155], a[56:59], v[40:43]\n"
      "v_fma_f32 v36, v36, s59, v28\n"
      "v_fma_f32 v37, v37, s59, v28\n"
      "v_mfma_f32_16x16x32_bf16 v[40:43], a[148:151], a[52:55], v[40:43]\n"
      "v_fma_f32 v38, v38, s59, v28\n"
      "v_fma_f32 v39, v39, s59, v28\n"
      "v_mfma_f32_16x16x32_bf16 v[40:43], a[144:147], a[48:51], v[40:43]\n"
      "v_exp_f32 v36, v36\n"
      "v_mfma_f32_16x16x32_bf16 v[40:43], a[140:143], a[44:47], v[40:43]\n"
      "buffer_load_dwordx4 v4, s[40:43], s53 offen offset:1024 sc1 lds\n"
      "v_mfma_f32_16x16x32_bf16 v[40:43], a[136:139], a[40:43], v[40:43]\n"
      "s_mov_b32 m0, s96\n"
      "v_exp_f32 v37, v37\n"
      "v_mfma_f32_16x16x32_bf16 v[40:43], a[132:135], a[36:39], v[40:43]\n"
      "v_exp_f32 v38, v38\n"
      "v_mfma_f32_16x16x32_bf16 v[40:43], a[128:131], a[32:35], v[40:43]\n"
      "s_waitcnt lgkmcnt(6)\n"
      "v_exp_f32 v39, v39\n"
      "v_mfma_f32_16x16x32_bf16 v[160:163], a[160:163], v[48:51], 0\n"
      "s_nop 0\n"
      "v_cvt_pk_bf16_f32 v66, v36, v37\n"
      "v_add_f32 v32, v32, v36\n"
      "v_mfma_f32_16x16x32_bf16 v[224:227], a[160:163], v[52:55], 0\n"
      "s_nop 0\n"
      "v_add_f32 v32, v32, v37\n"
      "v_cvt_pk_bf16_f32 v67, v38, v39\n"
      "v_mfma_f32_16x16x32_bf16 v[112:115], a[160:163], v[56:59], 0\n"
      "v_add_f32 v32, v32, v38\n"
      "v_add_f32 v32, v32, v39\n"
      "v_mfma_f32_16x16x32_bf16 a[224:227], a[160:163], v[60:63], 0\n"
      "s_waitcnt lgkmcnt(4)\n"
      "ds_read_b64_tr_b16 a[160:161], v2 offset:45056\n"
      "v_fma_f32 v40, v40, s59, v29\n"
      "v_mfma_f32_16x16x32_bf16 a[228:231], a[164:167], v[60:63], 0\n"
      "ds_read_b64_tr_b16 a[162:163], v2 offset:47104\n"
      "v_fma_f32 v41, v41, s59, v29\n"
      "v_mfma_f32_16x16x32_bf16 v[116:119], a[164:167], v[56:59], 0\n"
      "v_fma_f32 v42, v42, s59, v29\n"
      "v_fma_f32 v43, v43, s59, v29\n"
      "v_mfma_f32_16x16x32_bf16 v[228:231], a[164:167], v[52:55], 0\n"
      "v_exp_f32 v40, v40\n"
      "v_mfma_f32_16x16x32_bf16 v[164:167], a[164:167], v[48:51], 0\n"
      "s_waitcnt lgkmcnt(4)\n"
      "v_exp_f32 v41, v41\n"
      "v_mfma_f32_16x16x32_bf16 v[168:171], a[168:171], v[48:51], 0\n"
      "v_exp_f32 v42, v42\n"
      "v_mfma_f32_16x16x32_bf16 v[232:235], a[168:171], v[52:55], 0\n"
      "v_exp_f32 v43, v43\n"
      "v_mfma_f32_16x16x32_bf16 v[120:123], a[168:171], v[56:59], 0\n"
      "ds_read_b64_tr_b16 a[164:165], v3 offset:45056\n"
      "v_cvt_pk_bf16_f32 v70, v40, v41\n"
      "v_mfma_f32_16x16x32_bf16 a[232:235], a[168:171], v[60:63], 0\n"
      "s_waitcnt lgkmcnt(3)\n"
      "ds_read_b64_tr_b16 a[166:167], v3 offset:47104\n"
      "v_add_f32 v33, v33, v40\n"
      "v_mfma_f32_16x16x32_bf16 a[236:239], a[172:175], v[60:63], 0\n"
      "ds_read_b64_tr_b16 a[168:169], v2 offset:45568\n"
      "v_add_f32 v33, v33, v41\n"
      "v_mfma_f32_16x16x32_bf16 v[124:127], a[172:175], v[56:59], 0\n"
      "ds_read_b64_tr_b16 a[170:171], v2 offset:47616\n"
      "v_cvt_pk_bf16_f32 v71, v42, v43\n"
      "v_mfma_f32_16x16x32_bf16 v[236:239], a[172:175], v[52:55], 0\n"
      "v_add_f32 v33, v33, v42\n"
      "v_add_f32 v33, v33, v43\n"
      "v_mfma_f32_16x16x32_bf16 v[172:175], a[172:175], v[48:51], 0\n"
      "ds_read_b64_tr_b16 a[172:173], v3 offset:45568\n"
      "buffer_load_dwordx4 v5, s[44:47], s97 offen offset:0 nt lds\n"
      "v_mfma_f32_16x16x32_bf16 v[36:39], a[128:131], a[64:67], 0\n"
      "ds_read_b64_tr_b16 a[174:175], v3 offset:47616\n"
      "v_mfma_f32_16x16x32_bf16 v[36:39], a[132:135], a[68:71], v[36:39]\n"
      "v_mfma_f32_16x16x32_bf16 v[36:39], a[136:139], a[72:75], v[36:39]\n"
      "v_mfma_f32_16x16x32_bf16 v[36:39], a[140:143], a[76:79], v[36:39]\n"
      "v_mfma_f32_16x16x32_bf16 v[36:39], a[144:147], a[80:83], v[36:39]\n"
      "v_mfma_f32_16x16x32_bf16 v[36:39], a[148:151], a[84:87], v[36:39]\n"
      "v_mfma_f32_16x16x32_bf16 v[36:39], a[152:155], a[88:91], v[36:39]\n"
      "v_mfma_f32_16x16x32_bf16 v[36:39], a[156:159], a[92:95], v[36:39]\n"
      "s_nop 0\n"
      "s_nop 0\n"
      "s_nop 0\n"
      "v_mfma_f32_16x16x32_bf16 v[40:43], a[156:159], a[124:127], 0\n"
      "s_nop 0\n"
      "s_nop 0\n"
      "s_nop 0\n"
      "v_mfma_f32_16x16x32_bf16 v[40:43], a[152:155], a[120:123], v[40:43]\n"
      "v_fma_f32 v36, v36, s59, v30\n"
      "v_fma_f32 v37, v37, s59, v30\n"
      "v_mfma_f32_16x16x32_bf16 v[40:43], a[148:151], a[116:119], v[40:43]\n"
      "v_fma_f32 v38, v38, s59, v30\n"
      "v_fma_f32 v39, v39, s59, v30\n"
      "v_mfma_f32_16x16x32_bf16 v[40:43], a[144:147], a[112:115], v[40:43]\n"
      "v_exp_f32 v36, v36\n"
      "v_mfma_f32_16x16x32_bf16 v[40:43], a[140:143], a[108:111], v[40:43]\n"
      "v_exp_f32 v37, v37\n"
      "v_mfma_f32_16x16x32_bf16 v[40:43], a[136:139], a[104:107], v[40:43]\n"
      "v_mfma_f32_16x16x32_bf16 v[40:43], a[132:135], a[100:103], v[40:43]\n"
      "v_mfma_f32_16x16x32_bf16 v[40:43], a[128:131], a[96:99], v[40:43]\n"
      "s_waitcnt vmcnt(10) lgkmcnt(0)\n"
      "s_barrier\n"
      "LP4_%=:\n"
      "LENT4a_%=:\n"
      "v_mfma_f32_16x16x32_bf16 v[176:179], a[160:163], v[48:51], 0\n"
      "s_lshl_b32 s78, s61, 12\n"
      "ds_read_b128 v[16:19], v6 offset:0\n"
      "v_cvt_pk_bf16_f32 v74, v36, v37\n"
      "v_mfma_f32_16x16x32_bf16 v[240:243], a[160:163], v[52:55], 0\n"
      "s_add_u32 s76, s91, s78\n"
      "ds_read_b128 a[128:131], v1 offset:0\n"
      "v_add_f32 v34, v34, v36\n"
      "v_mfma_f32_16x16x32_bf16 a[176:179], a[160:163], v[56:59], 0\n"
      "s_add_u32 s74, s58, s78\n"
      "ds_read_b128 a[132:135], v1 offset:512\n"
      "v_add_f32 v34, v34, v37\n"
      "v_mfma_f32_16x16x32_bf16 a[240:243], a[160:163], v[60:63], 0\n"
      "s_lshr_b32 s78, s61, 1\n"
      "ds_read_b128 a[136:139], v1 offset:4096\n"
      "v_fma_f32 v40, v40, s59, v31\n"
      "v_mfma_f32_16x16x32_bf16 a[244:247], a[164:167], v[60:63], 0\n"
      "s_lshl_b32 s78, s78, 13\n"
      "ds_read_b128 a[140:143], v1 offset:4608\n"
      "v_fma_f32 v41, v41, s59, v31\n"
      "v_mfma_f32_16x16x32_bf16 a[180:183], a[164:167], v[56:59], 0\n"
      "s_and_b32 s79, s61, 1\n"
      "ds_read_b128 a[144:147], v1 offset:8192\n"
      "v_fma_f32 v42, v42, s59, v31\n"
      "v_mfma_f32_16x16x32_bf16 v[244:247], a[164:167], v[52:55], 0\n"
      "s_lshl_b32 s79, s79, 8\n"
      "ds_read_b128 a[148:151], v1 offset:8704\n"
      "v_fma_f32 v43, v43, s59, v31\n"
      "v_mfma_f32_16x16x32_bf16 v[180:183], a[164:167], v[48:51], 0\n"
      "s_add_u32 s75, s78, s79\n"
      "ds_read_b128 a[152:155], v1 offset:12288\n"
      "v_mfma_f32_16x16x32_bf16 v[184:187], a[168:171], v[48:51], 0\n"
      "s_add_u32 s75, s75, s92\n"
      "v_exp_f32 v38, v38\n"
      "v_mfma_f32_16x16x32_bf16 v[248:251], a[168:171], v[52:55], 0\n"
      "s_cmp_lt_u32 s61, 5\n"
      "v_exp_f32 v39, v39\n"
      "v_mfma_f32_16x16x32_bf16 a[184:187], a[168:171], v[56:59], 0\n"
      "s_cselect_b32 s77, 0x1000, 0\n"
      "ds_read_b128 a[156:159], v1 offset:12800\n"
      "v_cvt_pk_bf16_f32 v75, v38, v39\n"
      "v_mfma_f32_16x16x32_bf16 a[248:251], a[168:171], v[60:63], 0\n"
      "s_cmp_eq_u32 s61, 6\n"
      "ds_read_b128 v[20:23], v6 offset:1024\n"
      "v_add_f32 v34, v34, v38\n"
      "v_mfma_f32_16x16x32_bf16 a[252:255], a[172:175], v[60:63], 0\n"
      "s_cselect_b32 s76, 0x10000000, s76\n"
      "s_waitcnt lgkmcnt(9)\n"
      "buffer_store_dwordx4 v[16:19], v7, s[48:51], s76 offen offset:0 nt\n"
      "v_add_f32 v34, v34, v39\n"
      "v_exp_f32 v40, v40\n"
      "v_mfma_f32_16x16x32_bf16 a[188:191], a[172:175], v[56:59], 0\n"
      "s_mov_b32 m0, s55\n"
      "v_exp_f32 v41, v41\n"
      "v_mfma_f32_16x16x32_bf16 v[252:255], a[172:175], v[52:55], 0\n"
      "s_cselect_b32 s77, 0xffffb000, s77\n"
      "v_exp_f32 v42, v42\n"
      "v_mfma_f32_16x16x32_bf16 v[188:191], a[172:175], v[48:51], 0\n"
      "LLOOP4a_%=:\n"
      "s_waitcnt lgkmcnt(8)\n"
      "v_exp_f32 v43, v43\n"
      "v_mfma_f32_16x16x32_bf16 v[36:39], a[128:131], a[0:3], 0\n"
      "s_waitcnt lgkmcnt(7)\n"
      "buffer_load_dwordx4 v4, s[36:39], s52 offen offset:2048 sc1 lds\n"
      "ds_read_b64_tr_b16 a[160:161], v2 offset:49152\n"
      "v_mfma_f32_16x16x32_bf16 v[36:39], a[132:135], a[4:7], v[36:39]\n"
      "s_waitcnt lgkmcnt(7)\n"
      "ds_read_b64_tr_b16 a[162:163], v2 offset:51200\n"
      "v_cvt_pk_bf16_f32 v78, v40, v41\n"
      "v_mfma_f32_16x16x32_bf16 v[36:39], a[136:139], a[8:11], v[36:39]\n"
      "s_waitcnt lgkmcnt(7)\n"
      "ds_read_b64_tr_b16 a[164:165], v3 offset:49152\n"
      "v_add_f32 v35, v35, v40\n"
      "v_mfma_f32_16x16x32_bf16 v[36:39], a[140:143], a[12:15], v[36:39]\n"
      "s_waitcnt lgkmcnt(7)\n"
      "ds_read_b64_tr_b16 a[166:167], v3 offset:51200\n"
      "v_add_f32 v35, v35, v41\n"
      "v_mfma_f32_16x16x32_bf16 v[36:39], a[144:147], a[16:19], v[36:39]\n"
      "s_waitcnt lgkmcnt(7)\n"
      "s_waitcnt lgkmcnt(4)\n"
      "buffer_store_dwordx4 v[20:23], v7, s[48:51], s76 offen offset:128 nt\n"
      "v_cvt_pk_bf16_f32 v79, v42, v43\n"
      "v_add_f32 v35, v35, v42\n"
      "v_add_f32 v35, v35, v43\n"
      "v_mfma_f32_16x16x32_bf16 v[36:39], a[148:151], a[20:23], v[36:39]\n"
      "s_cselect_b32 s74, 0x30000, s74\n"
      "buffer_load_dwordx4 v4, s[36:39], s52 offen offset:3072 sc1 lds\n"
      "ds_read_b128 v[16:19], v6 offset:2048\n"
      "v_mfma_f32_16x16x32_bf16 v[36:39], a[152:155], a[24:27], v[36:39]\n"
      "s_cselect_b32 s75, 0x10000000, s75\n"
      "ds_read_b64_tr_b16 a[168:169], v2 offset:49664\n"
      "v_mfma_f32_16x16x32_bf16 v[36:39], a[156:159], a[28:31], v[36:39]\n"
      "s_add_u32 m0, s56, 0x2000\n"
      "ds_read_b64_tr_b16 a[170:171], v2 offset:51712\n"
      "s_nop 0\n"
      "v_mfma_f32_16x16x32_bf16 v[40:43], a[156:159], a[60:63], 0\n"
      "s_nop 0\n"
      "buffer_load_dwordx4 v4, s[40:43], s90 offen offset:0 sc1 lds\n"
      "ds_read_b64_tr_b16 a[172:173], v3 offset:49664\n"
      "v_mfma_f32_16x16x32_bf16 v[40:43], a[152:155], a[56:59], v[40:43]\n"
      "ds_read_b64_tr_b16 a[174:175], v3 offset:51712\n"
      "v_fma_f32 v36, v36, s59, v28\n"
      "v_mfma_f32_16x16x32_bf16 v[40:43], a[148:151], a[52:55], v[40:43]\n"
      "v_fma_f32 v37, v37, s59, v28\n"
      "v_fma_f32 v38, v38, s59, v28\n"
      "v_mfma_f32_16x16x32_bf16 v[40:43], a[144:147], a[48:51], v[40:43]\n"
      "v_fma_f32 v39, v39, s59, v28\n"
      "v_mfma_f32_16x16x32_bf16 v[40:43], a[140:143], a[44:47], v[40:43]\n"
      "buffer_load_dwordx4 v4, s[40:43], s90 offen offset:1024 sc1 lds\n"
      "v_mfma_f32_16x16x32_bf16 v[40:43], a[136:139], a[40:43], v[40:43]\n"
      "s_mov_b32 m0, s96\n"
      "v_exp_f32 v36, v36\n"
      "v_mfma_f32_16x16x32_bf16 v[40:43], a[132:135], a[36:39], v[40:43]\n"
      "s_waitcnt lgkmcnt(4)\n"
      "buffer_store_dwordx4 v[16:19], v7, s[48:51], s76 offen offset:256 nt\n"
      "v_exp_f32 v37, v37\n"
      "v_exp_f32 v38, v38\n"
      "v_mfma_f32_16x16x32_bf16 v[40:43], a[128:131], a[32:35], v[40:43]\n"
      "s_nop 0\n"
      "v_exp_f32 v39, v39\n"
      "v_mfma_f32_16x16x32_bf16 v[128:131], a[160:163], v[64:67], v[128:131]\n"
      "s_nop 0\n"
      "buffer_load_dwordx4 v5, s[44:47], s97 offen offset:1024 nt lds\n"
      "ds_read_b128 v[20:23], v6 offset:3072\n"
      "v_mfma_f32_16x16x32_bf16 v[192:195], a[160:163], v[68:71], v[192:195]\n"
      "s_nop 0\n"
      "v_cvt_pk_bf16_f32 v48, v36, v37\n"
      "v_add_f32 v32, v32, v36\n"
      "v_mfma_f32_16x16x32_bf16 v[80:83], a[160:163], v[72:75], v[80:83]\n"
      "v_add_f32 v32, v32, v37\n"
      "v_cvt_pk_bf16_f32 v49, v38, v39\n"
      "v_mfma_f32_16x16x32_bf16 a[192:195], a[160:163], v[76:79], a[192:195]\n"
      "ds_read_b64_tr_b16 a[160:161], v2 offset:53248\n"
      "v_add_f32 v32, v32, v38\n"
      "v_mfma_f32_16x16x32_bf16 a[196:199], a[164:167], v[76:79], a[196:199]\n"
      "ds_read_b64_tr_b16 a[162:163], v2 offset:55296\n"
      "v_add_f32 v32, v32, v39\n"
      "v_mfma_f32_16x16x32_bf16 v[84:87], a[164:167], v[72:75], v[84:87]\n"
      "v_fma_f32 v40, v40, s59, v29\n"
      "v_fma_f32 v41, v41, s59, v29\n"
      "v_mfma_f32_16x16x32_bf16 v[196:199], a[164:167], v[68:71], v[196:199]\n"
      "v_fma_f32 v42, v42, s59, v29\n"
      "v_fma_f32 v43, v43, s59, v29\n"
      "v_mfma_f32_16x16x32_bf16 v[132:135], a[164:167], v[64:67], v[132:135]\n"
      "s_waitcnt lgkmcnt(5)\n"
      "v_exp_f32 v40, v40\n"
      "v_mfma_f32_16x16x32_bf16 v[136:139], a[168:171], v[64:67], v[136:139]\n"
      "v_exp_f32 v41, v41\n"
      "v_mfma_f32_16x16x32_bf16 v[200:203], a[168:171], v[68:71], v[200:203]\n"
      "v_exp_f32 v42, v42\n"
      "v_mfma_f32_16x16x32_bf16 v[88:91], a[168:171], v[72:75], v[88:91]\n"
      "s_waitcnt lgkmcnt(2)\n"
      "buffer_store_dwordx4 v[20:23], v7, s[48:51], s76 offen offset:384 nt\n"
      "v_exp_f32 v43, v43\n"
      "v_cvt_pk_bf16_f32 v52, v40, v41\n"
      "v_add_f32 v33, v33, v40\n"
      "v_mfma_f32_16x16x32_bf16 a[200:203], a[168:171], v[76:79], a[200:203]\n"
      "ds_read_b64_tr_b16 a[164:165], v3 offset:53248\n"
      "v_add_f32 v33, v33, v41\n"
      "v_mfma_f32_16x16x32_bf16 a[204:207], a[172:175], v[76:79], a[204:207]\n"
      "ds_read_b64_tr_b16 a[166:167], v3 offset:55296\n"
      "v_cvt_pk_bf16_f32 v53, v42, v43\n"
      "v_mfma_f32_16x16x32_bf16 v[92:95], a[172:175], v[72:75], v[92:95]\n"
      "ds_read_b64_tr_b16 a[168:169], v2 offset:53760\n"
      "v_add_f32 v33, v33, v42\n"
      "v_mfma_f32_16x16x32_bf16 v[204:207], a[172:175], v[68:71], v[204:207]\n"
      "ds_read_b64_tr_b16 a[170:171], v2 offset:55808\n"
      "v_add_f32 v33, v33, v43\n"
      "v_mfma_f32_16x16x32_bf16 v[140:143], a[172:175], v[64:67], v[140:143]\n"
      "ds_read_b64_tr_b16 a[172:173], v3 offset:53760\n"
      "v_add_u32 v6, s77, v6\n"
      "v_mfma_f32_16x16x32_bf16 v[36:39], a[128:131], a[64:67], 0\n"
      "ds_read_b64_tr_b16 a[174:175], v3 offset:55808\n"
      "v_mfma_f32_16x16x32_bf16 v[36:39], a[132:135], a[68:71], v[36:39]\n"
      "v_mfma_f32_16x16x32_bf16 v[36:39], a[136:139], a[72:75], v[36:39]\n"
      "v_mfma_f32_16x16x32_bf16 v[36:39], a[140:143], a[76:79], v[36:39]\n"
      "v_mfma_f32_16x16x32_bf16 v[36:39], a[144:147], a[80:83], v[36:39]\n"
      "v_mfma_f32_16x16x32_bf16 v[36:39], a[148:151], a[84:87], v[36:39]\n"
      "v_mfma_f32_16x16x32_bf16 v[36:39], a[152:155], a[88:91], v[36:39]\n"
      "v_mfma_f32_16x16x32_bf16 v[36:39], a[156:159], a[92:95], v[36:39]\n"
      "s_nop 0\n"
      "s_nop 0\n"
      "s_nop 0\n"
      "v_mfma_f32_16x16x32_bf16 v[40:43], a[156:159], a[124:127], 0\n"
      "s_nop 0\n"
      "s_nop 0\n"
      "s_nop 0\n"
      "v_mfma_f32_16x16x32_bf16 v[40:43], a[152:155], a[120:123], v[40:43]\n"
      "v_fma_f32 v36, v36, s59, v30\n"
      "v_fma_f32 v37, v37, s59, v30\n"
      "v_mfma_f32_16x16x32_bf16 v[40:43], a[148:151], a[116:119], v[40:43]\n"
      "v_fma_f32 v38, v38, s59, v30\n"
      "v_fma_f32 v39, v39, s59, v30\n"
      "v_mfma_f32_16x16x32_bf16 v[40:43], a[144:147], a[112:115], v[40:43]\n"
      "v_exp_f32 v36, v36\n"
      "v_mfma_f32_16x16x32_bf16 v[40:43], a[140:143], a[108:111], v[40:43]\n"
      "v_exp_f32 v37, v37\n"
      "v_mfma_f32_16x16x32_bf16 v[40:43], a[136:139], a[104:107], v[40:43]\n"
      "v_exp_f32 v38, v38\n"
      "v_mfma_f32_16x16x32_bf16 v[40:43], a[132:135], a[100:103], v[40:43]\n"
      "v_exp_f32 v39, v39\n"
      "v_mfma_f32_16x16x32_bf16 v[40:43], a[128:131], a[96:99], v[40:43]\n"
      "s_waitcnt vmcnt(15) lgkmcnt(0)\n"
      "s_barrier\n"
      "v_mfma_f32_16x16x32_bf16 v[144:147], a[160:163], v[64:67], v[144:147]\n"
      "s_add_u32 s52, s52, 0x4000\n"
      "ds_read_b128 a[128:131], v1 offset:2048\n"
      "v_cvt_pk_bf16_f32 v56, v36, v37\n"
      "v_mfma_f32_16x16x32_bf16 v[208:211], a[160:163], v[68:71], v[208:211]\n"
      "s_mov_b32 m0, s54\n"
      "ds_read_b128 a[132:135], v1 offset:2560\n"
      "v_add_f32 v34, v34, v36\n"
      "v_mfma_f32_16x16x32_bf16 v[96:99], a[160:163], v[72:75], v[96:99]\n"
      "s_add_u32 s53, s53, 0x4000\n"
      "ds_read_b128 a[136:139], v1 offset:6144\n"
      "v_add_f32 v34, v34, v37\n"
      "v_mfma_f32_16x16x32_bf16 a[208:211], a[160:163], v[76:79], a[208:211]\n"
      "s_add_u32 s90, s90, 0x4000\n"
      "ds_read_b128 a[140:143], v1 offset:6656\n"
      "v_cvt_pk_bf16_f32 v57, v38, v39\n"
      "v_mfma_f32_16x16x32_bf16 a[212:215], a[164:167], v[76:79], a[212:215]\n"
      "ds_read_b128 a[144:147], v1 offset:10240\n"
      "v_add_f32 v34, v34, v38\n"
      "v_mfma_f32_16x16x32_bf16 v[100:103], a[164:167], v[72:75], v[100:103]\n"
      "ds_read_b128 a[148:151], v1 offset:10752\n"
      "v_add_f32 v34, v34, v39\n"
      "v_mfma_f32_16x16x32_bf16 v[212:215], a[164:167], v[68:71], v[212:215]\n"
      "ds_read_b128 a[152:155], v1 offset:14336\n"
      "v_fma_f32 v40, v40, s59, v31\n"
      "v_mfma_f32_16x16x32_bf16 v[148:151], a[164:167], v[64:67], v[148:151]\n"
      "ds_read_b128 a[156:159], v1 offset:14848\n"
      "v_fma_f32 v41, v41, s59, v31\n"
      "v_mfma_f32_16x16x32_bf16 v[152:155], a[168:171], v[64:67], v[152:155]\n"
      "ds_read_b64_tr_b16 a[160:161], v2 offset:57344\n"
      "v_fma_f32 v42, v42, s59, v31\n"
      "v_mfma_f32_16x16x32_bf16 v[216:219], a[168:171], v[68:71], v[216:219]\n"
      "ds_read_b64_tr_b16 a[162:163], v2 offset:59392\n"
      "v_fma_f32 v43, v43, s59, v31\n"
      "v_mfma_f32_16x16x32_bf16 v[104:107], a[168:171], v[72:75], v[104:107]\n"
      "v_exp_f32 v40, v40\n"
      "v_mfma_f32_16x16x32_bf16 a[216:219], a[168:171], v[76:79], a[216:219]\n"
      "v_exp_f32 v41, v41\n"
      "v_mfma_f32_16x16x32_bf16 a[220:223], a[172:175], v[76:79], a[220:223]\n"
      "v_exp_f32 v42, v42\n"
      "v_mfma_f32_16x16x32_bf16 v[108:111], a[172:175], v[72:75], v[108:111]\n"
      "v_exp_f32 v43, v43\n"
      "v_mfma_f32_16x16x32_bf16 v[220:223], a[172:175], v[68:71], v[220:223]\n"
      "ds_read_b64_tr_b16 a[164:165], v3 offset:57344\n"
      "v_cvt_pk_bf16_f32 v60, v40, v41\n"
      "v_mfma_f32_16x16x32_bf16 v[156:159], a[172:175], v[64:67], v[156:159]\n"
      "s_waitcnt lgkmcnt(10)\n"
      "ds_read_b64_tr_b16 a[166:167], v3 offset:59392\n"
      "v_add_f32 v35, v35, v40\n"
      "v_mfma_f32_16x16x32_bf16 v[36:39], a[128:131], a[0:3], 0\n"
      "s_waitcnt lgkmcnt(10)\n"
      "buffer_load_dwordx4 v4, s[36:39], s52 offen offset:0 sc1 lds\n"
      "ds_read_b64_tr_b16 a[168:169], v2 offset:57856\n"
      "v_mfma_f32_16x16x32_bf16 v[36:39], a[132:135], a[4:7], v[36:39]\n"
      "s_waitcnt lgkmcnt(10)\n"
      "ds_read_b64_tr_b16 a[170:171], v2 offset:59904\n"
      "v_add_f32 v35, v35, v41\n"
      "v_mfma_f32_16x16x32_bf16 v[36:39], a[136:139], a[8:11], v[36:39]\n"
      "s_waitcnt lgkmcnt(10)\n"
      "ds_read_b64_tr_b16 a[172:173], v3 offset:57856\n"
      "v_cvt_pk_bf16_f32 v61, v42, v43\n"
      "v_mfma_f32_16x16x32_bf16 v[36:39], a[140:143], a[12:15], v[36:39]\n"
      "s_waitcnt lgkmcnt(10)\n"
      "ds_read_b64_tr_b16 a[174:175], v3 offset:59904\n"
      "v_add_f32 v35, v35, v42\n"
      "v_mfma_f32_16x16x32_bf16 v[36:39], a[144:147], a[16:19], v[36:39]\n"
      "s_waitcnt lgkmcnt(10)\n"
      "buffer_load_dwordx4 v4, s[36:39], s52 offen offset:1024 sc1 lds\n"
      "v_mfma_f32_16x16x32_bf16 v[36:39], a[148:151], a[20:23], v[36:39]\n"
      "s_waitcnt lgkmcnt(9)\n"
      "v_add_f32 v35, v35, v43\n"
      "v_mfma_f32_16x16x32_bf16 v[36:39], a[152:155], a[24:27], v[36:39]\n"
      "s_waitcnt lgkmcnt(8)\n"
      "v_mfma_f32_16x16x32_bf16 v[36:39], a[156:159], a[28:31], v[36:39]\n"
      "s_mov_b32 m0, s57\n"
      "s_nop 0\n"
      "s_nop 0\n"
      "v_mfma_f32_16x16x32_bf16 v[40:43], a[156:159], a[60:63], 0\n"
      "s_nop 0\n"
      "buffer_load_dwordx4 v4, s[40:43], s53 offen offset:0 sc1 lds\n"
      "s_nop 0\n"
      "v_mfma_f32_16x16x32_bf16 v[40:43], a[152:155], a[56:59], v[40:43]\n"
      "v_fma_f32 v36, v36, s59, v28\n"
      "v_fma_f32 v37, v37, s59, v28\n"
      "v_mfma_f32_16x16x32_bf16 v[40:43], a[148:151], a[52:55], v[40:43]\n"
      "v_fma_f32 v38, v38, s59, v28\n"
      "v_fma_f32 v39, v39, s59, v28\n"
      "v_mfma_f32_16x16x32_bf16 v[40:43], a[144:147], a[48:51], v[40:43]\n"
      "v_exp_f32 v36, v36\n"
      "v_mfma_f32_16x16x32_bf16 v[40:43], a[140:143], a[44:47], v[40:43]\n"
      "buffer_load_dwordx4 v4, s[40:43], s53 offen offset:1024 sc1 lds\n"
      "v_mfma_f32_16x16x32_bf16 v[40:43], a[136:139], a[40:43], v[40:43]\n"
      "s_mov_b32 m0, s74\n"
      "v_exp_f32 v37, v37\n"
      "v_mfma_f32_16x16x32_bf16 v[40:43], a[132:135], a[36:39], v[40:43]\n"
      "v_exp_f32 v38, v38\n"
      "v_mfma_f32_16x16x32_bf16 v[40:43], a[128:131], a[32:35], v[40:43]\n"
      "s_waitcnt lgkmcnt(6)\n"
      "v_exp_f32 v39, v39\n"
      "v_mfma_f32_16x16x32_bf16 v[160:163], a[160:163], v[64:67], v[160:163]\n"
      "s_nop 0\n"
      "buffer_load_dwordx4 v5, s[44:47], s75 offen offset:0 nt lds\n"
      "s_nop 0\n"
      "v_mfma_f32_16x16x32_bf16 v[224:227], a[160:163], v[68:71], v[224:227]\n"
      "s_nop 0\n"
      "v_cvt_pk_bf16_f32 v50, v36, v37\n"
      "v_add_f32 v32, v32, v36\n"
      "v_mfma_f32_16x16x32_bf16 v[112:115], a[160:163], v[72:75], v[112:115]\n"
      "v_add_f32 v32, v32, v37\n"
      "v_cvt_pk_bf16_f32 v51, v38, v39\n"
      "v_mfma_f32_16x16x32_bf16 a[224:227], a[160:163], v[76:79], a[224:227]\n"
      "s_waitcnt lgkmcnt(4)\n"
      "ds_read_b64_tr_b16 a[160:161], v2 offset:61440\n"
      "v_add_f32 v32, v32, v38\n"
      "v_mfma_f32_16x16x32_bf16 a[228:231], a[164:167], v[76:79], a[228:231]\n"
      "ds_read_b64_tr_b16 a[162:163], v2 offset:63488\n"
      "v_add_f32 v32, v32, v39\n"
      "v_mfma_f32_16x16x32_bf16 v[116:119], a[164:167], v[72:75], v[116:119]\n"
      "v_fma_f32 v40, v40, s59, v29\n"
      "v_fma_f32 v41, v41, s59, v29\n"
      "v_mfma_f32_16x16x32_bf16 v[228:231], a[164:167], v[68:71], v[228:231]\n"
      "v_fma_f32 v42, v42, s59, v29\n"
      "v_fma_f32 v43, v43, s59, v29\n"
      "v_mfma_f32_16x16x32_bf16 v[164:167], a[164:167], v[64:67], v[164:167]\n"
      "s_waitcnt lgkmcnt(4)\n"
      "v_exp_f32 v40, v40\n"
      "v_mfma_f32_16x16x32_bf16 v[168:171], a[168:171], v[64:67], v[168:171]\n"
      "v_exp_f32 v41, v41\n"
      "v_mfma_f32_16x16x32_bf16 v[232:235], a[168:171], v[68:71], v[232:235]\n"
      "v_exp_f32 v42, v42\n"
      "v_mfma_f32_16x16x32_bf16 v[120:123], a[168:171], v[72:75], v[120:123]\n"
      "v_exp_f32 v43, v43\n"
      "v_mfma_f32_16x16x32_bf16 a[232:235], a[168:171], v[76:79], a[232:235]\n"
      "s_waitcnt lgkmcnt(2)\n"
      "ds_read_b64_tr_b16 a[164:165], v3 offset:61440\n"
      "v_cvt_pk_bf16_f32 v54, v40, v41\n"
      "v_mfma_f32_16x16x32_bf16 a[236:239], a[172:175], v[76:79], a[236:239]\n"
      "ds_read_b64_tr_b16 a[166:167], v3 offset:63488\n"
      "v_add_f32 v33, v33, v40\n"
      "v_mfma_f32_16x16x32_bf16 v[124:127], a[172:175], v[72:75], v[124:127]\n"
      "ds_read_b64_tr_b16 a[168:169], v2 offset:61952\n"
      "v_add_f32 v33, v33, v41\n"
      "v_mfma_f32_16x16x32_bf16 v[236:239], a[172:175], v[68:71], v[236:239]\n"
      "ds_read_b64_tr_b16 a[170:171], v2 offset:64000\n"
      "v_cvt_pk_bf16_f32 v55, v42, v43\n"
      "v_mfma_f32_16x16x32_bf16 v[172:175], a[172:175], v[64:67], v[172:175]\n"
      "ds_read_b64_tr_b16 a[172:173], v3 offset:61952\n"
      "v_add_f32 v33, v33, v42\n"
      "v_mfma_f32_16x16x32_bf16 v[36:39], a[128:131], a[64:67], 0\n"
      "ds_read_b64_tr_b16 a[174:175], v3 offset:64000\n"
      "v_add_f32 v33, v33, v43\n"
      "v_mfma_f32_16x16x32_bf16 v[36:39], a[132:135], a[68:71], v[36:39]\n"
      "v_mfma_f32_16x16x32_bf16 v[36:39], a[136:139], a[72:75], v[36:39]\n"
      "v_mfma_f32_16x16x32_bf16 v[36:39], a[140:143], a[76:79], v[36:39]\n"
      "v_mfma_f32_16x16x32_bf16 v[36:39], a[144:147], a[80:83], v[36:39]\n"
      "v_mfma_f32_16x16x32_bf16 v[36:39], a[148:151], a[84:87], v[36:39]\n"
      "v_mfma_f32_16x16x32_bf16 v[36:39], a[152:155], a[88:91], v[36:39]\n"
      "v_mfma_f32_16x16x32_bf16 v[36:39], a[156:159], a[92:95], v[36:39]\n"
      "s_nop 0\n"
      "s_nop 0\n"
      "s_nop 0\n"
      "v_mfma_f32_16x16x32_bf16 v[40:43], a[156:159], a[124:127], 0\n"
      "s_nop 0\n"
      "s_nop 0\n"
      "s_nop 0\n"
      "v_mfma_f32_16x16x32_bf16 v[40:43], a[152:155], a[120:123], v[40:43]\n"
      "v_fma_f32 v36, v36, s59, v30\n"
      "v_fma_f32 v37, v37, s59, v30\n"
      "v_mfma_f32_16x16x32_bf16 v[40:43], a[148:151], a[116:119], v[40:43]\n"
      "v_fma_f32 v38, v38, s59, v30\n"
      "v_fma_f32 v39, v39, s59, v30\n"
      "v_mfma_f32_16x16x32_bf16 v[40:43], a[144:147], a[112:115], v[40:43]\n"
      "v_exp_f32 v36, v36\n"
      "v_mfma_f32_16x16x32_bf16 v[40:43], a[140:143], a[108:111], v[40:43]\n"
      "v_exp_f32 v37, v37\n"
      "v_mfma_f32_16x16x32_bf16 v[40:43], a[136:139], a[104:107], v[40:43]\n"
      "v_exp_f32 v38, v38\n"
      "v_mfma_f32_16x16x32_bf16 v[40:43], a[132:135], a[100:103], v[40:43]\n"
      "v_exp_f32 v39, v39\n"
      "v_mfma_f32_16x16x32_bf16 v[40:43], a[128:131], a[96:99], v[40:43]\n"
      "s_waitcnt vmcnt(15) lgkmcnt(0)\n"
      "s_barrier\n"
      "v_mfma_f32_16x16x32_bf16 v[176:179], a[160:163], v[64:67], v[176:179]\n"
      "s_mov_b32 m0, s54\n"
      "ds_read_b128 a[128:131], v1 offset:16384\n"
      "v_cvt_pk_bf16_f32 v58, v36, v37\n"
      "v_mfma_f32_16x16x32_bf16 v[240:243], a[160:163], v[68:71], v[240:243]\n"
      "s_nop 0\n"
      "ds_read_b128 a[132:135], v1 offset:16896\n"
      "v_add_f32 v34, v34, v36\n"
      "v_mfma_f32_16x16x32_bf16 a[176:179], a[160:163], v[72:75], a[176:179]\n"
      "ds_read_b128 a[136:139], v1 offset:20480\n"
      "v_add_f32 v34, v34, v37\n"
      "v_mfma_f32_16x16x32_bf16 a[240:243], a[160:163], v[76:79], a[240:243]\n"
      "ds_read_b128 a[140:143], v1 offset:20992\n"
      "v_cvt_pk_bf16_f32 v59, v38, v39\n"
      "v_mfma_f32_16x16x32_bf16 a[244:247], a[164:167], v[76:79], a[244:247]\n"
      "ds_read_b128 a[144:147], v1 offset:24576\n"
      "v_add_f32 v34, v34, v38\n"
      "v_mfma_f32_16x16x32_bf16 a[180:183], a[164:167], v[72:75], a[180:183]\n"
      "ds_read_b128 a[148:151], v1 offset:25088\n"
      "v_add_f32 v34, v34, v39\n"
      "v_mfma_f32_16x16x32_bf16 v[244:247], a[164:167], v[68:71], v[244:247]\n"
      "ds_read_b128 a[152:155], v1 offset:28672\n"
      "v_fma_f32 v40, v40, s59, v31\n"
      "v_mfma_f32_16x16x32_bf16 v[180:183], a[164:167], v[64:67], v[180:183]\n"
      "ds_read_b128 a[156:159], v1 offset:29184\n"
      "v_fma_f32 v41, v41, s59, v31\n"
      "v_mfma_f32_16x16x32_bf16 v[184:187], a[168:171], v[64:67], v[184:187]\n"
      "ds_read_b64_tr_b16 a[160:161], v2 offset:32768\n"
      "v_fma_f32 v42, v42, s59, v31\n"
      "v_mfma_f32_16x16x32_bf16 v[248:251], a[168:171], v[68:71], v[248:251]\n"
      "ds_read_b64_tr_b16 a[162:163], v2 offset:34816\n"
      "v_fma_f32 v43, v43, s59, v31\n"
      "v_mfma_f32_16x16x32_bf16 a[184:187], a[168:171], v[72:75], a[184:187]\n"
      "v_exp_f32 v40, v40\n"
      "v_mfma_f32_16x16x32_bf16 a[248:251], a[168:171], v[76:79], a[248:251]\n"
      "v_exp_f32 v41, v41\n"
      "v_mfma_f32_16x16x32_bf16 a[252:255], a[172:175], v[76:79], a[252:255]\n"
      "v_exp_f32 v42, v42\n"
      "v_mfma_f32_16x16x32_bf16 a[188:191], a[172:175], v[72:75], a[188:191]\n"
      "v_exp_f32 v43, v43\n"
      "v_mfma_f32_16x16x32_bf16 v[252:255], a[172:175], v[68:71], v[252:255]\n"
      "ds_read_b64_tr_b16 a[164:165], v3 offset:32768\n"
      "v_cvt_pk_bf16_f32 v62, v40, v41\n"
      "v_mfma_f32_16x16x32_bf16 v[188:191], a[172:175], v[64:67], v[188:191]\n"
      "s_waitcnt lgkmcnt(10)\n"
      "ds_read_b64_tr_b16 a[166:167], v3 offset:34816\n"
      "v_add_f32 v35, v35, v40\n"
      "v_mfma_f32_16x16x32_bf16 v[36:39], a[128:131], a[0:3], 0\n"
      "s_waitcnt lgkmcnt(10)\n"
      "buffer_load_dwordx4 v4, s[36:39], s52 offen offset:2048 sc1 lds\n"
      "ds_read_b64_tr_b16 a[168:169], v2 offset:33280\n"
      "v_mfma_f32_16x16x32_bf16 v[36:39], a[132:135], a[4:7], v[36:39]\n"
      "s_waitcnt lgkmcnt(10)\n"
      "ds_read_b64_tr_b16 a[170:171], v2 offset:35328\n"
      "v_add_f32 v35, v35, v41\n"
      "v_mfma_f32_16x16x32_bf16 v[36:39], a[136:139], a[8:11], v[36:39]\n"
      "s_waitcnt lgkmcnt(10)\n"
      "ds_read_b64_tr_b16 a[172:173], v3 offset:33280\n"
      "v_cvt_pk_bf16_f32 v63, v42, v43\n"
      "v_mfma_f32_16x16x32_bf16 v[36:39], a[140:143], a[12:15], v[36:39]\n"
      "s_waitcnt lgkmcnt(10)\n"
      "ds_read_b64_tr_b16 a[174:175], v3 offset:35328\n"
      "v_add_f32 v35, v35, v42\n"
      "v_mfma_f32_16x16x32_bf16 v[36:39], a[144:147], a[16:19], v[36:39]\n"
      "s_waitcnt lgkmcnt(10)\n"
      "buffer_load_dwordx4 v4, s[36:39], s52 offen offset:3072 sc1 lds\n"
      "v_mfma_f32_16x16x32_bf16 v[36:39], a[148:151], a[20:23], v[36:39]\n"
      "s_waitcnt lgkmcnt(9)\n"
      "v_add_f32 v35, v35, v43\n"
      "v_mfma_f32_16x16x32_bf16 v[36:39], a[152:155], a[24:27], v[36:39]\n"
      "s_waitcnt lgkmcnt(8)\n"
      "v_mfma_f32_16x16x32_bf16 v[36:39], a[156:159], a[28:31], v[36:39]\n"
      "s_add_u32 m0, s57, 0x2000\n"
      "s_nop 0\n"
      "s_nop 0\n"
      "v_mfma_f32_16x16x32_bf16 v[40:43], a[156:159], a[60:63], 0\n"
      "s_nop 0\n"
      "buffer_load_dwordx4 v4, s[40:43], s90 offen offset:0 sc1 lds\n"
      "s_nop 0\n"
      "v_mfma_f32_16x16x32_bf16 v[40:43], a[152:155], a[56:59], v[40:43]\n"
      "v_fma_f32 v36, v36, s59, v28\n"
      "v_fma_f32 v37, v37, s59, v28\n"
      "v_mfma_f32_16x16x32_bf16 v[40:43], a[148:151], a[52:55], v[40:43]\n"
      "v_fma_f32 v38, v38, s59, v28\n"
      "v_fma_f32 v39, v39, s59, v28\n"
      "v_mfma_f32_16x16x32_bf16 v[40:43], a[144:147], a[48:51], v[40:43]\n"
      "v_exp_f32 v36, v36\n"
      "v_mfma_f32_16x16x32_bf16 v[40:43], a[140:143], a[44:47], v[40:43]\n"
      "buffer_load_dwordx4 v4, s[40:43], s90 offen offset:1024 sc1 lds\n"
      "v_mfma_f32_16x16x32_bf16 v[40:43], a[136:139], a[40:43], v[40:43]\n"
      "s_mov_b32 m0, s74\n"
      "v_exp_f32 v37, v37\n"
      "v_mfma_f32_16x16x32_bf16 v[40:43], a[132:135], a[36:39], v[40:43]\n"
      "v_exp_f32 v38, v38\n"
      "v_mfma_f32_16x16x32_bf16 v[40:43], a[128:131], a[32:35], v[40:43]\n"
      "s_waitcnt lgkmcnt(6)\n"
      "v_exp_f32 v39, v39\n"
      "v_mfma_f32_16x16x32_bf16 v[128:131], a[160:163], v[48:51], v[128:131]\n"
      "s_nop 0\n"
      "buffer_load_dwordx4 v5, s[44:47], s75 offen offset:1024 nt lds\n"
      "s_nop 0\n"
      "v_mfma_f32_16x16x32_bf16 v[192:195], a[160:163], v[52:55], v[192:195]\n"
      "s_nop 0\n"
      "v_cvt_pk_bf16_f32 v64, v36, v37\n"
      "v_add_f32 v32, v32, v36\n"
      "v_mfma_f32_16x16x32_bf16 v[80:83], a[160:163], v[56:59], v[80:83]\n"
      "v_add_f32 v32, v32, v37\n"
      "v_cvt_pk_bf16_f32 v65, v38, v39\n"
      "v_mfma_f32_16x16x32_bf16 a[192:195], a[160:163], v[60:63], a[192:195]\n"
      "s_waitcnt lgkmcnt(4)\n"
      "ds_read_b64_tr_b16 a[160:161], v2 offset:36864\n"
      "v_add_f32 v32, v32, v38\n"
      "v_mfma_f32_16x16x32_bf16 a[196:199], a[164:167], v[60:63], a[196:199]\n"
      "ds_read_b64_tr_b16 a[162:163], v2 offset:38912\n"
      "v_add_f32 v32, v32, v39\n"
      "v_mfma_f32_16x16x32_bf16 v[84:87], a[164:167], v[56:59], v[84:87]\n"
      "v_fma_f32 v40, v40, s59, v29\n"
      "v_fma_f32 v41, v41, s59, v29\n"
      "v_mfma_f32_16x16x32_bf16 v[196:199], a[164:167], v[52:55], v[196:199]\n"
      "v_fma_f32 v42, v42, s59, v29\n"
      "v_fma_f32 v43, v43, s59, v29\n"
      "v_mfma_f32_16x16x32_bf16 v[132:135], a[164:167], v[48:51], v[132:135]\n"
      "s_waitcnt lgkmcnt(4)\n"
      "v_exp_f32 v40, v40\n"
      "v_mfma_f32_16x16x32_bf16 v[136:139], a[168:171], v[48:51], v[136:139]\n"
      "v_exp_f32 v41, v41\n"
      "v_mfma_f32_16x16x32_bf16 v[200:203], a[168:171], v[52:55], v[200:203]\n"
      "v_exp_f32 v42, v42\n"
      "v_mfma_f32_16x16x32_bf16 v[88:91], a[168:171], v[56:59], v[88:91]\n"
      "v_exp_f32 v43, v43\n"
      "v_mfma_f32_16x16x32_bf16 a[200:203], a[168:171], v[60:63], a[200:203]\n"
      "s_waitcnt lgkmcnt(2)\n"
      "ds_read_b64_tr_b16 a[164:165], v3 offset:36864\n"
      "v_cvt_pk_bf16_f32 v68, v40, v41\n"
      "v_mfma_f32_16x16x32_bf16 a[204:207], a[172:175], v[60:63], a[204:207]\n"
      "ds_read_b64_tr_b16 a[166:167], v3 offset:38912\n"
      "v_add_f32 v33, v33, v40\n"
      "v_mfma_f32_16x16x32_bf16 v[92:95], a[172:175], v[56:59], v[92:95]\n"
      "ds_read_b64_tr_b16 a[168:169], v2 offset:37376\n"
      "v_add_f32 v33, v33, v41\n"
      "v_mfma_f32_16x16x32_bf16 v[204:207], a[172:175], v[52:55], v[204:207]\n"
      "ds_read_b64_tr_b16 a[170:171], v2 offset:39424\n"
      "v_cvt_pk_bf16_f32 v69, v42, v43\n"
      "v_mfma_f32_16x16x32_bf16 v[140:143], a[172:175], v[48:51], v[140:143]\n"
      "ds_read_b64_tr_b16 a[172:173], v3 offset:37376\n"
      "v_add_f32 v33, v33, v42\n"
      "v_mfma_f32_16x16x32_bf16 v[36:39], a[128:131], a[64:67], 0\n"
      "ds_read_b64_tr_b16 a[174:175], v3 offset:39424\n"
      "v_add_f32 v33, v33, v43\n"
      "v_mfma_f32_16x16x32_bf16 v[36:39], a[132:135], a[68:71], v[36:39]\n"
      "v_mfma_f32_16x16x32_bf16 v[36:39], a[136:139], a[72:75], v[36:39]\n"
      "v_mfma_f32_16x16x32_bf16 v[36:39], a[140:143], a[76:79], v[36:39]\n"
      "v_mfma_f32_16x16x32_bf16 v[36:39], a[144:147], a[80:83], v[36:39]\n"
      "v_mfma_f32_16x16x32_bf16 v[36:39], a[148:151], a[84:87], v[36:39]\n"
      "v_mfma_f32_16x16x32_bf16 v[36:39], a[152:155], a[88:91], v[36:39]\n"
      "v_mfma_f32_16x16x32_bf16 v[36:39], a[156:159], a[92:95], v[36:39]\n"
      "s_nop 0\n"
      "s_nop 0\n"
      "s_nop 0\n"
      "v_mfma_f32_16x16x32_bf16 v[40:43], a[156:159], a[124:127], 0\n"
      "s_nop 0\n"
      "s_nop 0\n"
      "s_nop 0\n"
      "v_mfma_f32_16x16x32_bf16 v[40:43], a[152:155], a[120:123], v[40:43]\n"
      "v_fma_f32 v36, v36, s59, v30\n"
      "v_fma_f32 v37, v37, s59, v30\n"
      "v_mfma_f32_16x16x32_bf16 v[40:43], a[148:151], a[116:119], v[40:43]\n"
      "v_fma_f32 v38, v38, s59, v30\n"
      "v_fma_f32 v39, v39, s59, v30\n"
      "v_mfma_f32_16x16x32_bf16 v[40:43], a[144:147], a[112:115], v[40:43]\n"
      "v_exp_f32 v36, v36\n"
      "v_mfma_f32_16x16x32_bf16 v[40:43], a[140:143], a[108:111], v[40:43]\n"
      "v_exp_f32 v37, v37\n"
      "v_mfma_f32_16x16x32_bf16 v[40:43], a[136:139], a[104:107], v[40:43]\n"
      "v_exp_f32 v38, v38\n"
      "v_mfma_f32_16x16x32_bf16 v[40:43], a[132:135], a[100:103], v[40:43]\n"
      "v_exp_f32 v39, v39\n"
      "v_mfma_f32_16x16x32_bf16 v[40:43], a[128:131], a[96:99], v[40:43]\n"
      "s_waitcnt vmcnt(13) lgkmcnt(0)\n"
      "s_barrier\n"
      "v_mfma_f32_16x16x32_bf16 v[144:147], a[160:163], v[48:51], v[144:147]\n"
      "s_add_u32 s52, s52, 0x4000\n"
      "ds_read_b128 a[128:131], v1 offset:18432\n"
      "v_cvt_pk_bf16_f32 v72, v36, v37\n"
      "v_mfma_f32_16x16x32_bf16 v[208:211], a[160:163], v[52:55], v[208:211]\n"
      "s_mov_b32 m0, s55\n"
      "ds_read_b128 a[132:135], v1 offset:18944\n"
      "v_add_f32 v34, v34, v36\n"
      "v_mfma_f32_16x16x32_bf16 v[96:99], a[160:163], v[56:59], v[96:99]\n"
      "s_add_u32 s53, s53, 0x4000\n"
      "ds_read_b128 a[136:139], v1 offset:22528\n"
      "v_add_f32 v34, v34, v37\n"
      "v_mfma_f32_16x16x32_bf16 a[208:211], a[160:163], v[60:63], a[208:211]\n"
      "s_add_u32 s90, s90, 0x4000\n"
      "ds_read_b128 a[140:143], v1 offset:23040\n"
      "v_cvt_pk_bf16_f32 v73, v38, v39\n"
      "v_mfma_f32_16x16x32_bf16 a[212:215], a[164:167], v[60:63], a[212:215]\n"
      "s_add_u32 s96, s74, 0x800\n"
      "ds_read_b128 a[144:147], v1 offset:26624\n"
      "v_add_f32 v34, v34, v38\n"
      "v_mfma_f32_16x16x32_bf16 v[100:103], a[164:167], v[56:59], v[100:103]\n"
      "s_add_u32 s97, s75, 0x80\n"
      "ds_read_b128 a[148:151], v1 offset:27136\n"
      "v_add_f32 v34, v34, v39\n"
      "v_mfma_f32_16x16x32_bf16 v[212:215], a[164:167], v[52:55], v[212:215]\n"
      "s_add_u32 s61, s61, 1\n"
      "ds_read_b128 a[152:155], v1 offset:30720\n"
      "v_fma_f32 v40, v40, s59, v31\n"
      "v_mfma_f32_16x16x32_bf16 v[148:151], a[164:167], v[48:51], v[148:151]\n"
      "s_cmp_lt_u32 s61, 7\n"
      "ds_read_b128 a[156:159], v1 offset:31232\n"
      "v_fma_f32 v41, v41, s59, v31\n"
      "v_mfma_f32_16x16x32_bf16 v[152:155], a[168:171], v[48:51], v[152:155]\n"
      "ds_read_b64_tr_b16 a[160:161], v2 offset:40960\n"
      "v_fma_f32 v42, v42, s59, v31\n"
      "v_mfma_f32_16x16x32_bf16 v[216:219], a[168:171], v[52:55], v[216:219]\n"
      "ds_read_b64_tr_b16 a[162:163], v2 offset:43008\n"
      "v_fma_f32 v43, v43, s59, v31\n"
      "v_mfma_f32_16x16x32_bf16 v[104:107], a[168:171], v[56:59], v[104:107]\n"
      "v_exp_f32 v40, v40\n"
      "v_mfma_f32_16x16x32_bf16 a[216:219], a[168:171], v[60:63], a[216:219]\n"
      "v_exp_f32 v41, v41\n"
      "v_mfma_f32_16x16x32_bf16 a[220:223], a[172:175], v[60:63], a[220:223]\n"
      "v_exp_f32 v42, v42\n"
      "v_mfma_f32_16x16x32_bf16 v[108:111], a[172:175], v[56:59], v[108:111]\n"
      "v_exp_f32 v43, v43\n"
      "v_mfma_f32_16x16x32_bf16 v[220:223], a[172:175], v[52:55], v[220:223]\n"
      "ds_read_b64_tr_b16 a[164:165], v3 offset:40960\n"
      "v_cvt_pk_bf16_f32 v76, v40, v41\n"
      "v_mfma_f32_16x16x32_bf16 v[156:159], a[172:175], v[48:51], v[156:159]\n"
      "s_waitcnt lgkmcnt(10)\n"
      "ds_read_b64_tr_b16 a[166:167], v3 offset:43008\n"
      "v_add_f32 v35, v35, v40\n"
      "v_mfma_f32_16x16x32_bf16 v[36:39], a[128:131], a[0:3], 0\n"
      "s_waitcnt lgkmcnt(10)\n"
      "buffer_load_dwordx4 v4, s[36:39], s52 offen offset:0 sc1 lds\n"
      "ds_read_b64_tr_b16 a[168:169], v2 offset:41472\n"
      "v_mfma_f32_16x16x32_bf16 v[36:39], a[132:135], a[4:7], v[36:39]\n"
      "s_waitcnt lgkmcnt(10)\n"
      "ds_read_b64_tr_b16 a[170:171], v2 offset:43520\n"
      "v_add_f32 v35, v35, v41\n"
      "v_mfma_f32_16x16x32_bf16 v[36:39], a[136:139], a[8:11], v[36:39]\n"
      "s_waitcnt lgkmcnt(10)\n"
      "ds_read_b64_tr_b16 a[172:173], v3 offset:41472\n"
      "v_cvt_pk_bf16_f32 v77, v42, v43\n"
      "v_mfma_f32_16x16x32_bf16 v[36:39], a[140:143], a[12:15], v[36:39]\n"
      "s_waitcnt lgkmcnt(10)\n"
      "ds_read_b64_tr_b16 a[174:175], v3 offset:43520\n"
      "v_add_f32 v35, v35, v42\n"
      "v_mfma_f32_16x16x32_bf16 v[36:39], a[144:147], a[16:19], v[36:39]\n"
      "s_waitcnt lgkmcnt(10)\n"
      "buffer_load_dwordx4 v4, s[36:39], s52 offen offset:1024 sc1 lds\n"
      "v_mfma_f32_16x16x32_bf16 v[36:39], a[148:151], a[20:23], v[36:39]\n"
      "s_waitcnt lgkmcnt(9)\n"
      "v_add_f32 v35, v35, v43\n"
      "v_mfma_f32_16x16x32_bf16 v[36:39], a[152:155], a[24:27], v[36:39]\n"
      "s_waitcnt lgkmcnt(8)\n"
      "v_mfma_f32_16x16x32_bf16 v[36:39], a[156:159], a[28:31], v[36:39]\n"
      "s_mov_b32 m0, s56\n"
      "s_nop 0\n"
      "s_nop 0\n"
      "v_mfma_f32_16x16x32_bf16 v[40:43], a[156:159], a[60:63], 0\n"
      "s_nop 0\n"
      "buffer_load_dwordx4 v4, s[40:43], s53 offen offset:0 sc1 lds\n"
      "s_nop 0\n"
      "v_mfma_f32_16x16x32_bf16 v[40:43], a[152:155], a[56:59], v[40:43]\n"
      "v_fma_f32 v36, v36, s59, v28\n"
      "v_fma_f32 v37, v37, s59, v28\n"
      "v_mfma_f32_16x16x32_bf16 v[40:43], a[148:151], a[52:55], v[40:43]\n"
      "v_fma_f32 v38, v38, s59, v28\n"
      "v_fma_f32 v39, v39, s59, v28\n"
      "v_mfma_f32_16x16x32_bf16 v[40:43], a[144:147], a[48:51], v[40:43]\n"
      "v_exp_f32 v36, v36\n"
      "v_mfma_f32_16x16x32_bf16 v[40:43], a[140:143], a[44:47], v[40:43]\n"
      "buffer_load_dwordx4 v4, s[40:43], s53 offen offset:1024 sc1 lds\n"
      "v_mfma_f32_16x16x32_bf16 v[40:43], a[136:139], a[40:43], v[40:43]\n"
      "s_mov_b32 m0, s96\n"
      "v_exp_f32 v37, v37\n"
      "v_mfma_f32_16x16x32_bf16 v[40:43], a[132:135], a[36:39], v[40:43]\n"
      "v_exp_f32 v38, v38\n"
      "v_mfma_f32_16x16x32_bf16 v[40:43], a[128:131], a[32:35], v[40:43]\n"
      "s_waitcnt lgkmcnt(6)\n"
      "v_exp_f32 v39, v39\n"
      "v_mfma_f32_16x16x32_bf16 v[160:163], a[160:163], v[48:51], v[160:163]\n"
      "s_nop 0\n"
      "buffer_load_dwordx4 v5, s[44:47], s97 offen offset:0 nt lds\n"
      "s_nop 0\n"
      "v_mfma_f32_16x16x32_bf16 v[224:227], a[160:163], v[52:55], v[224:227]\n"
      "s_nop 0\n"
      "v_cvt_pk_bf16_f32 v66, v36, v37\n"
      "v_add_f32 v32, v32, v36\n"
      "v_mfma_f32_16x16x32_bf16 v[112:115], a[160:163], v[56:59], v[112:115]\n"
      "v_add_f32 v32, v32, v37\n"
      "v_cvt_pk_bf16_f32 v67, v38, v39\n"
      "v_mfma_f32_16x16x32_bf16 a[224:227], a[160:163], v[60:63], a[224:227]\n"
      "s_waitcnt lgkmcnt(4)\n"
      "ds_read_b64_tr_b16 a[160:161], v2 offset:45056\n"
      "v_add_f32 v32, v32, v38\n"
      "v_mfma_f32_16x16x32_bf16 a[228:231], a[164:167], v[60:63], a[228:231]\n"
      "ds_read_b64_tr_b16 a[162:163], v2 offset:47104\n"
      "v_add_f32 v32, v32, v39\n"
      "v_mfma_f32_16x16x32_bf16 v[116:119], a[164:167], v[56:59], v[116:119]\n"
      "v_fma_f32 v40, v40, s59, v29\n"
      "v_fma_f32 v41, v41, s59, v29\n"
      "v_mfma_f32_16x16x32_bf16 v[228:231], a[164:167], v[52:55], v[228:231]\n"
      "v_fma_f32 v42, v42, s59, v29\n"
      "v_fma_f32 v43, v43, s59, v29\n"
      "v_mfma_f32_16x16x32_bf16 v[164:167], a[164:167], v[48:51], v[164:167]\n"
      "s_waitcnt lgkmcnt(4)\n"
      "v_exp_f32 v40, v40\n"
      "v_mfma_f32_16x16x32_bf16 v[168:171], a[168:171], v[48:51], v[168:171]\n"
      "v_exp_f32 v41, v41\n"
      "v_mfma_f32_16x16x32_bf16 v[232:235], a[168:171], v[52:55], v[232:235]\n"
      "v_exp_f32 v42, v42\n"
      "v_mfma_f32_16x16x32_bf16 v[120:123], a[168:171], v[56:59], v[120:123]\n"
      "v_exp_f32 v43, v43\n"
      "v_mfma_f32_16x16x32_bf16 a[232:235], a[168:171], v[60:63], a[232:235]\n"
      "s_waitcnt lgkmcnt(2)\n"
      "ds_read_b64_tr_b16 a[164:165], v3 offset:45056\n"
      "v_cvt_pk_bf16_f32 v70, v40, v41\n"
      "v_mfma_f32_16x16x32_bf16 a[236:239], a[172:175], v[60:63], a[236:239]\n"
      "ds_read_b64_tr_b16 a[166:167], v3 offset:47104\n"
      "v_add_f32 v33, v33, v40\n"
      "v_mfma_f32_16x16x32_bf16 v[124:127], a[172:175], v[56:59], v[124:127]\n"
      "ds_read_b64_tr_b16 a[168:169], v2 offset:45568\n"
      "v_add_f32 v33, v33, v41\n"
      "v_mfma_f32_16x16x32_bf16 v[236:239], a[172:175], v[52:55], v[236:239]\n"
      "ds_read_b64_tr_b16 a[170:171], v2 offset:47616\n"
      "v_cvt_pk_bf16_f32 v71, v42, v43\n"
      "v_mfma_f32_16x16x32_bf16 v[172:175], a[172:175], v[48:51], v[172:175]\n"
      "ds_read_b64_tr_b16 a[172:173], v3 offset:45568\n"
      "v_add_f32 v33, v33, v42\n"
      "v_mfma_f32_16x16x32_bf16 v[36:39], a[128:131], a[64:67], 0\n"
      "ds_read_b64_tr_b16 a[174:175], v3 offset:47616\n"
      "v_add_f32 v33, v33, v43\n"
      "v_mfma_f32_16x16x32_bf16 v[36:39], a[132:135], a[68:71], v[36:39]\n"
      "v_mfma_f32_16x16x32_bf16 v[36:39], a[136:139], a[72:75], v[36:39]\n"
      "v_mfma_f32_16x16x32_bf16 v[36:39], a[140:143], a[76:79], v[36:39]\n"
      "v_mfma_f32_16x16x32_bf16 v[36:39], a[144:147], a[80:83], v[36:39]\n"
      "v_mfma_f32_16x16x32_bf16 v[36:39], a[148:151], a[84:87], v[36:39]\n"
      "v_mfma_f32_16x16x32_bf16 v[36:39], a[152:155], a[88:91], v[36:39]\n"
      "v_mfma_f32_16x16x32_bf16 v[36:39], a[156:159], a[92:95], v[36:39]\n"
      "s_nop 0\n"
      "s_nop 0\n"
      "s_nop 0\n"
      "v_mfma_f32_16x16x32_bf16 v[40:43], a[156:159], a[124:127], 0\n"
      "s_nop 0\n"
      "s_nop 0\n"
      "s_nop 0\n"
      "v_mfma_f32_16x16x32_bf16 v[40:43], a[152:155], a[120:123], v[40:43]\n"
      "s_cbranch_scc0 LEX4a_%=\n"
      "v_fma_f32 v36, v36, s59, v30\n"
      "v_fma_f32 v37, v37, s59, v30\n"
      "v_mfma_f32_16x16x32_bf16 v[40:43], a[148:151], a[116:119], v[40:43]\n"
      "v_fma_f32 v38, v38, s59, v30\n"
      "v_fma_f32 v39, v39, s59, v30\n"
      "v_mfma_f32_16x16x32_bf16 v[40:43], a[144:147], a[112:115], v[40:43]\n"
      "v_exp_f32 v36, v36\n"
      "v_mfma_f32_16x16x32_bf16 v[40:43], a[140:143], a[108:111], v[40:43]\n"
      "v_exp_f32 v37, v37\n"
      "v_mfma_f32_16x16x32_bf16 v[40:43], a[136:139], a[104:107], v[40:43]\n"
      "v_mfma_f32_16x16x32_bf16 v[40:43], a[132:135], a[100:103], v[40:43]\n"
      "v_mfma_f32_16x16x32_bf16 v[40:43], a[128:131], a[96:99], v[40:43]\n"
      "s_waitcnt vmcnt(11) lgkmcnt(0)\n"
      "s_barrier\n"
      "v_mfma_f32_16x16x32_bf16 v[176:179], a[160:163], v[48:51], v[176:179]\n"
      "s_lshl_b32 s78, s61, 12\n"
      "ds_read_b128 v[16:19], v6 offset:0\n"
      "v_cvt_pk_bf16_f32 v74, v36, v37\n"
      "v_mfma_f32_16x16x32_bf16 v[240:243], a[160:163], v[52:55], v[240:243]\n"
      "s_add_u32 s76, s91, s78\n"
      "ds_read_b128 a[128:131], v1 offset:0\n"
      "v_add_f32 v34, v34, v36\n"
      "v_mfma_f32_16x16x32_bf16 a[176:179], a[160:163], v[56:59], a[176:179]\n"
      "s_add_u32 s74, s58, s78\n"
      "ds_read_b128 a[132:135], v1 offset:512\n"
      "v_add_f32 v34, v34, v37\n"
      "v_mfma_f32_16x16x32_bf16 a[240:243], a[160:163], v[60:63], a[240:243]\n"
      "s_lshr_b32 s78, s61, 1\n"
      "ds_read_b128 a[136:139], v1 offset:4096\n"
      "v_fma_f32 v40, v40, s59, v31\n"
      "v_mfma_f32_16x16x32_bf16 a[244:247], a[164:167], v[60:63], a[244:247]\n"
      "s_lshl_b32 s78, s78, 13\n"
      "ds_read_b128 a[140:143], v1 offset:4608\n"
      "v_fma_f32 v41, v41, s59, v31\n"
      "v_mfma_f32_16x16x32_bf16 a[180:183], a[164:167], v[56:59], a[180:183]\n"
      "s_and_b32 s79, s61, 1\n"
      "ds_read_b128 a[144:147], v1 offset:8192\n"
      "v_fma_f32 v42, v42, s59, v31\n"
      "v_mfma_f32_16x16x32_bf16 v[244:247], a[164:167], v[52:55], v[244:247]\n"
      "s_lshl_b32 s79, s79, 8\n"
      "ds_read_b128 a[148:151], v1 offset:8704\n"
      "v_fma_f32 v43, v43, s59, v31\n"
      "v_mfma_f32_16x16x32_bf16 v[180:183], a[164:167], v[48:51], v[180:183]\n"
      "s_add_u32 s75, s78, s79\n"
      "ds_read_b128 a[152:155], v1 offset:12288\n"
      "v_mfma_f32_16x16x32_bf16 v[184:187], a[168:171], v[48:51], v[184:187]\n"
      "s_add_u32 s75, s75, s92\n"
      "v_exp_f32 v38, v38\n"
      "v_mfma_f32_16x16x32_bf16 v[248:251], a[168:171], v[52:55], v[248:251]\n"
      "s_cmp_lt_u32 s61, 5\n"
      "v_exp_f32 v39, v39\n"
      "v_mfma_f32_16x16x32_bf16 a[184:187], a[168:171], v[56:59], a[184:187]\n"
      "s_cselect_b32 s77, 0x1000, 0\n"
      "ds_read_b128 a[156:159], v1 offset:12800\n"
      "v_cvt_pk_bf16_f32 v75, v38, v39\n"
      "v_mfma_f32_16x16x32_bf16 a[248:251], a[168:171], v[60:63], a[248:251]\n"
      "s_cmp_eq_u32 s61, 6\n"
      "ds_read_b128 v[20:23], v6 offset:1024\n"
      "v_add_f32 v34, v34, v38\n"
      "v_mfma_f32_16x16x32_bf16 a[252:255], a[172:175], v[60:63], a[252:255]\n"
      "s_cselect_b32 s76, 0x10000000, s76\n"
      "s_waitcnt lgkmcnt(9)\n"
      "buffer_store_dwordx4 v[16:19], v7, s[48:51], s76 offen offset:0 nt\n"
      "v_add_f32 v34, v34, v39\n"
      "v_exp_f32 v40, v40\n"
      "v_mfma_f32_16x16x32_bf16 a[188:191], a[172:175], v[56:59], a[188:191]\n"
      "s_mov_b32 m0, s55\n"
      "v_exp_f32 v41, v41\n"
      "v_mfma_f32_16x16x32_bf16 v[252:255], a[172:175], v[52:55], v[252:255]\n"
      "s_cselect_b32 s77, 0xffffb000, s77\n"
      "v_exp_f32 v42, v42\n"
      "v_mfma_f32_16x16x32_bf16 v[188:191], a[172:175], v[48:51], v[188:191]\n"
      "s_branch LLOOP4a_%=\n"
      "LEX4a_%=:\n"
      "v_fma_f32 v36, v36, s59, v30\n"
      "v_fma_f32 v37, v37, s59, v30\n"
      "v_mfma_f32_16x16x32_bf16 v[40:43], a[148:151], a[116:119], v[40:43]\n"
      "v_fma_f32 v38, v38, s59, v30\n"
      "v_fma_f32 v39, v39, s59, v30\n"
      "v_mfma_f32_16x16x32_bf16 v[40:43], a[144:147], a[112:115], v[40:43]\n"
      "v_exp_f32 v36, v36\n"
      "v_mfma_f32_16x16x32_bf16 v[40:43], a[140:143], a[108:111], v[40:43]\n"
      "v_exp_f32 v37, v37\n"
      "v_mfma_f32_16x16x32_bf16 v[40:43], a[136:139], a[104:107], v[40:43]\n"
      "v_mfma_f32_16x16x32_bf16 v[40:43], a[132:135], a[100:103], v[40:43]\n"
      "v_mfma_f32_16x16x32_bf16 v[40:43], a[128:131], a[96:99], v[40:43]\n"
      "s_waitcnt vmcnt(11) lgkmcnt(0)\n"
      "s_barrier\n"
      "LD4_%=:\n"
      "s_cmp_eq_u32 s62, 0\n"
      "s_cbranch_scc1 LEND4_%=\n"
      "v_mfma_f32_16x16x32_bf16 v[176:179], a[160:163], v[48:51], v[176:179]\n"
      "s_mov_b32 m0, s55\n"
      "ds_read_b128 a[0:3], v12 offset:0\n"
      "v_cvt_pk_bf16_f32 v74, v36, v37\n"
      "v_mfma_f32_16x16x32_bf16 v[240:243], a[160:163], v[52:55], v[240:243]\n"
      "s_and_b32 s79, s63, 0xffffe000\n"
      "ds_read_b128 a[128:131], v1 offset:0\n"
      "v_add_f32 v34, v34, v36\n"
      "v_mfma_f32_16x16x32_bf16 a[176:179], a[160:163], v[56:59], a[176:179]\n"
      "s_add_u32 s48, s82, s79\n"
      "ds_read_b128 a[4:7], v12 offset:512\n"
      "v_add_f32 v34, v34, v37\n"
      "v_mfma_f32_16x16x32_bf16 a[240:243], a[160:163], v[60:63], a[240:243]\n"
      "s_addc_u32 s49, s83, 0\n"
      "ds_read_b128 a[132:135], v1 offset:512\n"
      "v_fma_f32 v40, v40, s59, v31\n"
      "v_mfma_f32_16x16x32_bf16 a[244:247], a[164:167], v[60:63], a[244:247]\n"
      "s_and_b32 s79, s63, 0xf8\n"
      "ds_read_b128 a[8:11], v12 offset:2048\n"
      "v_fma_f32 v41, v41, s59, v31\n"
      "v_mfma_f32_16x16x32_bf16 a[180:183], a[164:167], v[56:59], a[180:183]\n"
      "s_lshl_b32 s50, s79, 10\n"
      "ds_read_b128 a[136:139], v1 offset:4096\n"
      "v_fma_f32 v42, v42, s59, v31\n"
      "v_mfma_f32_16x16x32_bf16 v[244:247], a[164:167], v[52:55], v[244:247]\n"
      "s_and_b32 s79, s63, 7\n"
      "ds_read_b128 a[12:15], v12 offset:2560\n"
      "v_fma_f32 v43, v43, s59, v31\n"
      "v_mfma_f32_16x16x32_bf16 v[180:183], a[164:167], v[48:51], v[180:183]\n"
      "s_mul_i32 s79, s79, s93\n"
      "ds_read_b128 a[140:143], v1 offset:4608\n"
      "v_mov_b32 v24, v32\n"
      "v_mfma_f32_16x16x32_bf16 v[184:187], a[168:171], v[48:51], v[184:187]\n"
      "s_lshl_b32 s91, s79, 13\n"
      "ds_read_b128 a[16:19], v12 offset:4096\n"
      "v_mov_b32 v26, v33\n"
      "v_mfma_f32_16x16x32_bf16 v[248:251], a[168:171], v[52:55], v[248:251]\n"
      "s_and_b32 s79, s65, 0xffffe000\n"
      "ds_read_b128 a[144:147], v1 offset:8192\n"
      "v_exp_f32 v38, v38\n"
      "v_mfma_f32_16x16x32_bf16 a[184:187], a[168:171], v[56:59], a[184:187]\n"
      "s_add_u32 s44, s80, s79\n"
      "ds_read_b128 a[20:23], v12 offset:4608\n"
      "v_add_f32 v34, v34, v38\n"
      "v_mfma_f32_16x16x32_bf16 a[248:251], a[168:171], v[60:63], a[248:251]\n"
      "s_addc_u32 s45, s81, 0\n"
      "ds_read_b128 a[148:151], v1 offset:8704\n"
      "v_mfma_f32_16x16x32_bf16 a[252:255], a[172:175], v[60:63], a[252:255]\n"
      "s_and_b32 s79, s65, 0xf8\n"
      "ds_read_b128 a[24:27], v12 offset:6144\n"
      "v_exp_f32 v39, v39\n"
      "v_mfma_f32_16x16x32_bf16 a[188:191], a[172:175], v[56:59], a[188:191]\n"
      "s_lshl_b32 s46, s79, 10\n"
      "ds_read_b128 a[152:155], v1 offset:12288\n"
      "v_cvt_pk_bf16_f32 v75, v38, v39\n"
      "v_mfma_f32_16x16x32_bf16 v[252:255], a[172:175], v[52:55], v[252:255]\n"
      "s_and_b32 s79, s65, 7\n"
      "ds_read_b128 a[28:31], v12 offset:6656\n"
      "ds_read_b128 a[156:159], v1 offset:12800\n"
      "v_mfma_f32_16x16x32_bf16 v[188:191], a[172:175], v[48:51], v[188:191]\n"
      "s_waitcnt lgkmcnt(14)\n"
      "ds_read_b128 a[60:63], v12 offset:14848\n"
      "v_add_f32 v34, v34, v39\n"
      "v_mfma_f32_16x16x32_bf16 v[36:39], a[128:131], a[0:3], 0\n"
      "s_waitcnt lgkmcnt(13)\n"
      "ds_read_b128 a[56:59], v12 offset:14336\n"
      "buffer_load_dwordx4 v4, s[36:39], s52 offen offset:2048 sc1 lds\n"
      "v_mfma_f32_16x16x32_bf16 v[36:39], a[132:135], a[4:7], v[36:39]\n"
      "s_waitcnt lgkmcnt(12)\n"
      "ds_read_b128 a[52:55], v12 offset:12800\n"
      "v_exp_f32 v40, v40\n"
      "v_mov_b32 v46, v34\n"
      "v_mfma_f32_16x16x32_bf16 v[36:39], a[136:139], a[8:11], v[36:39]\n"
      "s_waitcnt lgkmcnt(11)\n"
      "ds_read_b128 a[48:51], v12 offset:12288\n"
      "v_exp_f32 v41, v41\n"
      "v_add_f32 v35, v35, v40\n"
      "v_mfma_f32_16x16x32_bf16 v[36:39], a[140:143], a[12:15], v[36:39]\n"
      "s_waitcnt lgkmcnt(10)\n"
      "ds_read_b128 a[44:47], v12 offset:10752\n"
      "v_exp_f32 v42, v42\n"
      "v_cvt_pk_bf16_f32 v78, v40, v41\n"
      "v_mfma_f32_16x16x32_bf16 v[36:39], a[144:147], a[16:19], v[36:39]\n"
      "s_waitcnt lgkmcnt(9)\n"
      "ds_read_b128 a[40:43], v12 offset:10240\n"
      "buffer_load_dwordx4 v4, s[36:39], s52 offen offset:3072 sc1 lds\n"
      "v_mfma_f32_16x16x32_bf16 v[36:39], a[148:151], a[20:23], v[36:39]\n"
      "s_waitcnt lgkmcnt(8)\n"
      "ds_read_b128 a[36:39], v12 offset:8704\n"
      "v_exp_f32 v43, v43\n"
      "v_add_f32 v35, v35, v41\n"
      "v_mfma_f32_16x16x32_bf16 v[36:39], a[152:155], a[24:27], v[36:39]\n"
      "s_waitcnt lgkmcnt(7)\n"
      "ds_read_b128 a[32:35], v12 offset:8192\n"
      "v_cvt_pk_bf16_f32 v79, v42, v43\n"
      "v_add_f32 v35, v35, v42\n"
      "v_mfma_f32_16x16x32_bf16 v[36:39], a[156:159], a[28:31], v[36:39]\n"
      "s_waitcnt lgkmcnt(7)\n"
      "ds_read_b64_tr_b16 a[160:161], v2 offset:49152\n"
      "v_add_f32 v35, v35, v43\n"
      "ds_read_b64_tr_b16 a[162:163], v2 offset:51200\n"
      "v_mfma_f32_16x16x32_bf16 v[40:43], a[156:159], a[60:63], 0\n"
      "s_waitcnt lgkmcnt(8)\n"
      "s_mul_i32 s79, s79, s93\n"
      "ds_read_b64_tr_b16 a[164:165], v3 offset:49152\n"
      "v_mfma_f32_16x16x32_bf16 v[40:43], a[152:155], a[56:59], v[40:43]\n"
      "s_waitcnt lgkmcnt(8)\n"
      "s_lshl_b32 s92, s79, 13\n"
      "ds_read_b64_tr_b16 a[166:167], v3 offset:51200\n"
      "v_mfma_f32_16x16x32_bf16 v[40:43], a[148:151], a[52:55], v[40:43]\n"
      "s_waitcnt lgkmcnt(8)\n"
      "s_add_u32 s95, s91, 0x6000\n"
      "ds_read_b64_tr_b16 a[168:169], v2 offset:49664\n"
      "v_mfma_f32_16x16x32_bf16 v[40:43], a[144:147], a[48:51], v[40:43]\n"
      "s_waitcnt lgkmcnt(8)\n"
      "s_sub_u32 s62, s62, 1\n"
      "ds_read_b64_tr_b16 a[170:171], v2 offset:51712\n"
      "v_mfma_f32_16x16x32_bf16 v[40:43], a[140:143], a[44:47], v[40:43]\n"
      "s_waitcnt lgkmcnt(8)\n"
      "s_add_u32 m0, s56, 0x2000\n"
      "ds_read_b64_tr_b16 a[172:173], v3 offset:49664\n"
      "buffer_load_dwordx4 v4, s[40:43], s90 offen offset:0 sc1 lds\n"
      "v_mfma_f32_16x16x32_bf16 v[40:43], a[136:139], a[40:43], v[40:43]\n"
      "s_waitcnt lgkmcnt(8)\n"
      "buffer_load_dwordx4 v4, s[40:43], s90 offen offset:1024 sc1 lds\n"
      "ds_read_b64_tr_b16 a[174:175], v3 offset:51712\n"
      "v_mfma_f32_16x16x32_bf16 v[40:43], a[132:135], a[36:39], v[40:43]\n"
      "s_waitcnt lgkmcnt(8)\n"
      "ds_read_b128 a[64:67], v12 offset:16384\n"
      "v_max3_f32 v28, v36, v37, v38\n"
      "v_mfma_f32_16x16x32_bf16 v[40:43], a[128:131], a[32:35], v[40:43]\n"
      "s_waitcnt lgkmcnt(7)\n"
      "ds_read_b128 a[68:71], v12 offset:16896\n"
      "v_max_f32 v28, v28, v39\n"
      "v_mfma_f32_16x16x32_bf16 v[128:131], a[160:163], v[64:67], v[128:131]\n"
      "s_mov_b32 s63, s64\n"
      "ds_read_b128 a[72:75], v12 offset:18432\n"
      "v_mov_b32 v0, v28\n"
      "v_mfma_f32_16x16x32_bf16 v[192:195], a[160:163], v[68:71], v[192:195]\n"
      "s_mov_b32 s64, s65\n"
      "ds_read_b128 a[76:79], v12 offset:18944\n"
      "v_max3_f32 v29, v40, v41, v42\n"
      "v_mfma_f32_16x16x32_bf16 v[80:83], a[160:163], v[72:75], v[80:83]\n"
      "s_mov_b32 s65, s66\n"
      "v_permlane32_swap_b32 v32, v24\n"
      "v_mfma_f32_16x16x32_bf16 a[192:195], a[160:163], v[76:79], a[192:195]\n"
      "s_waitcnt lgkmcnt(8)\n"
      "v_permlane32_swap_b32 v28, v0\n"
      "v_mfma_f32_16x16x32_bf16 a[196:199], a[164:167], v[76:79], a[196:199]\n"
      "s_mov_b32 s66, 0\n"
      "ds_read_b128 a[80:83], v12 offset:20480\n"
      "v_add_f32 v32, v32, v24\n"
      "v_mfma_f32_16x16x32_bf16 v[84:87], a[164:167], v[72:75], v[84:87]\n"
      "s_mov_b32 s61, 0\n"
      "ds_read_b128 a[84:87], v12 offset:20992\n"
      "v_mov_b32 v24, v32\n"
      "v_mfma_f32_16x16x32_bf16 v[196:199], a[164:167], v[68:71], v[196:199]\n"
      "s_mov_b32 s96, 0x30000\n"
      "ds_read_b128 a[88:91], v12 offset:22528\n"
      "v_max_f32 v28, v28, v0\n"
      "v_mfma_f32_16x16x32_bf16 v[132:135], a[164:167], v[64:67], v[132:135]\n"
      "s_waitcnt lgkmcnt(9)\n"
      "ds_read_b128 a[92:95], v12 offset:23040\n"
      "v_mov_b32 v0, v28\n"
      "v_mfma_f32_16x16x32_bf16 v[136:139], a[168:171], v[64:67], v[136:139]\n"
      "s_mov_b32 s97, 0x10000000\n"
      "v_permlane16_swap_b32 v32, v24\n"
      "v_add_f32 v32, v32, v24\n"
      "v_mfma_f32_16x16x32_bf16 v[200:203], a[168:171], v[68:71], v[200:203]\n"
      "v_permlane16_swap_b32 v28, v0\n"
      "v_max_f32 v28, v28, v0\n"
      "v_mul_f32 v28, s69, v28\n"
      "v_mfma_f32_16x16x32_bf16 v[88:91], a[168:171], v[72:75], v[88:91]\n"
      "ds_read_b64_tr_b16 a[160:161], v2 offset:53248\n"
      "v_fma_f32 v36, v36, s59, v28\n"
      "v_fma_f32 v37, v37, s59, v28\n"
      "v_fma_f32 v38, v38, s59, v28\n"
      "v_mfma_f32_16x16x32_bf16 a[200:203], a[168:171], v[76:79], a[200:203]\n"
      "s_waitcnt lgkmcnt(9)\n"
      "ds_read_b64_tr_b16 a[162:163], v2 offset:55296\n"
      "v_fma_f32 v39, v39, s59, v28\n"
      "v_rcp_f32 v24, v32\n"
      "v_mfma_f32_16x16x32_bf16 a[204:207], a[172:175], v[76:79], a[204:207]\n"
      "v_exp_f32 v36, v36\n"
      "v_exp_f32 v37, v37\n"
      "v_mfma_f32_16x16x32_bf16 v[92:95], a[172:175], v[72:75], v[92:95]\n"
      "v_exp_f32 v38, v38\n"
      "v_exp_f32 v39, v39\n"
      "v_mfma_f32_16x16x32_bf16 v[204:207], a[172:175], v[68:71], v[204:207]\n"
      "ds_read_b64_tr_b16 a[164:165], v3 offset:53248\n"
      "v_cmp_nle_f32 s[72:73], v32, s60\n"
      "v_cvt_pk_bf16_f32 v48, v36, v37\n"
      "v_mfma_f32_16x16x32_bf16 v[140:143], a[172:175], v[64:67], v[140:143]\n"
      "s_waitcnt lgkmcnt(10)\n"
      "ds_read_b64_tr_b16 a[166:167], v3 offset:55296\n"
      "v_add_f32 v32, v36, v37\n"
      "v_cvt_pk_bf16_f32 v49, v38, v39\n"
      "v_add_f32 v32, v32, v38\n"
      "v_add_f32 v32, v32, v39\n"
      "v_mfma_f32_16x16x32_bf16 v[36:39], a[128:131], a[64:67], 0\n"
      "s_waitcnt lgkmcnt(10)\n"
      "ds_read_b64_tr_b16 a[168:169], v2 offset:53760\n"
      "v_max_f32 v29, v29, v43\n"
      "v_mfma_f32_16x16x32_bf16 v[36:39], a[132:135], a[68:71], v[36:39]\n"
      "s_waitcnt lgkmcnt(10)\n"
      "ds_read_b64_tr_b16 a[170:171], v2 offset:55808\n"
      "v_mov_b32 v0, v29\n"
      "v_mfma_f32_16x16x32_bf16 v[36:39], a[136:139], a[72:75], v[36:39]\n"
      "s_waitcnt lgkmcnt(10)\n"
      "ds_read_b64_tr_b16 a[172:173], v3 offset:53760\n"
      "ds_read_b64_tr_b16 a[174:175], v3 offset:55808\n"
      "v_mfma_f32_16x16x32_bf16 v[36:39], a[140:143], a[76:79], v[36:39]\n"
      "s_waitcnt lgkmcnt(11)\n"
      "v_permlane32_swap_b32 v33, v26\n"
      "v_mfma_f32_16x16x32_bf16 v[36:39], a[144:147], a[80:83], v[36:39]\n"
      "s_waitcnt lgkmcnt(10)\n"
      "v_permlane32_swap_b32 v29, v0\n"
      "v_mfma_f32_16x16x32_bf16 v[36:39], a[148:151], a[84:87], v[36:39]\n"
      "s_waitcnt lgkmcnt(9)\n"
      "v_add_f32 v33, v33, v26\n"
      "v_mov_b32 v26, v33\n"
      "v_mfma_f32_16x16x32_bf16 v[36:39], a[152:155], a[88:91], v[36:39]\n"
      "s_waitcnt lgkmcnt(8)\n"
      "v_max_f32 v29, v29, v0\n"
      "v_mov_b32 v0, v29\n"
      "v_mfma_f32_16x16x32_bf16 v[36:39], a[156:159], a[92:95], v[36:39]\n"
      "s_or_b64 s[70:71], s[70:71], s[72:73]\n"
      "s_waitcnt vmcnt(10) lgkmcnt(0)\n"
      "s_barrier\n"
      "v_mfma_f32_16x16x32_bf16 v[144:147], a[160:163], v[64:67], v[144:147]\n"
      "s_mov_b32 m0, s54\n"
      "ds_read_b128 a[128:131], v1 offset:2048\n"
      "v_mov_b32 v14, v35\n"
      "v_mfma_f32_16x16x32_bf16 v[208:211], a[160:163], v[68:71], v[208:211]\n"
      "s_nop 0\n"
      "ds_read_b128 a[132:135], v1 offset:2560\n"
      "v_max3_f32 v30, v36, v37, v38\n"
      "v_mfma_f32_16x16x32_bf16 v[96:99], a[160:163], v[72:75], v[96:99]\n"
      "ds_read_b128 a[136:139], v1 offset:6144\n"
      "v_max_f32 v30, v30, v39\n"
      "v_mfma_f32_16x16x32_bf16 a[208:211], a[160:163], v[76:79], a[208:211]\n"
      "ds_read_b128 a[140:143], v1 offset:6656\n"
      "v_mov_b32 v25, v24\n"
      "v_mfma_f32_16x16x32_bf16 a[212:215], a[164:167], v[76:79], a[212:215]\n"
      "ds_read_b128 a[144:147], v1 offset:10240\n"
      "v_permlane16_swap_b32 v33, v26\n"
      "v_permlane16_swap_b32 v29, v0\n"
      "v_pk_mul_f32 v[128:129], v[128:129], v[24:25]\n"
      "v_pk_mul_f32 v[130:131], v[130:131], v[24:25]\n"
      "v_pk_mul_f32 v[132:133], v[132:133], v[24:25]\n"
      "v_pk_mul_f32 v[134:135], v[134:135], v[24:25]\n"
      "v_pk_mul_f32 v[136:137], v[136:137], v[24:25]\n"
      "v_pk_mul_f32 v[138:139], v[138:139], v[24:25]\n"
      "v_pk_mul_f32 v[140:141], v[140:141], v[24:25]\n"
      "v_pk_mul_f32 v[142:143], v[142:143], v[24:25]\n"
      "v_pk_mul_f32 v[144:145], v[144:145], v[24:25]\n"
      "v_pk_mul_f32 v[146:147], v[146:147], v[24:25]\n"
      "v_add_f32 v33, v33, v26\n"
      "v_mfma_f32_16x16x32_bf16 v[100:103], a[164:167], v[72:75], v[100:103]\n"
      "ds_read_b128 a[148:151], v1 offset:10752\n"
      "v_max_f32 v29, v29, v0\n"
      "v_mfma_f32_16x16x32_bf16 v[212:215], a[164:167], v[68:71], v[212:215]\n"
      "v_permlane32_swap_b32 v34, v46\n"
      "v_mfma_f32_16x16x32_bf16 v[148:151], a[164:167], v[64:67], v[148:151]\n"
      "s_nop 0\n"
      "v_permlane32_swap_b32 v35, v14\n"
      "v_mfma_f32_16x16x32_bf16 v[152:155], a[168:171], v[64:67], v[152:155]\n"
      "s_nop 0\n"
      "ds_read_b128 a[152:155], v1 offset:14336\n"
      "v_mul_f32 v29, s69, v29\n"
      "v_fma_f32 v40, v40, s59, v29\n"
      "v_fma_f32 v41, v41, s59, v29\n"
      "v_fma_f32 v42, v42, s59, v29\n"
      "v_pk_mul_f32 v[148:149], v[148:149], v[24:25]\n"
      "v_pk_mul_f32 v[150:151], v[150:151], v[24:25]\n"
      "v_pk_mul_f32 v[152:153], v[152:153], v[24:25]\n"
      "v_pk_mul_f32 v[154:155], v[154:155], v[24:25]\n"
      "v_fma_f32 v43, v43, s59, v29\n"
      "v_mfma_f32_16x16x32_bf16 v[216:219], a[168:171], v[68:71], v[216:219]\n"
      "v_rcp_f32 v26, v33\n"
      "v_add_f32 v34, v34, v46\n"
      "v_mov_b32 v46, v34\n"
      "v_mfma_f32_16x16x32_bf16 v[104:107], a[168:171], v[72:75], v[104:107]\n"
      "s_nop 0\n"
      "ds_read_b128 a[156:159], v1 offset:14848\n"
      "v_add_f32 v35, v35, v14\n"
      "v_mov_b32 v14, v35\n"
      "v_cmp_nle_f32 s[72:73], v33, s60\n"
      "v_mfma_f32_16x16x32_bf16 a[216:219], a[168:171], v[76:79], a[216:219]\n"
      "s_or_b64 s[70:71], s[70:71], s[72:73]\n"
      "v_exp_f32 v40, v40\n"
      "v_exp_f32 v41, v41\n"
      "v_mfma_f32_16x16x32_bf16 a[220:223], a[172:175], v[76:79], a[220:223]\n"
      "v_exp_f32 v42, v42\n"
      "v_exp_f32 v43, v43\n"
      "v_mfma_f32_16x16x32_bf16 v[108:111], a[172:175], v[72:75], v[108:111]\n"
      "v_permlane16_swap_b32 v34, v46\n"
      "v_permlane16_swap_b32 v35, v14\n"
      "v_mfma_f32_16x16x32_bf16 v[220:223], a[172:175], v[68:71], v[220:223]\n"
      "ds_read_b64_tr_b16 a[160:161], v2 offset:57344\n"
      "v_cvt_pk_bf16_f32 v52, v40, v41\n"
      "v_add_f32 v33, v40, v41\n"
      "v_mfma_f32_16x16x32_bf16 v[156:159], a[172:175], v[64:67], v[156:159]\n"
      "s_waitcnt lgkmcnt(8)\n"
      "ds_read_b64_tr_b16 a[162:163], v2 offset:59392\n"
      "v_cvt_pk_bf16_f32 v53, v42, v43\n"
      "v_add_f32 v33, v33, v42\n"
      "v_add_f32 v33, v33, v43\n"
      "v_mfma_f32_16x16x32_bf16 v[40:43], a[128:131], a[0:3], 0\n"
      "s_waitcnt lgkmcnt(8)\n"
      "ds_read_b64_tr_b16 a[164:165], v3 offset:57344\n"
      "v_add_f32 v34, v34, v46\n"
      "v_cmp_nle_f32 s[72:73], v34, s60\n"
      "v_add_f32 v35, v35, v14\n"
      "v_mov_b32 v0, v30\n"
      "v_mfma_f32_16x16x32_bf16 v[40:43], a[132:135], a[4:7], v[40:43]\n"
      "s_waitcnt lgkmcnt(8)\n"
      "s_or_b64 s[70:71], s[70:71], s[72:73]\n"
      "ds_read_b64_tr_b16 a[166:167], v3 offset:59392\n"
      "v_cmp_nle_f32 s[72:73], v35, s60\n"
      "v_permlane32_swap_b32 v30, v0\n"
      "v_mfma_f32_16x16x32_bf16 v[40:43], a[136:139], a[8:11], v[40:43]\n"
      "s_waitcnt lgkmcnt(8)\n"
      "s_or_b64 s[70:71], s[70:71], s[72:73]\n"
      "ds_read_b64_tr_b16 a[168:169], v2 offset:57856\n"
      "v_max_f32 v30, v30, v0\n"
      "v_mov_b32 v0, v30\n"
      "v_mov_b32 v27, v26\n"
      "v_mfma_f32_16x16x32_bf16 v[40:43], a[140:143], a[12:15], v[40:43]\n"
      "s_waitcnt lgkmcnt(8)\n"
      "s_cmp_lg_u64 s[70:71], 0\n"
      "v_permlane16_swap_b32 v30, v0\n"
      "v_max_f32 v30, v30, v0\n"
      "v_mul_f32 v30, s69, v30\n"
      "v_pk_mul_f32 v[192:193], v[192:193], v[26:27]\n"
      "v_pk_mul_f32 v[194:195], v[194:195], v[26:27]\n"
      "v_pk_mul_f32 v[196:197], v[196:197], v[26:27]\n"
      "v_pk_mul_f32 v[198:199], v[198:199], v[26:27]\n"
      "v_pk_mul_f32 v[200:201], v[200:201], v[26:27]\n"
      "v_pk_mul_f32 v[202:203], v[202:203], v[26:27]\n"
      "v_pk_mul_f32 v[204:205], v[204:205], v[26:27]\n"
      "v_pk_mul_f32 v[206:207], v[206:207], v[26:27]\n"
      "v_pk_mul_f32 v[208:209], v[208:209], v[26:27]\n"
      "v_pk_mul_f32 v[210:211], v[210:211], v[26:27]\n"
      "v_pk_mul_f32 v[212:213], v[212:213], v[26:27]\n"
      "v_pk_mul_f32 v[214:215], v[214:215], v[26:27]\n"
      "v_pk_mul_f32 v[216:217], v[216:217], v[26:27]\n"
      "v_pk_mul_f32 v[218:219], v[218:219], v[26:27]\n"
      "v_pk_mul_f32 v[156:157], v[156:157], v[24:25]\n"
      "v_pk_mul_f32 v[158:159], v[158:159], v[24:25]\n"
      "v_pk_mul_f32 v[220:221], v[220:221], v[26:27]\n"
      "v_pk_mul_f32 v[222:223], v[222:223], v[26:27]\n"
      "v_fma_f32 v36, v36, s59, v30\n"
      "v_mfma_f32_16x16x32_bf16 v[40:43], a[144:147], a[16:19], v[40:43]\n"
      "s_waitcnt lgkmcnt(7)\n"
      "s_cselect_b32 s78, s68, 0\n"
      "ds_read_b64_tr_b16 a[170:171], v2 offset:59904\n"
      "v_fma_f32 v37, v37, s59, v30\n"
      "v_fma_f32 v38, v38, s59, v30\n"
      "v_fma_f32 v39, v39, s59, v30\n"
      "v_mfma_f32_16x16x32_bf16 v[40:43], a[148:151], a[20:23], v[40:43]\n"
      "s_waitcnt lgkmcnt(7)\n"
      "s_or_b32 s67, s67, s78\n"
      "v_rcp_f32 v46, v34\n"
      "v_exp_f32 v36, v36\n"
      "v_cvt_pk_bf16_f32 v128, v128, v129\n"
      "v_mfma_f32_16x16x32_bf16 v[40:43], a[152:155], a[24:27], v[40:43]\n"
      "s_waitcnt lgkmcnt(6)\n"
      "s_lshl_b32 s68, s68, 1\n"
      "v_exp_f32 v37, v37\n"
      "v_exp_f32 v38, v38\n"
      "v_exp_f32 v39, v39\n"
      "v_cvt_pk_bf16_f32 v56, v36, v37\n"
      "v_mfma_f32_16x16x32_bf16 v[40:43], a[156:159], a[28:31], v[40:43]\n"
      "s_add_u32 s52, s52, 0x4000\n"
      "buffer_load_dwordx4 v4, s[36:39], s52 offen offset:0 sc1 lds\n"
      "buffer_load_dwordx4 v4, s[36:39], s52 offen offset:1024 sc1 lds\n"
      "ds_read_b64_tr_b16 a[172:173], v3 offset:57856\n"
      "v_add_f32 v34, v36, v37\n"
      "v_cvt_pk_bf16_f32 v57, v38, v39\n"
      "v_add_f32 v34, v34, v38\n"
      "v_add_f32 v34, v34, v39\n"
      "v_mfma_f32_16x16x32_bf16 v[36:39], a[156:159], a[60:63], 0\n"
      "s_add_u32 s53, s53, 0x4000\n"
      "s_mov_b32 m0, s57\n"
      "ds_read_b64_tr_b16 a[174:175], v3 offset:59904\n"
      "v_mfma_f32_16x16x32_bf16 v[36:39], a[152:155], a[56:59], v[36:39]\n"
      "s_add_u32 s90, s90, 0x4000\n"
      "buffer_load_dwordx4 v4, s[40:43], s53 offen offset:0 sc1 lds\n"
      "v_mfma_f32_16x16x32_bf16 v[36:39], a[148:151], a[52:55], v[36:39]\n"
      "s_mov_b64 s[70:71], 0\n"
      "v_fma_f32 v40, v40, s59, v28\n"
      "v_fma_f32 v41, v41, s59, v28\n"
      "v_mfma_f32_16x16x32_bf16 v[36:39], a[144:147], a[48:51], v[36:39]\n"
      "v_fma_f32 v42, v42, s59, v28\n"
      "v_fma_f32 v43, v43, s59, v28\n"
      "v_mfma_f32_16x16x32_bf16 v[36:39], a[140:143], a[44:47], v[36:39]\n"
      "buffer_load_dwordx4 v4, s[40:43], s53 offen offset:1024 sc1 lds\n"
      "v_mfma_f32_16x16x32_bf16 v[36:39], a[136:139], a[40:43], v[36:39]\n"
      "v_exp_f32 v40, v40\n"
      "v_mfma_f32_16x16x32_bf16 v[36:39], a[132:135], a[36:39], v[36:39]\n"
      "v_exp_f32 v41, v41\n"
      "v_mfma_f32_16x16x32_bf16 v[36:39], a[128:131], a[32:35], v[36:39]\n"
      "s_waitcnt lgkmcnt(6)\n"
      "v_exp_f32 v42, v42\n"
      "v_mfma_f32_16x16x32_bf16 v[160:163], a[160:163], v[64:67], v[160:163]\n"
      "s_nop 0\n"
      "v_exp_f32 v43, v43\n"
      "v_mfma_f32_16x16x32_bf16 v[224:227], a[160:163], v[68:71], v[224:227]\n"
      "s_nop 0\n"
      "v_cvt_pk_bf16_f32 v50, v40, v41\n"
      "v_add_f32 v32, v32, v40\n"
      "v_add_f32 v32, v32, v41\n"
      "v_cvt_pk_bf16_f32 v51, v42, v43\n"
      "v_add_f32 v32, v32, v42\n"
      "v_pk_mul_f32 v[160:161], v[160:161], v[24:25]\n"
      "v_pk_mul_f32 v[162:163], v[162:163], v[24:25]\n"
      "v_pk_mul_f32 v[224:225], v[224:225], v[26:27]\n"
      "v_pk_mul_f32 v[226:227], v[226:227], v[26:27]\n"
      "v_add_f32 v32, v32, v43\n"
      "v_mfma_f32_16x16x32_bf16 v[112:115], a[160:163], v[72:75], v[112:115]\n"
      "v_fma_f32 v36, v36, s59, v29\n"
      "v_fma_f32 v37, v37, s59, v29\n"
      "v_mfma_f32_16x16x32_bf16 a[224:227], a[160:163], v[76:79], a[224:227]\n"
      "s_waitcnt lgkmcnt(4)\n"
      "ds_read_b64_tr_b16 a[160:161], v2 offset:61440\n"
      "v_fma_f32 v38, v38, s59, v29\n"
      "v_mfma_f32_16x16x32_bf16 a[228:231], a[164:167], v[76:79], a[228:231]\n"
      "ds_read_b64_tr_b16 a[162:163], v2 offset:63488\n"
      "v_fma_f32 v39, v39, s59, v29\n"
      "v_mfma_f32_16x16x32_bf16 v[116:119], a[164:167], v[72:75], v[116:119]\n"
      "v_exp_f32 v36, v36\n"
      "v_mfma_f32_16x16x32_bf16 v[228:231], a[164:167], v[68:71], v[228:231]\n"
      "s_nop 0\n"
      "v_exp_f32 v37, v37\n"
      "v_mfma_f32_16x16x32_bf16 v[164:167], a[164:167], v[64:67], v[164:167]\n"
      "s_waitcnt lgkmcnt(4)\n"
      "ds_read_b64_tr_b16 a[164:165], v3 offset:61440\n"
      "v_exp_f32 v38, v38\n"
      "v_exp_f32 v39, v39\n"
      "v_cvt_pk_bf16_f32 v54, v36, v37\n"
      "v_pk_mul_f32 v[228:229], v[228:229], v[26:27]\n"
      "v_pk_mul_f32 v[230:231], v[230:231], v[26:27]\n"
      "v_add_f32 v33, v33, v36\n"
      "v_pk_mul_f32 v[164:165], v[164:165], v[24:25]\n"
      "v_pk_mul_f32 v[166:167], v[166:167], v[24:25]\n"
      "v_add_f32 v33, v33, v37\n"
      "v_mfma_f32_16x16x32_bf16 v[168:171], a[168:171], v[64:67], v[168:171]\n"
      "s_nop 0\n"
      "ds_read_b64_tr_b16 a[166:167], v3 offset:63488\n"
      "v_cvt_pk_bf16_f32 v55, v38, v39\n"
      "v_mfma_f32_16x16x32_bf16 v[232:235], a[168:171], v[68:71], v[232:235]\n"
      "s_nop 0\n"
      "v_add_f32 v33, v33, v38\n"
      "v_add_f32 v33, v33, v39\n"
      "v_cvt_pk_bf16_f32 v129, v130, v131\n"
      "v_cvt_pk_bf16_f32 v192, v192, v193\n"
      "v_cvt_pk_bf16_f32 v193, v194, v195\n"
      "v_pk_mul_f32 v[168:169], v[168:169], v[24:25]\n"
      "v_pk_mul_f32 v[170:171], v[170:171], v[24:25]\n"
      "v_pk_mul_f32 v[232:233], v[232:233], v[26:27]\n"
      "v_pk_mul_f32 v[234:235], v[234:235], v[26:27]\n"
      "v_mov_b32 v47, v46\n"
      "v_pk_mul_f32 v[80:81], v[80:81], v[46:47]\n"
      "v_pk_mul_f32 v[82:83], v[82:83], v[46:47]\n"
      "v_pk_mul_f32 v[96:97], v[96:97], v[46:47]\n"
      "v_pk_mul_f32 v[98:99], v[98:99], v[46:47]\n"
      "v_pk_mul_f32 v[84:85], v[84:85], v[46:47]\n"
      "v_pk_mul_f32 v[86:87], v[86:87], v[46:47]\n"
      "v_pk_mul_f32 v[100:101], v[100:101], v[46:47]\n"
      "v_pk_mul_f32 v[102:103], v[102:103], v[46:47]\n"
      "v_pk_mul_f32 v[88:89], v[88:89], v[46:47]\n"
      "v_pk_mul_f32 v[90:91], v[90:91], v[46:47]\n"
      "v_pk_mul_f32 v[104:105], v[104:105], v[46:47]\n"
      "v_pk_mul_f32 v[106:107], v[106:107], v[46:47]\n"
      "v_pk_mul_f32 v[92:93], v[92:93], v[46:47]\n"
      "v_pk_mul_f32 v[94:95], v[94:95], v[46:47]\n"
      "v_pk_mul_f32 v[108:109], v[108:109], v[46:47]\n"
      "v_pk_mul_f32 v[110:111], v[110:111], v[46:47]\n"
      "v_pk_mul_f32 v[112:113], v[112:113], v[46:47]\n"
      "v_pk_mul_f32 v[114:115], v[114:115], v[46:47]\n"
      "v_pk_mul_f32 v[116:117], v[116:117], v[46:47]\n"
      "v_pk_mul_f32 v[118:119], v[118:119], v[46:47]\n"
      "v_cvt_pk_bf16_f32 v80, v80, v81\n"
      "v_mfma_f32_16x16x32_bf16 v[120:123], a[168:171], v[72:75], v[120:123]\n"
      "s_nop 0\n"
      "ds_write2st64_b64 v8, v[128:129], v[192:193] offset0:0 offset1:16\n"
      "v_mfma_f32_16x16x32_bf16 a[232:235], a[168:171], v[76:79], a[232:235]\n"
      "s_waitcnt lgkmcnt(5)\n"
      "ds_read_b64_tr_b16 a[168:169], v2 offset:61952\n"
      "v_cvt_pk_bf16_f32 v81, v82, v83\n"
      "v_mfma_f32_16x16x32_bf16 a[236:239], a[172:175], v[76:79], a[236:239]\n"
      "s_nop 0\n"
      "ds_read_b64_tr_b16 a[170:171], v2 offset:64000\n"
      "v_cvt_pk_bf16_f32 v96, v96, v97\n"
      "v_mfma_f32_16x16x32_bf16 v[124:127], a[172:175], v[72:75], v[124:127]\n"
      "s_nop 0\n"
      "v_cvt_pk_bf16_f32 v97, v98, v99\n"
      "v_cvt_pk_bf16_f32 v84, v84, v85\n"
      "v_cvt_pk_bf16_f32 v85, v86, v87\n"
      "v_cvt_pk_bf16_f32 v100, v100, v101\n"
      "v_cvt_pk_bf16_f32 v101, v102, v103\n"
      "v_pk_mul_f32 v[120:121], v[120:121], v[46:47]\n"
      "v_pk_mul_f32 v[122:123], v[122:123], v[46:47]\n"
      "v_pk_mul_f32 v[124:125], v[124:125], v[46:47]\n"
      "v_pk_mul_f32 v[126:127], v[126:127], v[46:47]\n"
      "v_cvt_pk_bf16_f32 v132, v132, v133\n"
      "v_mfma_f32_16x16x32_bf16 v[236:239], a[172:175], v[68:71], v[236:239]\n"
      "s_nop 0\n"
      "ds_write2st64_b64 v8, v[80:81], v[96:97] offset0:32 offset1:34\n"
      "v_mfma_f32_16x16x32_bf16 v[172:175], a[172:175], v[64:67], v[172:175]\n"
      "s_nop 0\n"
      "ds_write2st64_b64 v9, v[84:85], v[100:101] offset0:32 offset1:34\n"
      "ds_read_b64_tr_b16 a[172:173], v3 offset:61952\n"
      "v_mfma_f32_16x16x32_bf16 v[40:43], a[128:131], a[64:67], 0\n"
      "s_nop 0\n"
      "ds_read_b64_tr_b16 a[174:175], v3 offset:64000\n"
      "v_cvt_pk_bf16_f32 v133, v134, v135\n"
      "v_cvt_pk_bf16_f32 v196, v196, v197\n"
      "v_cvt_pk_bf16_f32 v197, v198, v199\n"
      "v_cvt_pk_bf16_f32 v136, v136, v137\n"
      "v_pk_mul_f32 v[172:173], v[172:173], v[24:25]\n"
      "v_pk_mul_f32 v[174:175], v[174:175], v[24:25]\n"
      "v_pk_mul_f32 v[236:237], v[236:237], v[26:27]\n"
      "v_pk_mul_f32 v[238:239], v[238:239], v[26:27]\n"
      "v_cvt_pk_bf16_f32 v137, v138, v139\n"
      "v_mfma_f32_16x16x32_bf16 v[40:43], a[132:135], a[68:71], v[40:43]\n"
      "ds_write2st64_b64 v9, v[132:133], v[196:197] offset0:0 offset1:16\n"
      "v_mfma_f32_16x16x32_bf16 v[40:43], a[136:139], a[72:75], v[40:43]\n"
      "v_cvt_pk_bf16_f32 v200, v200, v201\n"
      "v_cvt_pk_bf16_f32 v201, v202, v203\n"
      "v_mfma_f32_16x16x32_bf16 v[40:43], a[140:143], a[76:79], v[40:43]\n"
      "ds_write2st64_b64 v10, v[136:137], v[200:201] offset0:0 offset1:16\n"
      "v_mfma_f32_16x16x32_bf16 v[40:43], a[144:147], a[80:83], v[40:43]\n"
      "v_cvt_pk_bf16_f32 v88, v88, v89\n"
      "v_cvt_pk_bf16_f32 v89, v90, v91\n"
      "v_mfma_f32_16x16x32_bf16 v[40:43], a[148:151], a[84:87], v[40:43]\n"
      "v_cvt_pk_bf16_f32 v104, v104, v105\n"
      "v_cvt_pk_bf16_f32 v105, v106, v107\n"
      "v_mfma_f32_16x16x32_bf16 v[40:43], a[152:155], a[88:91], v[40:43]\n"
      "v_cvt_pk_bf16_f32 v92, v92, v93\n"
      "v_cvt_pk_bf16_f32 v93, v94, v95\n"
      "v_mfma_f32_16x16x32_bf16 v[40:43], a[156:159], a[92:95], v[40:43]\n"
      "s_waitcnt vmcnt(9) lgkmcnt(0)\n"
      "s_barrier\n"
      "v_mfma_f32_16x16x32_bf16 v[176:179], a[160:163], v[64:67], v[176:179]\n"
      "s_mov_b32 m0, s54\n"
      "ds_read_b128 a[128:131], v1 offset:16384\n"
      "v_cvt_pk_bf16_f32 v108, v108, v109\n"
      "v_mfma_f32_16x16x32_bf16 v[240:243], a[160:163], v[68:71], v[240:243]\n"
      "s_nop 0\n"
      "ds_write2st64_b64 v10, v[88:89], v[104:105] offset0:32 offset1:34\n"
      "ds_read_b128 a[132:135], v1 offset:16896\n"
      "v_mfma_f32_16x16x32_bf16 a[176:179], a[160:163], v[72:75], a[176:179]\n"
      "s_nop 0\n"
      "ds_read_b128 a[136:139], v1 offset:20480\n"
      "v_fma_f32 v40, v40, s59, v30\n"
      "v_fma_f32 v41, v41, s59, v30\n"
      "v_fma_f32 v42, v42, s59, v30\n"
      "v_fma_f32 v43, v43, s59, v30\n"
      "v_pk_mul_f32 v[176:177], v[176:177], v[24:25]\n"
      "v_pk_mul_f32 v[178:179], v[178:179], v[24:25]\n"
      "v_pk_mul_f32 v[240:241], v[240:241], v[26:27]\n"
      "v_pk_mul_f32 v[242:243], v[242:243], v[26:27]\n"
      "v_cvt_pk_bf16_f32 v109, v110, v111\n"
      "v_mfma_f32_16x16x32_bf16 a[240:243], a[160:163], v[76:79], a[240:243]\n"
      "ds_read_b128 a[140:143], v1 offset:20992\n"
      "v_cvt_pk_bf16_f32 v140, v140, v141\n"
      "v_mfma_f32_16x16x32_bf16 a[244:247], a[164:167], v[76:79], a[244:247]\n"
      "ds_read_b128 a[144:147], v1 offset:24576\n"
      "v_cvt_pk_bf16_f32 v141, v142, v143\n"
      "v_mfma_f32_16x16x32_bf16 a[180:183], a[164:167], v[72:75], a[180:183]\n"
      "ds_read_b128 a[148:151], v1 offset:25088\n"
      "v_cvt_pk_bf16_f32 v204, v204, v205\n"
      "v_mfma_f32_16x16x32_bf16 v[244:247], a[164:167], v[68:71], v[244:247]\n"
      "s_nop 0\n"
      "ds_read_b128 a[152:155], v1 offset:28672\n"
      "v_cvt_pk_bf16_f32 v205, v206, v207\n"
      "v_mfma_f32_16x16x32_bf16 v[180:183], a[164:167], v[64:67], v[180:183]\n"
      "s_nop 0\n"
      "ds_write2st64_b64 v11, v[92:93], v[108:109] offset0:32 offset1:34\n"
      "ds_read_b128 a[156:159], v1 offset:29184\n"
      "v_mfma_f32_16x16x32_bf16 v[184:187], a[168:171], v[64:67], v[184:187]\n"
      "s_nop 0\n"
      "ds_write2st64_b64 v11, v[140:141], v[204:205] offset0:0 offset1:16\n"
      "v_pk_mul_f32 v[244:245], v[244:245], v[26:27]\n"
      "v_pk_mul_f32 v[246:247], v[246:247], v[26:27]\n"
      "v_pk_mul_f32 v[180:181], v[180:181], v[24:25]\n"
      "v_pk_mul_f32 v[182:183], v[182:183], v[24:25]\n"
      "v_cvt_pk_bf16_f32 v144, v144, v145\n"
      "v_mfma_f32_16x16x32_bf16 v[248:251], a[168:171], v[68:71], v[248:251]\n"
      "s_nop 0\n"
      "v_exp_f32 v40, v40\n"
      "v_exp_f32 v41, v41\n"
      "v_exp_f32 v42, v42\n"
      "v_pk_mul_f32 v[184:185], v[184:185], v[24:25]\n"
      "v_pk_mul_f32 v[186:187], v[186:187], v[24:25]\n"
      "v_cvt_pk_bf16_f32 v58, v40, v41\n"
      "v_mfma_f32_16x16x32_bf16 a[184:187], a[168:171], v[72:75], a[184:187]\n"
      "v_exp_f32 v43, v43\n"
      "v_mfma_f32_16x16x32_bf16 a[248:251], a[168:171], v[76:79], a[248:251]\n"
      "ds_read_b64_tr_b16 a[160:161], v2 offset:32768\n"
      "v_add_f32 v34, v34, v40\n"
      "v_mfma_f32_16x16x32_bf16 a[252:255], a[172:175], v[76:79], a[252:255]\n"
      "ds_read_b64_tr_b16 a[162:163], v2 offset:34816\n"
      "v_add_f32 v34, v34, v41\n"
      "v_mfma_f32_16x16x32_bf16 a[188:191], a[172:175], v[72:75], a[188:191]\n"
      "ds_read_b64_tr_b16 a[164:165], v3 offset:32768\n"
      "v_cvt_pk_bf16_f32 v59, v42, v43\n"
      "v_mfma_f32_16x16x32_bf16 v[252:255], a[172:175], v[68:71], v[252:255]\n"
      "s_nop 0\n"
      "ds_read_b64_tr_b16 a[166:167], v3 offset:34816\n"
      "v_add_f32 v34, v34, v42\n"
      "v_add_f32 v34, v34, v43\n"
      "v_cvt_pk_bf16_f32 v145, v146, v147\n"
      "v_cvt_pk_bf16_f32 v208, v208, v209\n"
      "v_pk_mul_f32 v[248:249], v[248:249], v[26:27]\n"
      "v_pk_mul_f32 v[250:251], v[250:251], v[26:27]\n"
      "v_pk_mul_f32 v[252:253], v[252:253], v[26:27]\n"
      "v_pk_mul_f32 v[254:255], v[254:255], v[26:27]\n"
      "v_cvt_pk_bf16_f32 v209, v210, v211\n"
      "v_mfma_f32_16x16x32_bf16 v[188:191], a[172:175], v[64:67], v[188:191]\n"
      "s_waitcnt lgkmcnt(14)\n"
      "ds_read_b64_tr_b16 a[168:169], v2 offset:33280\n"
      "v_cvt_pk_bf16_f32 v148, v148, v149\n"
      "v_mfma_f32_16x16x32_bf16 v[36:39], a[128:131], a[0:3], 0\n"
      "s_waitcnt lgkmcnt(13)\n"
      "buffer_load_dwordx4 v4, s[36:39], s52 offen offset:2048 sc1 lds\n"
      "ds_read_b64_tr_b16 a[170:171], v2 offset:35328\n"
      "v_mfma_f32_16x16x32_bf16 v[36:39], a[132:135], a[4:7], v[36:39]\n"
      "s_waitcnt lgkmcnt(13)\n"
      "ds_read_b64_tr_b16 a[172:173], v3 offset:33280\n"
      "v_cvt_pk_bf16_f32 v149, v150, v151\n"
      "v_mfma_f32_16x16x32_bf16 v[36:39], a[136:139], a[8:11], v[36:39]\n"
      "s_waitcnt lgkmcnt(13)\n"
      "ds_read_b64_tr_b16 a[174:175], v3 offset:35328\n"
      "v_cvt_pk_bf16_f32 v212, v212, v213\n"
      "v_mfma_f32_16x16x32_bf16 v[36:39], a[140:143], a[12:15], v[36:39]\n"
      "s_waitcnt lgkmcnt(13)\n"
      "ds_write2st64_b64 v8, v[144:145], v[208:209] offset0:2 offset1:18\n"
      "v_mfma_f32_16x16x32_bf16 v[36:39], a[144:147], a[16:19], v[36:39]\n"
      "s_waitcnt lgkmcnt(13)\n"
      "buffer_load_dwordx4 v4, s[36:39], s52 offen offset:3072 sc1 lds\n"
      "v_mfma_f32_16x16x32_bf16 v[36:39], a[148:151], a[20:23], v[36:39]\n"
      "s_waitcnt lgkmcnt(12)\n"
      "v_cvt_pk_bf16_f32 v213, v214, v215\n"
      "v_cvt_pk_bf16_f32 v152, v152, v153\n"
      "v_mfma_f32_16x16x32_bf16 v[36:39], a[152:155], a[24:27], v[36:39]\n"
      "s_waitcnt lgkmcnt(10)\n"
      "ds_write2st64_b64 v9, v[148:149], v[212:213] offset0:2 offset1:18\n"
      "v_mfma_f32_16x16x32_bf16 v[36:39], a[156:159], a[28:31], v[36:39]\n"
      "s_add_u32 m0, s57, 0x2000\n"
      "v_cvt_pk_bf16_f32 v153, v154, v155\n"
      "v_cvt_pk_bf16_f32 v216, v216, v217\n"
      "v_mfma_f32_16x16x32_bf16 v[40:43], a[156:159], a[60:63], 0\n"
      "s_add_u32 s94, s92, 0x6000\n"
      "buffer_load_dwordx4 v4, s[40:43], s90 offen offset:0 sc1 lds\n"
      "s_nop 0\n"
      "v_mfma_f32_16x16x32_bf16 v[40:43], a[152:155], a[56:59], v[40:43]\n"
      "v_fma_f32 v36, v36, s59, v28\n"
      "v_fma_f32 v37, v37, s59, v28\n"
      "v_mfma_f32_16x16x32_bf16 v[40:43], a[148:151], a[52:55], v[40:43]\n"
      "v_fma_f32 v38, v38, s59, v28\n"
      "v_fma_f32 v39, v39, s59, v28\n"
      "v_mfma_f32_16x16x32_bf16 v[40:43], a[144:147], a[48:51], v[40:43]\n"
      "v_exp_f32 v36, v36\n"
      "v_mfma_f32_16x16x32_bf16 v[40:43], a[140:143], a[44:47], v[40:43]\n"
      "buffer_load_dwordx4 v4, s[40:43], s90 offen offset:1024 sc1 lds\n"
      "v_mfma_f32_16x16x32_bf16 v[40:43], a[136:139], a[40:43], v[40:43]\n"
      "v_exp_f32 v37, v37\n"
      "v_mfma_f32_16x16x32_bf16 v[40:43], a[132:135], a[36:39], v[40:43]\n"
      "v_exp_f32 v38, v38\n"
      "v_mfma_f32_16x16x32_bf16 v[40:43], a[128:131], a[32:35], v[40:43]\n"
      "s_waitcnt lgkmcnt(8)\n"
      "v_exp_f32 v39, v39\n"
      "v_mfma_f32_16x16x32_bf16 v[128:131], a[160:163], v[48:51], 0\n"
      "s_nop 0\n"
      "v_cvt_pk_bf16_f32 v64, v36, v37\n"
      "v_add_f32 v32, v32, v36\n"
      "v_mfma_f32_16x16x32_bf16 v[192:195], a[160:163], v[52:55], 0\n"
      "s_nop 0\n"
      "v_add_f32 v32, v32, v37\n"
      "v_cvt_pk_bf16_f32 v65, v38, v39\n"
      "v_mfma_f32_16x16x32_bf16 v[80:83], a[160:163], v[56:59], 0\n"
      "s_waitcnt lgkmcnt(6)\n"
      "ds_read_b64_tr_b16 a[160:161], v2 offset:36864\n"
      "v_add_f32 v32, v32, v38\n"
      "v_mfma_f32_16x16x32_bf16 v[84:87], a[164:167], v[56:59], 0\n"
      "ds_read_b64_tr_b16 a[162:163], v2 offset:38912\n"
      "v_add_f32 v32, v32, v39\n"
      "v_mfma_f32_16x16x32_bf16 v[196:199], a[164:167], v[52:55], 0\n"
      "buffer_load_dwordx4 a[96:99], v13, s[44:47], s94 offen offset:0\n"
      "buffer_load_dwordx4 a[100:103], v13, s[44:47], s94 offen offset:64\n"
      "v_mfma_f32_16x16x32_bf16 v[132:135], a[164:167], v[48:51], 0\n"
      "s_waitcnt lgkmcnt(6)\n"
      "ds_read_b64_tr_b16 a[164:165], v3 offset:36864\n"
      "v_cvt_pk_bf16_f32 v217, v218, v219\n"
      "v_mfma_f32_16x16x32_bf16 v[136:139], a[168:171], v[48:51], 0\n"
      "ds_read_b64_tr_b16 a[166:167], v3 offset:38912\n"
      "v_cvt_pk_bf16_f32 v156, v156, v157\n"
      "v_mfma_f32_16x16x32_bf16 v[200:203], a[168:171], v[52:55], 0\n"
      "ds_write2st64_b64 v10, v[152:153], v[216:217] offset0:2 offset1:18\n"
      "v_mfma_f32_16x16x32_bf16 v[88:91], a[168:171], v[56:59], 0\n"
      "s_waitcnt lgkmcnt(7)\n"
      "ds_read_b64_tr_b16 a[168:169], v2 offset:37376\n"
      "buffer_load_dwordx4 a[104:107], v13, s[44:47], s94 offen offset:128\n"
      "v_mfma_f32_16x16x32_bf16 v[92:95], a[172:175], v[56:59], 0\n"
      "ds_read_b64_tr_b16 a[170:171], v2 offset:39424\n"
      "v_cvt_pk_bf16_f32 v157, v158, v159\n"
      "v_mfma_f32_16x16x32_bf16 v[204:207], a[172:175], v[52:55], 0\n"
      "v_cvt_pk_bf16_f32 v220, v220, v221\n"
      "v_cvt_pk_bf16_f32 v221, v222, v223\n"
      "v_mfma_f32_16x16x32_bf16 v[140:143], a[172:175], v[48:51], 0\n"
      "ds_read_b64_tr_b16 a[172:173], v3 offset:37376\n"
      "v_fma_f32 v40, v40, s59, v29\n"
      "v_mfma_f32_16x16x32_bf16 v[36:39], a[128:131], a[64:67], 0\n"
      "ds_read_b64_tr_b16 a[174:175], v3 offset:39424\n"
      "buffer_load_dwordx4 a[108:111], v13, s[44:47], s94 offen offset:192\n"
      "v_mfma_f32_16x16x32_bf16 v[36:39], a[132:135], a[68:71], v[36:39]\n"
      "ds_write2st64_b64 v11, v[156:157], v[220:221] offset0:2 offset1:18\n"
      "v_mfma_f32_16x16x32_bf16 v[36:39], a[136:139], a[72:75], v[36:39]\n"
      "v_fma_f32 v41, v41, s59, v29\n"
      "v_fma_f32 v42, v42, s59, v29\n"
      "v_mfma_f32_16x16x32_bf16 v[36:39], a[140:143], a[76:79], v[36:39]\n"
      "v_fma_f32 v43, v43, s59, v29\n"
      "v_accvgpr_read_b32 v16, a192\n"
      "v_mfma_f32_16x16x32_bf16 v[36:39], a[144:147], a[80:83], v[36:39]\n"
      "v_exp_f32 v40, v40\n"
      "v_mfma_f32_16x16x32_bf16 v[36:39], a[148:151], a[84:87], v[36:39]\n"
      "v_exp_f32 v41, v41\n"
      "v_mfma_f32_16x16x32_bf16 v[36:39], a[152:155], a[88:91], v[36:39]\n"
      "v_exp_f32 v42, v42\n"
      "v_mfma_f32_16x16x32_bf16 v[36:39], a[156:159], a[92:95], v[36:39]\n"
      "s_waitcnt vmcnt(12) lgkmcnt(0)\n"
      "s_barrier\n"
      "v_mfma_f32_16x16x32_bf16 v[144:147], a[160:163], v[48:51], 0\n"
      "s_add_u32 s52, s52, 0x4000\n"
      "ds_read_b128 a[128:131], v1 offset:18432\n"
      "v_cvt_pk_bf16_f32 v68, v40, v41\n"
      "v_mfma_f32_16x16x32_bf16 v[208:211], a[160:163], v[52:55], 0\n"
      "s_mov_b32 m0, s55\n"
      "ds_read_b128 a[132:135], v1 offset:18944\n"
      "v_add_f32 v33, v33, v40\n"
      "v_mfma_f32_16x16x32_bf16 v[96:99], a[160:163], v[56:59], 0\n"
      "s_add_u32 s53, s53, 0x4000\n"
      "ds_read_b128 a[136:139], v1 offset:22528\n"
      "v_add_f32 v33, v33, v41\n"
      "v_mfma_f32_16x16x32_bf16 v[100:103], a[164:167], v[56:59], 0\n"
      "s_add_u32 s90, s90, 0x4000\n"
      "ds_read_b128 a[140:143], v1 offset:23040\n"
      "v_add_f32 v33, v33, v42\n"
      "v_mfma_f32_16x16x32_bf16 v[212:215], a[164:167], v[52:55], 0\n"
      "ds_read_b128 a[144:147], v1 offset:26624\n"
      "v_accvgpr_read_b32 v17, a193\n"
      "v_mfma_f32_16x16x32_bf16 v[148:151], a[164:167], v[48:51], 0\n"
      "ds_read_b128 a[148:151], v1 offset:27136\n"
      "v_accvgpr_read_b32 v18, a194\n"
      "v_mfma_f32_16x16x32_bf16 v[152:155], a[168:171], v[48:51], 0\n"
      "ds_read_b128 a[152:155], v1 offset:30720\n"
      "v_accvgpr_read_b32 v19, a195\n"
      "v_mfma_f32_16x16x32_bf16 v[216:219], a[168:171], v[52:55], 0\n"
      "v_exp_f32 v43, v43\n"
      "v_mfma_f32_16x16x32_bf16 v[104:107], a[168:171], v[56:59], 0\n"
      "ds_read_b128 a[156:159], v1 offset:31232\n"
      "v_cvt_pk_bf16_f32 v69, v42, v43\n"
      "v_mfma_f32_16x16x32_bf16 v[108:111], a[172:175], v[56:59], 0\n"
      "ds_read_b64_tr_b16 a[160:161], v2 offset:40960\n"
      "v_add_f32 v33, v33, v43\n"
      "v_mfma_f32_16x16x32_bf16 v[220:223], a[172:175], v[52:55], 0\n"
      "v_rcp_f32 v14, v35\n"
      "v_mfma_f32_16x16x32_bf16 v[156:159], a[172:175], v[48:51], 0\n"
      "s_waitcnt lgkmcnt(8)\n"
      "ds_read_b64_tr_b16 a[162:163], v2 offset:43008\n"
      "v_mov_b32 v15, v14\n"
      "v_accvgpr_read_b32 v20, a196\n"
      "v_mfma_f32_16x16x32_bf16 v[40:43], a[128:131], a[0:3], 0\n"
      "s_waitcnt lgkmcnt(8)\n"
      "ds_read_b64_tr_b16 a[164:165], v3 offset:40960\n"
      "v_accvgpr_read_b32 v21, a197\n"
      "v_accvgpr_read_b32 v22, a198\n"
      "v_accvgpr_read_b32 v23, a199\n"
      "v_fma_f32 v36, v36, s59, v30\n"
      "v_pk_mul_f32 v[16:17], v[16:17], v[14:15]\n"
      "v_pk_mul_f32 v[18:19], v[18:19], v[14:15]\n"
      "v_pk_mul_f32 v[20:21], v[20:21], v[14:15]\n"
      "v_pk_mul_f32 v[22:23], v[22:23], v[14:15]\n"
      "v_pk_mul_f32 v[188:189], v[188:189], v[24:25]\n"
      "v_pk_mul_f32 v[190:191], v[190:191], v[24:25]\n"
      "v_cvt_pk_bf16_f32 v16, v16, v17\n"
      "v_mfma_f32_16x16x32_bf16 v[40:43], a[132:135], a[4:7], v[40:43]\n"
      "s_waitcnt lgkmcnt(8)\n"
      "ds_read_b64_tr_b16 a[166:167], v3 offset:43008\n"
      "v_cvt_pk_bf16_f32 v17, v18, v19\n"
      "v_cvt_pk_bf16_f32 v18, v20, v21\n"
      "v_cvt_pk_bf16_f32 v19, v22, v23\n"
      "v_fma_f32 v37, v37, s59, v30\n"
      "v_fma_f32 v38, v38, s59, v30\n"
      "v_fma_f32 v39, v39, s59, v30\n"
      "v_mfma_f32_16x16x32_bf16 v[40:43], a[136:139], a[8:11], v[40:43]\n"
      "s_waitcnt lgkmcnt(8)\n"
      "v_permlane16_swap_b32 v16, v18\n"
      "v_permlane16_swap_b32 v17, v19\n"
      "v_exp_f32 v36, v36\n"
      "v_accvgpr_read_b32 v20, a204\n"
      "v_mfma_f32_16x16x32_bf16 v[40:43], a[140:143], a[12:15], v[40:43]\n"
      "s_waitcnt lgkmcnt(7)\n"
      "v_exp_f32 v37, v37\n"
      "v_exp_f32 v38, v38\n"
      "v_exp_f32 v39, v39\n"
      "v_accvgpr_read_b32 v21, a205\n"
      "v_accvgpr_read_b32 v22, a206\n"
      "v_mfma_f32_16x16x32_bf16 v[40:43], a[144:147], a[16:19], v[40:43]\n"
      "s_waitcnt lgkmcnt(6)\n"
      "buffer_load_dwordx4 v4, s[36:39], s52 offen offset:0 sc1 lds\n"
      "ds_read_b64_tr_b16 a[168:169], v2 offset:41472\n"
      "v_accvgpr_read_b32 v23, a207\n"
      "v_cvt_pk_bf16_f32 v72, v36, v37\n"
      "v_add_f32 v34, v34, v36\n"
      "v_pk_mul_f32 v[20:21], v[20:21], v[14:15]\n"
      "v_pk_mul_f32 v[22:23], v[22:23], v[14:15]\n"
      "v_add_f32 v34, v34, v37\n"
      "v_mfma_f32_16x16x32_bf16 v[40:43], a[148:151], a[20:23], v[40:43]\n"
      "s_waitcnt lgkmcnt(6)\n"
      "buffer_store_dwordx4 v[16:19], v44, s[48:51], s95 offen offset:0 nt\n"
      "ds_read_b64_tr_b16 a[170:171], v2 offset:43520\n"
      "v_cvt_pk_bf16_f32 v73, v38, v39\n"
      "v_accvgpr_read_b32 v16, a200\n"
      "v_accvgpr_read_b32 v17, a201\n"
      "v_accvgpr_read_b32 v18, a202\n"
      "v_accvgpr_read_b32 v19, a203\n"
      "v_pk_mul_f32 v[16:17], v[16:17], v[14:15]\n"
      "v_cvt_pk_bf16_f32 v16, v16, v17\n"
      "v_mfma_f32_16x16x32_bf16 v[40:43], a[152:155], a[24:27], v[40:43]\n"
      "s_waitcnt lgkmcnt(6)\n"
      "ds_read_b64_tr_b16 a[172:173], v3 offset:41472\n"
      "v_add_f32 v34, v34, v38\n"
      "v_add_f32 v34, v34, v39\n"
      "v_cvt_pk_bf16_f32 v160, v160, v161\n"
      "v_cvt_pk_bf16_f32 v161, v162, v163\n"
      "v_pk_mul_f32 v[18:19], v[18:19], v[14:15]\n"
      "v_cvt_pk_bf16_f32 v17, v18, v19\n"
      "v_cvt_pk_bf16_f32 v18, v20, v21\n"
      "v_cvt_pk_bf16_f32 v19, v22, v23\n"
      "v_accvgpr_read_b32 v20, a212\n"
      "v_permlane16_swap_b32 v16, v18\n"
      "v_mfma_f32_16x16x32_bf16 v[40:43], a[156:159], a[28:31], v[40:43]\n"
      "s_nop 0\n"
      "v_permlane16_swap_b32 v17, v19\n"
      "v_accvgpr_read_b32 v21, a213\n"
      "v_accvgpr_read_b32 v22, a214\n"
      "v_accvgpr_read_b32 v23, a215\n"
      "v_pk_mul_f32 v[20:21], v[20:21], v[14:15]\n"
      "v_pk_mul_f32 v[22:23], v[22:23], v[14:15]\n"
      "v_cvt_pk_bf16_f32 v224, v224, v225\n"
      "v_cvt_pk_bf16_f32 v225, v226, v227\n"
      "v_cvt_pk_bf16_f32 v112, v112, v113\n"
      "v_mfma_f32_16x16x32_bf16 v[36:39], a[156:159], a[60:63], 0\n"
      "buffer_store_dwordx4 v[16:19], v44, s[48:51], s95 offen offset:64 nt\n"
      "buffer_load_dwordx4 v4, s[36:39], s52 offen offset:1024 sc1 lds\n"
      "ds_write2st64_b64 v8, v[160:161], v[224:225] offset0:4 offset1:20\n"
      "ds_read_b64_tr_b16 a[174:175], v3 offset:43520\n"
      "v_accvgpr_read_b32 v16, a208\n"
      "v_mfma_f32_16x16x32_bf16 v[36:39], a[152:155], a[56:59], v[36:39]\n"
      "s_mov_b32 m0, s56\n"
      "v_accvgpr_read_b32 v17, a209\n"
      "v_accvgpr_read_b32 v18, a210\n"
      "v_accvgpr_read_b32 v19, a211\n"
      "v_pk_mul_f32 v[16:17], v[16:17], v[14:15]\n"
      "v_pk_mul_f32 v[18:19], v[18:19], v[14:15]\n"
      "v_cvt_pk_bf16_f32 v16, v16, v17\n"
      "v_cvt_pk_bf16_f32 v17, v18, v19\n"
      "v_cvt_pk_bf16_f32 v18, v20, v21\n"
      "v_cvt_pk_bf16_f32 v19, v22, v23\n"
      "v_accvgpr_read_b32 v20, a220\n"
      "v_accvgpr_read_b32 v21, a221\n"
      "v_mfma_f32_16x16x32_bf16 v[36:39], a[148:151], a[52:55], v[36:39]\n"
      "v_permlane16_swap_b32 v16, v18\n"
      "v_permlane16_swap_b32 v17, v19\n"
      "v_accvgpr_read_b32 v22, a222\n"
      "v_pk_mul_f32 v[20:21], v[20:21], v[14:15]\n"
      "v_accvgpr_read_b32 v23, a223\n"
      "v_pk_mul_f32 v[22:23], v[22:23], v[14:15]\n"
      "v_cvt_pk_bf16_f32 v113, v114, v115\n"
      "v_cvt_pk_bf16_f32 v116, v116, v117\n"
      "v_cvt_pk_bf16_f32 v117, v118, v119\n"
      "v_cvt_pk_bf16_f32 v164, v164, v165\n"
      "v_mfma_f32_16x16x32_bf16 v[36:39], a[144:147], a[48:51], v[36:39]\n"
      "buffer_store_dwordx4 v[16:19], v44, s[48:51], s95 offen offset:128 nt\n"
      "v_cvt_pk_bf16_f32 v165, v166, v167\n"
      "v_cvt_pk_bf16_f32 v228, v228, v229\n"
      "v_accvgpr_read_b32 v16, a216\n"
      "v_accvgpr_read_b32 v17, a217\n"
      "v_accvgpr_read_b32 v18, a218\n"
      "v_accvgpr_read_b32 v19, a219\n"
      "v_pk_mul_f32 v[16:17], v[16:17], v[14:15]\n"
      "v_pk_mul_f32 v[18:19], v[18:19], v[14:15]\n"
      "v_cvt_pk_bf16_f32 v16, v16, v17\n"
      "v_cvt_pk_bf16_f32 v17, v18, v19\n"
      "v_cvt_pk_bf16_f32 v18, v20, v21\n"
      "v_cvt_pk_bf16_f32 v19, v22, v23\n"
      "v_mfma_f32_16x16x32_bf16 v[36:39], a[140:143], a[44:47], v[36:39]\n"
      "s_nop 0\n"
      "v_permlane16_swap_b32 v16, v18\n"
      "v_permlane16_swap_b32 v17, v19\n"
      "v_accvgpr_read_b32 v20, a228\n"
      "v_accvgpr_read_b32 v21, a229\n"
      "v_accvgpr_read_b32 v22, a230\n"
      "v_accvgpr_read_b32 v23, a231\n"
      "v_pk_mul_f32 v[20:21], v[20:21], v[14:15]\n"
      "v_pk_mul_f32 v[22:23], v[22:23], v[14:15]\n"
      "v_cvt_pk_bf16_f32 v229, v230, v231\n"
      "v_cvt_pk_bf16_f32 v168, v168, v169\n"
      "v_cvt_pk_bf16_f32 v169, v170, v171\n"
      "v_mfma_f32_16x16x32_bf16 v[36:39], a[136:139], a[40:43], v[36:39]\n"
      "buffer_store_dwordx4 v[16:19], v44, s[48:51], s95 offen offset:192 nt\n"
      "buffer_load_dwordx4 v4, s[40:43], s53 offen offset:0 sc1 lds\n"
      "ds_write2st64_b64 v9, v[164:165], v[228:229] offset0:4 offset1:20\n"
      "v_accvgpr_read_b32 v16, a224\n"
      "v_accvgpr_read_b32 v17, a225\n"
      "v_accvgpr_read_b32 v18, a226\n"
      "v_accvgpr_read_b32 v19, a227\n"
      "v_pk_mul_f32 v[16:17], v[16:17], v[14:15]\n"
      "v_pk_mul_f32 v[18:19], v[18:19], v[14:15]\n"
      "v_cvt_pk_bf16_f32 v16, v16, v17\n"
      "v_cvt_pk_bf16_f32 v17, v18, v19\n"
      "v_cvt_pk_bf16_f32 v18, v20, v21\n"
      "v_cvt_pk_bf16_f32 v19, v22, v23\n"
      "v_mfma_f32_16x16x32_bf16 v[36:39], a[132:135], a[36:39], v[36:39]\n"
      "s_nop 0\n"
      "v_permlane16_swap_b32 v16, v18\n"
      "v_permlane16_swap_b32 v17, v19\n"
      "v_accvgpr_read_b32 v20, a236\n"
      "v_accvgpr_read_b32 v21, a237\n"
      "v_accvgpr_read_b32 v22, a238\n"
      "v_accvgpr_read_b32 v23, a239\n"
      "v_pk_mul_f32 v[20:21], v[20:21], v[14:15]\n"
      "v_pk_mul_f32 v[22:23], v[22:23], v[14:15]\n"
      "v_cvt_pk_bf16_f32 v232, v232, v233\n"
      "v_cvt_pk_bf16_f32 v233, v234, v235\n"
      "v_cvt_pk_bf16_f32 v120, v120, v121\n"
      "v_mfma_f32_16x16x32_bf16 v[36:39], a[128:131], a[32:35], v[36:39]\n"
      "s_waitcnt lgkmcnt(8)\n"
      "buffer_store_dwordx4 v[16:19], v44, s[48:51], s95 offen offset:256 nt\n"
      "ds_write2st64_b64 v10, v[168:169], v[232:233] offset0:4 offset1:20\n"
      "v_cvt_pk_bf16_f32 v121, v122, v123\n"
      "v_accvgpr_read_b32 v16, a232\n"
      "v_accvgpr_read_b32 v17, a233\n"
      "v_accvgpr_read_b32 v18, a234\n"
      "v_accvgpr_read_b32 v19, a235\n"
      "v_pk_mul_f32 v[16:17], v[16:17], v[14:15]\n"
      "v_pk_mul_f32 v[18:19], v[18:19], v[14:15]\n"
      "v_cvt_pk_bf16_f32 v16, v16, v17\n"
      "v_cvt_pk_bf16_f32 v17, v18, v19\n"
      "v_cvt_pk_bf16_f32 v18, v20, v21\n"
      "v_cvt_pk_bf16_f32 v19, v22, v23\n"
      "v_mfma_f32_16x16x32_bf16 v[160:163], a[160:163], v[48:51], 0\n"
      "s_nop 0\n"
      "v_permlane16_swap_b32 v16, v18\n"
      "v_permlane16_swap_b32 v17, v19\n"
      "v_accvgpr_read_b32 v20, a176\n"
      "v_accvgpr_read_b32 v21, a177\n"
      "v_accvgpr_read_b32 v22, a178\n"
      "v_accvgpr_read_b32 v23, a179\n"
      "v_pk_mul_f32 v[20:21], v[20:21], v[46:47]\n"
      "v_pk_mul_f32 v[22:23], v[22:23], v[46:47]\n"
      "v_cvt_pk_bf16_f32 v20, v20, v21\n"
      "v_cvt_pk_bf16_f32 v21, v22, v23\n"
      "v_accvgpr_read_b32 v22, a246\n"
      "v_accvgpr_read_b32 v23, a247\n"
      "v_mfma_f32_16x16x32_bf16 v[224:227], a[160:163], v[52:55], 0\n"
      "buffer_store_dwordx4 v[16:19], v44, s[48:51], s95 offen offset:320 nt\n"
      "ds_write2st64_b64 v8, v[112:113], v[20:21] offset0:36 offset1:38\n"
      "buffer_load_dwordx4 v4, s[40:43], s53 offen offset:1024 sc1 lds\n"
      "v_accvgpr_read_b32 v16, a180\n"
      "v_accvgpr_read_b32 v17, a181\n"
      "v_accvgpr_read_b32 v18, a182\n"
      "v_accvgpr_read_b32 v19, a183\n"
      "v_pk_mul_f32 v[16:17], v[16:17], v[46:47]\n"
      "v_pk_mul_f32 v[18:19], v[18:19], v[46:47]\n"
      "v_cvt_pk_bf16_f32 v16, v16, v17\n"
      "v_cvt_pk_bf16_f32 v17, v18, v19\n"
      "v_mfma_f32_16x16x32_bf16 v[112:115], a[160:163], v[56:59], 0\n"
      "s_waitcnt lgkmcnt(8)\n"
      "ds_write2st64_b64 v9, v[116:117], v[16:17] offset0:36 offset1:38\n"
      "ds_read_b64_tr_b16 a[160:161], v2 offset:45056\n"
      "v_accvgpr_read_b32 v16, a240\n"
      "v_accvgpr_read_b32 v17, a241\n"
      "v_accvgpr_read_b32 v18, a242\n"
      "v_mfma_f32_16x16x32_bf16 v[116:119], a[164:167], v[56:59], 0\n"
      "ds_read_b64_tr_b16 a[162:163], v2 offset:47104\n"
      "v_accvgpr_read_b32 v19, a243\n"
      "v_accvgpr_read_b32 v20, a244\n"
      "v_accvgpr_read_b32 v21, a245\n"
      "v_cvt_pk_bf16_f32 v124, v124, v125\n"
      "v_pk_mul_f32 v[16:17], v[16:17], v[14:15]\n"
      "v_pk_mul_f32 v[18:19], v[18:19], v[14:15]\n"
      "v_cvt_pk_bf16_f32 v16, v16, v17\n"
      "v_cvt_pk_bf16_f32 v17, v18, v19\n"
      "v_mfma_f32_16x16x32_bf16 v[228:231], a[164:167], v[52:55], 0\n"
      "v_cvt_pk_bf16_f32 v125, v126, v127\n"
      "v_cvt_pk_bf16_f32 v172, v172, v173\n"
      "v_cvt_pk_bf16_f32 v173, v174, v175\n"
      "v_cvt_pk_bf16_f32 v236, v236, v237\n"
      "v_cvt_pk_bf16_f32 v237, v238, v239\n"
      "v_pk_mul_f32 v[20:21], v[20:21], v[14:15]\n"
      "v_pk_mul_f32 v[22:23], v[22:23], v[14:15]\n"
      "v_cvt_pk_bf16_f32 v18, v20, v21\n"
      "v_cvt_pk_bf16_f32 v19, v22, v23\n"
      "v_accvgpr_read_b32 v20, a184\n"
      "v_mfma_f32_16x16x32_bf16 v[164:167], a[164:167], v[48:51], 0\n"
      "s_waitcnt lgkmcnt(9)\n"
      "v_permlane16_swap_b32 v16, v18\n"
      "v_permlane16_swap_b32 v17, v19\n"
      "v_accvgpr_read_b32 v21, a185\n"
      "v_accvgpr_read_b32 v22, a186\n"
      "v_accvgpr_read_b32 v23, a187\n"
      "v_pk_mul_f32 v[20:21], v[20:21], v[46:47]\n"
      "v_pk_mul_f32 v[22:23], v[22:23], v[46:47]\n"
      "v_cvt_pk_bf16_f32 v20, v20, v21\n"
      "v_mfma_f32_16x16x32_bf16 v[168:171], a[168:171], v[48:51], 0\n"
      "ds_read_b64_tr_b16 a[164:165], v3 offset:45056\n"
      "v_cvt_pk_bf16_f32 v21, v22, v23\n"
      "v_fma_f32 v40, v40, s59, v28\n"
      "v_fma_f32 v41, v41, s59, v28\n"
      "v_fma_f32 v42, v42, s59, v28\n"
      "v_fma_f32 v43, v43, s59, v28\n"
      "v_exp_f32 v40, v40\n"
      "v_mfma_f32_16x16x32_bf16 v[232:235], a[168:171], v[52:55], 0\n"
      "buffer_store_dwordx4 v[16:19], v44, s[48:51], s95 offen offset:384 nt\n"
      "ds_write2st64_b64 v10, v[120:121], v[20:21] offset0:36 offset1:38\n"
      "ds_write2st64_b64 v11, v[172:173], v[236:237] offset0:4 offset1:20\n"
      "ds_read_b64_tr_b16 a[166:167], v3 offset:47104\n"
      "v_accvgpr_read_b32 v16, a188\n"
      "v_accvgpr_read_b32 v17, a189\n"
      "v_accvgpr_read_b32 v18, a190\n"
      "v_accvgpr_read_b32 v19, a191\n"
      "v_pk_mul_f32 v[16:17], v[16:17], v[46:47]\n"
      "v_pk_mul_f32 v[18:19], v[18:19], v[46:47]\n"
      "v_cvt_pk_bf16_f32 v16, v16, v17\n"
      "v_cvt_pk_bf16_f32 v17, v18, v19\n"
      "v_mfma_f32_16x16x32_bf16 v[120:123], a[168:171], v[56:59], 0\n"
      "s_waitcnt lgkmcnt(10)\n"
      "ds_write2st64_b64 v11, v[124:125], v[16:17] offset0:36 offset1:38\n"
      "v_mfma_f32_16x16x32_bf16 v[124:127], a[172:175], v[56:59], 0\n"
      "v_exp_f32 v41, v41\n"
      "v_exp_f32 v42, v42\n"
      "v_mfma_f32_16x16x32_bf16 v[236:239], a[172:175], v[52:55], 0\n"
      "v_exp_f32 v43, v43\n"
      "v_cvt_pk_bf16_f32 v66, v40, v41\n"
      "v_add_f32 v32, v32, v40\n"
      "v_mfma_f32_16x16x32_bf16 v[172:175], a[172:175], v[48:51], 0\n"
      "ds_read_b64_tr_b16 a[168:169], v2 offset:45568\n"
      "v_add_f32 v32, v32, v41\n"
      "v_cvt_pk_bf16_f32 v67, v42, v43\n"
      "v_add_f32 v32, v32, v42\n"
      "v_add_f32 v32, v32, v43\n"
      "v_cvt_pk_bf16_f32 v176, v176, v177\n"
      "v_cvt_pk_bf16_f32 v177, v178, v179\n"
      "v_cvt_pk_bf16_f32 v240, v240, v241\n"
      "v_mfma_f32_16x16x32_bf16 v[40:43], a[128:131], a[64:67], 0\n"
      "ds_read_b64_tr_b16 a[170:171], v2 offset:47616\n"
      "v_cvt_pk_bf16_f32 v241, v242, v243\n"
      "v_cvt_pk_bf16_f32 v180, v180, v181\n"
      "v_cvt_pk_bf16_f32 v181, v182, v183\n"
      "v_cvt_pk_bf16_f32 v244, v244, v245\n"
      "v_cvt_pk_bf16_f32 v245, v246, v247\n"
      "v_cvt_pk_bf16_f32 v184, v184, v185\n"
      "v_cvt_pk_bf16_f32 v185, v186, v187\n"
      "v_cvt_pk_bf16_f32 v248, v248, v249\n"
      "v_mfma_f32_16x16x32_bf16 v[40:43], a[132:135], a[68:71], v[40:43]\n"
      "ds_read_b64_tr_b16 a[172:173], v3 offset:45568\n"
      "ds_write2st64_b64 v8, v[176:177], v[240:241] offset0:6 offset1:22\n"
      "ds_write2st64_b64 v9, v[180:181], v[244:245] offset0:6 offset1:22\n"
      "v_mfma_f32_16x16x32_bf16 v[40:43], a[136:139], a[72:75], v[40:43]\n"
      "ds_read_b64_tr_b16 a[174:175], v3 offset:47616\n"
      "v_cvt_pk_bf16_f32 v249, v250, v251\n"
      "v_accvgpr_read_b32 v16, a248\n"
      "v_accvgpr_read_b32 v17, a249\n"
      "v_accvgpr_read_b32 v18, a250\n"
      "v_accvgpr_read_b32 v19, a251\n"
      "v_pk_mul_f32 v[16:17], v[16:17], v[14:15]\n"
      "v_pk_mul_f32 v[18:19], v[18:19], v[14:15]\n"
      "v_cvt_pk_bf16_f32 v16, v16, v17\n"
      "v_cvt_pk_bf16_f32 v17, v18, v19\n"
      "v_mfma_f32_16x16x32_bf16 v[40:43], a[140:143], a[76:79], v[40:43]\n"
      "v_accvgpr_read_b32 v20, a252\n"
      "v_accvgpr_read_b32 v21, a253\n"
      "v_accvgpr_read_b32 v22, a254\n"
      "v_accvgpr_read_b32 v23, a255\n"
      "v_fma_f32 v36, v36, s59, v29\n"
      "v_pk_mul_f32 v[20:21], v[20:21], v[14:15]\n"
      "v_pk_mul_f32 v[22:23], v[22:23], v[14:15]\n"
      "v_cvt_pk_bf16_f32 v18, v20, v21\n"
      "v_cvt_pk_bf16_f32 v19, v22, v23\n"
      "v_fma_f32 v37, v37, s59, v29\n"
      "v_mfma_f32_16x16x32_bf16 v[40:43], a[144:147], a[80:83], v[40:43]\n"
      "v_fma_f32 v38, v38, s59, v29\n"
      "v_fma_f32 v39, v39, s59, v29\n"
      "v_cvt_pk_bf16_f32 v188, v188, v189\n"
      "v_cvt_pk_bf16_f32 v189, v190, v191\n"
      "v_cvt_pk_bf16_f32 v252, v252, v253\n"
      "v_cvt_pk_bf16_f32 v253, v254, v255\n"
      "v_permlane16_swap_b32 v16, v18\n"
      "v_permlane16_swap_b32 v17, v19\n"
      "v_mfma_f32_16x16x32_bf16 v[40:43], a[148:151], a[84:87], v[40:43]\n"
      "ds_write2st64_b64 v10, v[184:185], v[248:249] offset0:6 offset1:22\n"
      "v_exp_f32 v36, v36\n"
      "v_exp_f32 v37, v37\n"
      "v_add_f32 v33, v33, v36\n"
      "v_mfma_f32_16x16x32_bf16 v[40:43], a[152:155], a[88:91], v[40:43]\n"
      "v_exp_f32 v38, v38\n"
      "v_exp_f32 v39, v39\n"
      "v_cvt_pk_bf16_f32 v70, v36, v37\n"
      "v_add_f32 v33, v33, v37\n"
      "v_cvt_pk_bf16_f32 v71, v38, v39\n"
      "v_add_f32 v33, v33, v38\n"
      "v_add_f32 v33, v33, v39\n"
      "v_mfma_f32_16x16x32_bf16 v[40:43], a[156:159], a[92:95], v[40:43]\n"
      "ds_write2st64_b64 v11, v[188:189], v[252:253] offset0:6 offset1:22\n"
      "buffer_store_dwordx4 v[16:19], v44, s[48:51], s95 offen offset:448 nt\n"
      "buffer_load_dwordx4 a[112:115], v13, s[44:47], s94 offen offset:256\n"
      "buffer_load_dwordx4 a[116:119], v13, s[44:47], s94 offen offset:320\n"
      "buffer_load_dwordx4 a[120:123], v13, s[44:47], s94 offen offset:384\n"
      "buffer_load_dwordx4 a[124:127], v13, s[44:47], s94 offen offset:448\n"
      "s_waitcnt vmcnt(24) lgkmcnt(0)\n"
      "s_barrier\n"
      "LP3_%=:\n"
      "s_bfe_u32 s78, s63, 0x50003\n"
      "s_mul_i32 s79, s93, 3\n"
      "s_cmp_ge_u32 s79, s78\n"
      "s_cbranch_scc1 LLOOP0_%=\n"
      "LENT3a_%=:\n"
      "v_mfma_f32_16x16x32_bf16 v[176:179], a[160:163], v[48:51], 0\n"
      "s_lshl_b32 s78, s61, 12\n"
      "ds_read_b128 v[16:19], v6 offset:0\n"
      "s_nop 0\n"
      "v_mfma_f32_16x16x32_bf16 v[240:243], a[160:163], v[52:55], 0\n"
      "s_add_u32 s76, s91, s78\n"
      "ds_read_b128 a[128:131], v1 offset:0\n"
      "v_fma_f32 v40, v40, s59, v30\n"
      "v_mfma_f32_16x16x32_bf16 a[176:179], a[160:163], v[56:59], 0\n"
      "s_add_u32 s74, s58, s78\n"
      "ds_read_b128 a[132:135], v1 offset:512\n"
      "v_fma_f32 v41, v41, s59, v30\n"
      "v_mfma_f32_16x16x32_bf16 a[180:183], a[164:167], v[56:59], 0\n"
      "s_lshr_b32 s78, s61, 1\n"
      "ds_read_b128 a[136:139], v1 offset:4096\n"
      "v_fma_f32 v42, v42, s59, v30\n"
      "v_mfma_f32_16x16x32_bf16 v[244:247], a[164:167], v[52:55], 0\n"
      "s_lshl_b32 s78, s78, 13\n"
      "ds_read_b128 a[140:143], v1 offset:4608\n"
      "v_fma_f32 v43, v43, s59, v30\n"
      "v_mfma_f32_16x16x32_bf16 v[180:183], a[164:167], v[48:51], 0\n"
      "s_and_b32 s79, s61, 1\n"
      "ds_read_b128 a[144:147], v1 offset:8192\n"
      "v_mfma_f32_16x16x32_bf16 v[184:187], a[168:171], v[48:51], 0\n"
      "s_lshl_b32 s79, s79, 8\n"
      "ds_read_b128 a[148:151], v1 offset:8704\n"
      "v_mfma_f32_16x16x32_bf16 v[248:251], a[168:171], v[52:55], 0\n"
      "s_add_u32 s75, s78, s79\n"
      "ds_read_b128 a[152:155], v1 offset:12288\n"
      "v_mfma_f32_16x16x32_bf16 a[184:187], a[168:171], v[56:59], 0\n"
      "s_add_u32 s75, s75, s92\n"
      "ds_read_b128 a[156:159], v1 offset:12800\n"
      "v_mfma_f32_16x16x32_bf16 a[188:191], a[172:175], v[56:59], 0\n"
      "s_cmp_lt_u32 s61, 5\n"
      "ds_read_b128 v[20:23], v6 offset:1024\n"
      "v_mfma_f32_16x16x32_bf16 v[252:255], a[172:175], v[52:55], 0\n"
      "s_cselect_b32 s77, 0x1000, 0\n"
      "v_exp_f32 v40, v40\n"
      "v_mfma_f32_16x16x32_bf16 v[188:191], a[172:175], v[48:51], 0\n"
      "LLOOP3a_%=:\n"
      "s_waitcnt lgkmcnt(8)\n"
      "s_cmp_eq_u32 s61, 6\n"
      "v_exp_f32 v41, v41\n"
      "v_mfma_f32_16x16x32_bf16 v[36:39], a[128:131], a[0:3], 0\n"
      "s_waitcnt lgkmcnt(7)\n"
      "s_cselect_b32 s76, 0x10000000, s76\n"
      "buffer_store_dwordx4 v[16:19], v7, s[48:51], s76 offen offset:0 nt\n"
      "v_exp_f32 v42, v42\n"
      "v_cvt_pk_bf16_f32 v74, v40, v41\n"
      "v_mfma_f32_16x16x32_bf16 v[36:39], a[132:135], a[4:7], v[36:39]\n"
      "s_waitcnt lgkmcnt(6)\n"
      "v_exp_f32 v43, v43\n"
      "v_mfma_f32_16x16x32_bf16 v[36:39], a[136:139], a[8:11], v[36:39]\n"
      "s_waitcnt lgkmcnt(5)\n"
      "ds_read_b64_tr_b16 a[160:161], v2 offset:49152\n"
      "v_add_f32 v34, v34, v40\n"
      "v_mfma_f32_16x16x32_bf16 v[36:39], a[140:143], a[12:15], v[36:39]\n"
      "s_waitcnt lgkmcnt(5)\n"
      "ds_read_b64_tr_b16 a[162:163], v2 offset:51200\n"
      "v_add_f32 v34, v34, v41\n"
      "v_mfma_f32_16x16x32_bf16 v[36:39], a[144:147], a[16:19], v[36:39]\n"
      "s_waitcnt lgkmcnt(5)\n"
      "s_mov_b32 m0, s55\n"
      "v_cvt_pk_bf16_f32 v75, v42, v43\n"
      "v_mfma_f32_16x16x32_bf16 v[36:39], a[148:151], a[20:23], v[36:39]\n"
      "s_waitcnt lgkmcnt(4)\n"
      "buffer_load_dwordx4 v4, s[36:39], s52 offen offset:2048 sc1 lds\n"
      "s_waitcnt lgkmcnt(2)\n"
      "buffer_store_dwordx4 v[20:23], v7, s[48:51], s76 offen offset:128 nt\n"
      "v_add_f32 v34, v34, v42\n"
      "v_add_f32 v34, v34, v43\n"
      "v_mfma_f32_16x16x32_bf16 v[36:39], a[152:155], a[24:27], v[36:39]\n"
      "s_cselect_b32 s77, 0xffffb000, s77\n"
      "ds_read_b128 v[16:19], v6 offset:2048\n"
      "v_mfma_f32_16x16x32_bf16 v[36:39], a[156:159], a[28:31], v[36:39]\n"
      "s_cselect_b32 s74, 0x30000, s74\n"
      "ds_read_b64_tr_b16 a[164:165], v3 offset:49152\n"
      "s_nop 0\n"
      "v_mfma_f32_16x16x32_bf16 v[40:43], a[156:159], a[60:63], 0\n"
      "s_cselect_b32 s75, 0x10000000, s75\n"
      "buffer_load_dwordx4 v4, s[36:39], s52 offen offset:3072 sc1 lds\n"
      "ds_read_b64_tr_b16 a[166:167], v3 offset:51200\n"
      "v_mfma_f32_16x16x32_bf16 v[40:43], a[152:155], a[56:59], v[40:43]\n"
      "s_add_u32 m0, s56, 0x2000\n"
      "ds_read_b64_tr_b16 a[168:169], v2 offset:49664\n"
      "v_fma_f32 v36, v36, s59, v28\n"
      "v_mfma_f32_16x16x32_bf16 v[40:43], a[148:151], a[52:55], v[40:43]\n"
      "ds_read_b64_tr_b16 a[170:171], v2 offset:51712\n"
      "v_fma_f32 v37, v37, s59, v28\n"
      "v_mfma_f32_16x16x32_bf16 v[40:43], a[144:147], a[48:51], v[40:43]\n"
      "ds_read_b64_tr_b16 a[172:173], v3 offset:49664\n"
      "v_fma_f32 v38, v38, s59, v28\n"
      "v_mfma_f32_16x16x32_bf16 v[40:43], a[140:143], a[44:47], v[40:43]\n"
      "buffer_load_dwordx4 v4, s[40:43], s90 offen offset:0 sc1 lds\n"
      "ds_read_b64_tr_b16 a[174:175], v3 offset:51712\n"
      "v_mfma_f32_16x16x32_bf16 v[40:43], a[136:139], a[40:43], v[40:43]\n"
      "s_waitcnt lgkmcnt(6)\n"
      "buffer_store_dwordx4 v[16:19], v7, s[48:51], s76 offen offset:256 nt\n"
      "v_fma_f32 v39, v39, s59, v28\n"
      "v_exp_f32 v36, v36\n"
      "v_mfma_f32_16x16x32_bf16 v[40:43], a[132:135], a[36:39], v[40:43]\n"
      "ds_read_b128 v[20:23], v6 offset:3072\n"
      "v_add_f32 v32, v32, v36\n"
      "v_mfma_f32_16x16x32_bf16 v[40:43], a[128:131], a[32:35], v[40:43]\n"
      "s_nop 0\n"
      "v_exp_f32 v37, v37\n"
      "v_mfma_f32_16x16x32_bf16 v[128:131], a[160:163], v[64:67], v[128:131]\n"
      "s_nop 0\n"
      "buffer_load_dwordx4 v4, s[40:43], s90 offen offset:1024 sc1 lds\n"
      "s_nop 0\n"
      "v_mfma_f32_16x16x32_bf16 v[192:195], a[160:163], v[68:71], v[192:195]\n"
      "s_mov_b32 m0, s96\n"
      "v_exp_f32 v38, v38\n"
      "v_mfma_f32_16x16x32_bf16 v[80:83], a[160:163], v[72:75], v[80:83]\n"
      "s_waitcnt lgkmcnt(5)\n"
      "v_exp_f32 v39, v39\n"
      "v_mfma_f32_16x16x32_bf16 v[84:87], a[164:167], v[72:75], v[84:87]\n"
      "ds_read_b64_tr_b16 a[160:161], v2 offset:53248\n"
      "v_cvt_pk_bf16_f32 v48, v36, v37\n"
      "v_mfma_f32_16x16x32_bf16 v[196:199], a[164:167], v[68:71], v[196:199]\n"
      "buffer_load_dwordx4 v5, s[44:47], s97 offen offset:1024 nt lds\n"
      "ds_read_b64_tr_b16 a[162:163], v2 offset:55296\n"
      "v_mfma_f32_16x16x32_bf16 v[132:135], a[164:167], v[64:67], v[132:135]\n"
      "s_waitcnt lgkmcnt(5)\n"
      "ds_read_b64_tr_b16 a[164:165], v3 offset:53248\n"
      "v_add_f32 v32, v32, v37\n"
      "v_mfma_f32_16x16x32_bf16 v[136:139], a[168:171], v[64:67], v[136:139]\n"
      "s_waitcnt lgkmcnt(3)\n"
      "buffer_store_dwordx4 v[20:23], v7, s[48:51], s76 offen offset:384 nt\n"
      "v_cvt_pk_bf16_f32 v49, v38, v39\n"
      "v_add_f32 v32, v32, v38\n"
      "v_add_f32 v32, v32, v39\n"
      "v_fma_f32 v40, v40, s59, v29\n"
      "v_mfma_f32_16x16x32_bf16 v[200:203], a[168:171], v[68:71], v[200:203]\n"
      "ds_read_b64_tr_b16 a[166:167], v3 offset:55296\n"
      "v_fma_f32 v41, v41, s59, v29\n"
      "v_mfma_f32_16x16x32_bf16 v[88:91], a[168:171], v[72:75], v[88:91]\n"
      "ds_read_b64_tr_b16 a[168:169], v2 offset:53760\n"
      "v_fma_f32 v42, v42, s59, v29\n"
      "v_mfma_f32_16x16x32_bf16 v[92:95], a[172:175], v[72:75], v[92:95]\n"
      "ds_read_b64_tr_b16 a[170:171], v2 offset:55808\n"
      "v_fma_f32 v43, v43, s59, v29\n"
      "v_mfma_f32_16x16x32_bf16 v[204:207], a[172:175], v[68:71], v[204:207]\n"
      "v_exp_f32 v40, v40\n"
      "v_mfma_f32_16x16x32_bf16 v[140:143], a[172:175], v[64:67], v[140:143]\n"
      "ds_read_b64_tr_b16 a[172:173], v3 offset:53760\n"
      "v_add_f32 v33, v33, v40\n"
      "v_mfma_f32_16x16x32_bf16 v[36:39], a[128:131], a[64:67], 0\n"
      "ds_read_b64_tr_b16 a[174:175], v3 offset:55808\n"
      "v_add_u32 v6, s77, v6\n"
      "v_mfma_f32_16x16x32_bf16 v[36:39], a[132:135], a[68:71], v[36:39]\n"
      "v_exp_f32 v41, v41\n"
      "v_mfma_f32_16x16x32_bf16 v[36:39], a[136:139], a[72:75], v[36:39]\n"
      "v_exp_f32 v42, v42\n"
      "v_mfma_f32_16x16x32_bf16 v[36:39], a[140:143], a[76:79], v[36:39]\n"
      "v_exp_f32 v43, v43\n"
      "v_mfma_f32_16x16x32_bf16 v[36:39], a[144:147], a[80:83], v[36:39]\n"
      "v_cvt_pk_bf16_f32 v52, v40, v41\n"
      "v_add_f32 v33, v33, v41\n"
      "v_mfma_f32_16x16x32_bf16 v[36:39], a[148:151], a[84:87], v[36:39]\n"
      "v_cvt_pk_bf16_f32 v53, v42, v43\n"
      "v_add_f32 v33, v33, v42\n"
      "v_mfma_f32_16x16x32_bf16 v[36:39], a[152:155], a[88:91], v[36:39]\n"
      "v_add_f32 v33, v33, v43\n"
      "v_mfma_f32_16x16x32_bf16 v[36:39], a[156:159], a[92:95], v[36:39]\n"
      "s_waitcnt vmcnt(15) lgkmcnt(0)\n"
      "s_barrier\n"
      "v_mfma_f32_16x16x32_bf16 v[144:147], a[160:163], v[64:67], v[144:147]\n"
      "s_add_u32 s52, s52, 0x4000\n"
      "ds_read_b128 a[128:131], v1 offset:2048\n"
      "s_nop 0\n"
      "v_mfma_f32_16x16x32_bf16 v[208:211], a[160:163], v[68:71], v[208:211]\n"
      "s_mov_b32 m0, s54\n"
      "ds_read_b128 a[132:135], v1 offset:2560\n"
      "v_fma_f32 v36, v36, s59, v30\n"
      "v_mfma_f32_16x16x32_bf16 v[96:99], a[160:163], v[72:75], v[96:99]\n"
      "s_add_u32 s53, s53, 0x4000\n"
      "ds_read_b128 a[136:139], v1 offset:6144\n"
      "v_fma_f32 v37, v37, s59, v30\n"
      "v_mfma_f32_16x16x32_bf16 v[100:103], a[164:167], v[72:75], v[100:103]\n"
      "s_add_u32 s90, s90, 0x4000\n"
      "ds_read_b128 a[140:143], v1 offset:6656\n"
      "v_fma_f32 v38, v38, s59, v30\n"
      "v_mfma_f32_16x16x32_bf16 v[212:215], a[164:167], v[68:71], v[212:215]\n"
      "ds_read_b128 a[144:147], v1 offset:10240\n"
      "v_fma_f32 v39, v39, s59, v30\n"
      "v_mfma_f32_16x16x32_bf16 v[148:151], a[164:167], v[64:67], v[148:151]\n"
      "ds_read_b128 a[148:151], v1 offset:10752\n"
      "v_mfma_f32_16x16x32_bf16 v[152:155], a[168:171], v[64:67], v[152:155]\n"
      "ds_read_b128 a[152:155], v1 offset:14336\n"
      "v_mfma_f32_16x16x32_bf16 v[216:219], a[168:171], v[68:71], v[216:219]\n"
      "ds_read_b128 a[156:159], v1 offset:14848\n"
      "v_mfma_f32_16x16x32_bf16 v[104:107], a[168:171], v[72:75], v[104:107]\n"
      "v_exp_f32 v36, v36\n"
      "v_mfma_f32_16x16x32_bf16 v[108:111], a[172:175], v[72:75], v[108:111]\n"
      "v_exp_f32 v37, v37\n"
      "v_mfma_f32_16x16x32_bf16 v[220:223], a[172:175], v[68:71], v[220:223]\n"
      "v_exp_f32 v38, v38\n"
      "v_mfma_f32_16x16x32_bf16 v[156:159], a[172:175], v[64:67], v[156:159]\n"
      "s_waitcnt lgkmcnt(7)\n"
      "v_exp_f32 v39, v39\n"
      "v_mfma_f32_16x16x32_bf16 v[40:43], a[128:131], a[0:3], 0\n"
      "s_waitcnt lgkmcnt(6)\n"
      "ds_read_b64_tr_b16 a[160:161], v2 offset:57344\n"
      "v_cvt_pk_bf16_f32 v56, v36, v37\n"
      "v_mfma_f32_16x16x32_bf16 v[40:43], a[132:135], a[4:7], v[40:43]\n"
      "s_waitcnt lgkmcnt(6)\n"
      "ds_read_b64_tr_b16 a[162:163], v2 offset:59392\n"
      "v_add_f32 v34, v34, v36\n"
      "v_mfma_f32_16x16x32_bf16 v[40:43], a[136:139], a[8:11], v[40:43]\n"
      "s_waitcnt lgkmcnt(6)\n"
      "ds_read_b64_tr_b16 a[164:165], v3 offset:57344\n"
      "v_add_f32 v34, v34, v37\n"
      "v_mfma_f32_16x16x32_bf16 v[40:43], a[140:143], a[12:15], v[40:43]\n"
      "s_waitcnt lgkmcnt(6)\n"
      "ds_read_b64_tr_b16 a[166:167], v3 offset:59392\n"
      "v_cvt_pk_bf16_f32 v57, v38, v39\n"
      "v_mfma_f32_16x16x32_bf16 v[40:43], a[144:147], a[16:19], v[40:43]\n"
      "s_waitcnt lgkmcnt(6)\n"
      "buffer_load_dwordx4 v4, s[36:39], s52 offen offset:0 sc1 lds\n"
      "ds_read_b64_tr_b16 a[168:169], v2 offset:57856\n"
      "v_mfma_f32_16x16x32_bf16 v[40:43], a[148:151], a[20:23], v[40:43]\n"
      "s_waitcnt lgkmcnt(6)\n"
      "ds_read_b64_tr_b16 a[170:171], v2 offset:59904\n"
      "v_add_f32 v34, v34, v38\n"
      "v_mfma_f32_16x16x32_bf16 v[40:43], a[152:155], a[24:27], v[40:43]\n"
      "s_waitcnt lgkmcnt(6)\n"
      "ds_read_b64_tr_b16 a[172:173], v3 offset:57856\n"
      "v_add_f32 v34, v34, v39\n"
      "v_mfma_f32_16x16x32_bf16 v[40:43], a[156:159], a[28:31], v[40:43]\n"
      "s_nop 0\n"
      "ds_read_b64_tr_b16 a[174:175], v3 offset:59904\n"
      "s_nop 0\n"
      "v_mfma_f32_16x16x32_bf16 v[36:39], a[156:159], a[60:63], 0\n"
      "s_nop 0\n"
      "buffer_load_dwordx4 v4, s[36:39], s52 offen offset:1024 sc1 lds\n"
      "s_nop 0\n"
      "v_mfma_f32_16x16x32_bf16 v[36:39], a[152:155], a[56:59], v[36:39]\n"
      "s_mov_b32 m0, s57\n"
      "v_fma_f32 v40, v40, s59, v28\n"
      "v_fma_f32 v41, v41, s59, v28\n"
      "v_mfma_f32_16x16x32_bf16 v[36:39], a[148:151], a[52:55], v[36:39]\n"
      "v_fma_f32 v42, v42, s59, v28\n"
      "v_fma_f32 v43, v43, s59, v28\n"
      "v_mfma_f32_16x16x32_bf16 v[36:39], a[144:147], a[48:51], v[36:39]\n"
      "v_exp_f32 v40, v40\n"
      "v_mfma_f32_16x16x32_bf16 v[36:39], a[140:143], a[44:47], v[36:39]\n"
      "buffer_load_dwordx4 v4, s[40:43], s53 offen offset:0 sc1 lds\n"
      "v_mfma_f32_16x16x32_bf16 v[36:39], a[136:139], a[40:43], v[36:39]\n"
      "v_exp_f32 v41, v41\n"
      "v_mfma_f32_16x16x32_bf16 v[36:39], a[132:135], a[36:39], v[36:39]\n"
      "v_exp_f32 v42, v42\n"
      "v_mfma_f32_16x16x32_bf16 v[36:39], a[128:131], a[32:35], v[36:39]\n"
      "s_waitcnt lgkmcnt(6)\n"
      "v_exp_f32 v43, v43\n"
      "v_mfma_f32_16x16x32_bf16 v[160:163], a[160:163], v[64:67], v[160:163]\n"
      "s_nop 0\n"
      "buffer_load_dwordx4 v4, s[40:43], s53 offen offset:1024 sc1 lds\n"
      "s_nop 0\n"
      "v_mfma_f32_16x16x32_bf16 v[224:227], a[160:163], v[68:71], v[224:227]\n"
      "s_mov_b32 m0, s74\n"
      "v_cvt_pk_bf16_f32 v50, v40, v41\n"
      "v_add_f32 v32, v32, v40\n"
      "v_mfma_f32_16x16x32_bf16 v[112:115], a[160:163], v[72:75], v[112:115]\n"
      "s_waitcnt lgkmcnt(4)\n"
      "ds_read_b64_tr_b16 a[160:161], v2 offset:61440\n"
      "v_add_f32 v32, v32, v41\n"
      "v_mfma_f32_16x16x32_bf16 v[116:119], a[164:167], v[72:75], v[116:119]\n"
      "ds_read_b64_tr_b16 a[162:163], v2 offset:63488\n"
      "v_cvt_pk_bf16_f32 v51, v42, v43\n"
      "v_mfma_f32_16x16x32_bf16 v[228:231], a[164:167], v[68:71], v[228:231]\n"
      "buffer_load_dwordx4 v5, s[44:47], s75 offen offset:0 nt lds\n"
      "v_mfma_f32_16x16x32_bf16 v[164:167], a[164:167], v[64:67], v[164:167]\n"
      "s_waitcnt lgkmcnt(4)\n"
      "ds_read_b64_tr_b16 a[164:165], v3 offset:61440\n"
      "v_add_f32 v32, v32, v42\n"
      "v_mfma_f32_16x16x32_bf16 v[168:171], a[168:171], v[64:67], v[168:171]\n"
      "ds_read_b64_tr_b16 a[166:167], v3 offset:63488\n"
      "v_add_f32 v32, v32, v43\n"
      "v_mfma_f32_16x16x32_bf16 v[232:235], a[168:171], v[68:71], v[232:235]\n"
      "v_fma_f32 v36, v36, s59, v29\n"
      "v_fma_f32 v37, v37, s59, v29\n"
      "v_mfma_f32_16x16x32_bf16 v[120:123], a[168:171], v[72:75], v[120:123]\n"
      "s_waitcnt lgkmcnt(4)\n"
      "ds_read_b64_tr_b16 a[168:169], v2 offset:61952\n"
      "v_fma_f32 v38, v38, s59, v29\n"
      "v_mfma_f32_16x16x32_bf16 v[124:127], a[172:175], v[72:75], v[124:127]\n"
      "ds_read_b64_tr_b16 a[170:171], v2 offset:64000\n"
      "v_fma_f32 v39, v39, s59, v29\n"
      "v_mfma_f32_16x16x32_bf16 v[236:239], a[172:175], v[68:71], v[236:239]\n"
      "v_exp_f32 v36, v36\n"
      "v_mfma_f32_16x16x32_bf16 v[172:175], a[172:175], v[64:67], v[172:175]\n"
      "ds_read_b64_tr_b16 a[172:173], v3 offset:61952\n"
      "v_add_f32 v33, v33, v36\n"
      "v_mfma_f32_16x16x32_bf16 v[40:43], a[128:131], a[64:67], 0\n"
      "ds_read_b64_tr_b16 a[174:175], v3 offset:64000\n"
      "v_mfma_f32_16x16x32_bf16 v[40:43], a[132:135], a[68:71], v[40:43]\n"
      "v_exp_f32 v37, v37\n"
      "v_mfma_f32_16x16x32_bf16 v[40:43], a[136:139], a[72:75], v[40:43]\n"
      "v_exp_f32 v38, v38\n"
      "v_mfma_f32_16x16x32_bf16 v[40:43], a[140:143], a[76:79], v[40:43]\n"
      "v_exp_f32 v39, v39\n"
      "v_mfma_f32_16x16x32_bf16 v[40:43], a[144:147], a[80:83], v[40:43]\n"
      "v_cvt_pk_bf16_f32 v54, v36, v37\n"
      "v_add_f32 v33, v33, v37\n"
      "v_mfma_f32_16x16x32_bf16 v[40:43], a[148:151], a[84:87], v[40:43]\n"
      "v_cvt_pk_bf16_f32 v55, v38, v39\n"
      "v_add_f32 v33, v33, v38\n"
      "v_mfma_f32_16x16x32_bf16 v[40:43], a[152:155], a[88:91], v[40:43]\n"
      "v_add_f32 v33, v33, v39\n"
      "v_mfma_f32_16x16x32_bf16 v[40:43], a[156:159], a[92:95], v[40:43]\n"
      "s_waitcnt vmcnt(15) lgkmcnt(0)\n"
      "s_barrier\n"
      "v_mfma_f32_16x16x32_bf16 v[176:179], a[160:163], v[64:67], v[176:179]\n"
      "s_mov_b32 m0, s54\n"
      "ds_read_b128 a[128:131], v1 offset:16384\n"
      "s_nop 0\n"
      "v_mfma_f32_16x16x32_bf16 v[240:243], a[160:163], v[68:71], v[240:243]\n"
      "s_nop 0\n"
      "ds_read_b128 a[132:135], v1 offset:16896\n"
      "v_fma_f32 v40, v40, s59, v30\n"
      "v_mfma_f32_16x16x32_bf16 a[176:179], a[160:163], v[72:75], a[176:179]\n"
      "ds_read_b128 a[136:139], v1 offset:20480\n"
      "v_fma_f32 v41, v41, s59, v30\n"
      "v_mfma_f32_16x16x32_bf16 a[180:183], a[164:167], v[72:75], a[180:183]\n"
      "ds_read_b128 a[140:143], v1 offset:20992\n"
      "v_fma_f32 v42, v42, s59, v30\n"
      "v_mfma_f32_16x16x32_bf16 v[244:247], a[164:167], v[68:71], v[244:247]\n"
      "ds_read_b128 a[144:147], v1 offset:24576\n"
      "v_fma_f32 v43, v43, s59, v30\n"
      "v_mfma_f32_16x16x32_bf16 v[180:183], a[164:167], v[64:67], v[180:183]\n"
      "ds_read_b128 a[148:151], v1 offset:25088\n"
      "v_mfma_f32_16x16x32_bf16 v[184:187], a[168:171], v[64:67], v[184:187]\n"
      "ds_read_b128 a[152:155], v1 offset:28672\n"
      "v_mfma_f32_16x16x32_bf16 v[248:251], a[168:171], v[68:71], v[248:251]\n"
      "ds_read_b128 a[156:159], v1 offset:29184\n"
      "v_mfma_f32_16x16x32_bf16 a[184:187], a[168:171], v[72:75], a[184:187]\n"
      "v_exp_f32 v40, v40\n"
      "v_mfma_f32_16x16x32_bf16 a[188:191], a[172:175], v[72:75], a[188:191]\n"
      "v_exp_f32 v41, v41\n"
      "v_mfma_f32_16x16x32_bf16 v[252:255], a[172:175], v[68:71], v[252:255]\n"
      "v_exp_f32 v42, v42\n"
      "v_mfma_f32_16x16x32_bf16 v[188:191], a[172:175], v[64:67], v[188:191]\n"
      "s_waitcnt lgkmcnt(7)\n"
      "v_exp_f32 v43, v43\n"
      "v_mfma_f32_16x16x32_bf16 v[36:39], a[128:131], a[0:3], 0\n"
      "s_waitcnt lgkmcnt(6)\n"
      "ds_read_b64_tr_b16 a[160:161], v2 offset:32768\n"
      "v_cvt_pk_bf16_f32 v58, v40, v41\n"
      "v_mfma_f32_16x16x32_bf16 v[36:39], a[132:135], a[4:7], v[36:39]\n"
      "s_waitcnt lgkmcnt(6)\n"
      "ds_read_b64_tr_b16 a[162:163], v2 offset:34816\n"
      "v_add_f32 v34, v34, v40\n"
      "v_mfma_f32_16x16x32_bf16 v[36:39], a[136:139], a[8:11], v[36:39]\n"
      "s_waitcnt lgkmcnt(6)\n"
      "ds_read_b64_tr_b16 a[164:165], v3 offset:32768\n"
      "v_add_f32 v34, v34, v41\n"
      "v_mfma_f32_16x16x32_bf16 v[36:39], a[140:143], a[12:15], v[36:39]\n"
      "s_waitcnt lgkmcnt(6)\n"
      "ds_read_b64_tr_b16 a[166:167], v3 offset:34816\n"
      "v_cvt_pk_bf16_f32 v59, v42, v43\n"
      "v_mfma_f32_16x16x32_bf16 v[36:39], a[144:147], a[16:19], v[36:39]\n"
      "s_waitcnt lgkmcnt(6)\n"
      "buffer_load_dwordx4 v4, s[36:39], s52 offen offset:2048 sc1 lds\n"
      "ds_read_b64_tr_b16 a[168:169], v2 offset:33280\n"
      "v_mfma_f32_16x16x32_bf16 v[36:39], a[148:151], a[20:23], v[36:39]\n"
      "s_waitcnt lgkmcnt(6)\n"
      "ds_read_b64_tr_b16 a[170:171], v2 offset:35328\n"
      "v_add_f32 v34, v34, v42\n"
      "v_mfma_f32_16x16x32_bf16 v[36:39], a[152:155], a[24:27], v[36:39]\n"
      "s_waitcnt lgkmcnt(6)\n"
      "ds_read_b64_tr_b16 a[172:173], v3 offset:33280\n"
      "v_add_f32 v34, v34, v43\n"
      "v_mfma_f32_16x16x32_bf16 v[36:39], a[156:159], a[28:31], v[36:39]\n"
      "s_nop 0\n"
      "ds_read_b64_tr_b16 a[174:175], v3 offset:35328\n"
      "s_nop 0\n"
      "v_mfma_f32_16x16x32_bf16 v[40:43], a[156:159], a[60:63], 0\n"
      "s_nop 0\n"
      "buffer_load_dwordx4 v4, s[36:39], s52 offen offset:3072 sc1 lds\n"
      "s_nop 0\n"
      "v_mfma_f32_16x16x32_bf16 v[40:43], a[152:155], a[56:59], v[40:43]\n"
      "s_add_u32 m0, s57, 0x2000\n"
      "v_fma_f32 v36, v36, s59, v28\n"
      "v_fma_f32 v37, v37, s59, v28\n"
      "v_mfma_f32_16x16x32_bf16 v[40:43], a[148:151], a[52:55], v[40:43]\n"
      "v_fma_f32 v38, v38, s59, v28\n"
      "v_fma_f32 v39, v39, s59, v28\n"
      "v_mfma_f32_16x16x32_bf16 v[40:43], a[144:147], a[48:51], v[40:43]\n"
      "v_exp_f32 v36, v36\n"
      "v_mfma_f32_16x16x32_bf16 v[40:43], a[140:143], a[44:47], v[40:43]\n"
      "buffer_load_dwordx4 v4, s[40:43], s90 offen offset:0 sc1 lds\n"
      "v_mfma_f32_16x16x32_bf16 v[40:43], a[136:139], a[40:43], v[40:43]\n"
      "v_exp_f32 v37, v37\n"
      "v_mfma_f32_16x16x32_bf16 v[40:43], a[132:135], a[36:39], v[40:43]\n"
      "v_exp_f32 v38, v38\n"
      "v_mfma_f32_16x16x32_bf16 v[40:43], a[128:131], a[32:35], v[40:43]\n"
      "s_waitcnt lgkmcnt(6)\n"
      "v_exp_f32 v39, v39\n"
      "v_mfma_f32_16x16x32_bf16 v[128:131], a[160:163], v[48:51], v[128:131]\n"
      "s_nop 0\n"
      "buffer_load_dwordx4 v4, s[40:43], s90 offen offset:1024 sc1 lds\n"
      "s_nop 0\n"
      "v_mfma_f32_16x16x32_bf16 v[192:195], a[160:163], v[52:55], v[192:195]\n"
      "s_mov_b32 m0, s74\n"
      "v_cvt_pk_bf16_f32 v64, v36, v37\n"
      "v_add_f32 v32, v32, v36\n"
      "v_mfma_f32_16x16x32_bf16 v[80:83], a[160:163], v[56:59], v[80:83]\n"
      "s_waitcnt lgkmcnt(4)\n"
      "ds_read_b64_tr_b16 a[160:161], v2 offset:36864\n"
      "v_add_f32 v32, v32, v37\n"
      "v_mfma_f32_16x16x32_bf16 v[84:87], a[164:167], v[56:59], v[84:87]\n"
      "ds_read_b64_tr_b16 a[162:163], v2 offset:38912\n"
      "v_cvt_pk_bf16_f32 v65, v38, v39\n"
      "v_mfma_f32_16x16x32_bf16 v[196:199], a[164:167], v[52:55], v[196:199]\n"
      "buffer_load_dwordx4 v5, s[44:47], s75 offen offset:1024 nt lds\n"
      "v_mfma_f32_16x16x32_bf16 v[132:135], a[164:167], v[48:51], v[132:135]\n"
      "s_waitcnt lgkmcnt(4)\n"
      "ds_read_b64_tr_b16 a[164:165], v3 offset:36864\n"
      "v_add_f32 v32, v32, v38\n"
      "v_mfma_f32_16x16x32_bf16 v[136:139], a[168:171], v[48:51], v[136:139]\n"
      "ds_read_b64_tr_b16 a[166:167], v3 offset:38912\n"
      "v_add_f32 v32, v32, v39\n"
      "v_mfma_f32_16x16x32_bf16 v[200:203], a[168:171], v[52:55], v[200:203]\n"
      "v_fma_f32 v40, v40, s59, v29\n"
      "v_fma_f32 v41, v41, s59, v29\n"
      "v_mfma_f32_16x16x32_bf16 v[88:91], a[168:171], v[56:59], v[88:91]\n"
      "s_waitcnt lgkmcnt(4)\n"
      "ds_read_b64_tr_b16 a[168:169], v2 offset:37376\n"
      "v_fma_f32 v42, v42, s59, v29\n"
      "v_mfma_f32_16x16x32_bf16 v[92:95], a[172:175], v[56:59], v[92:95]\n"
      "ds_read_b64_tr_b16 a[170:171], v2 offset:39424\n"
      "v_fma_f32 v43, v43, s59, v29\n"
      "v_mfma_f32_16x16x32_bf16 v[204:207], a[172:175], v[52:55], v[204:207]\n"
      "v_exp_f32 v40, v40\n"
      "v_mfma_f32_16x16x32_bf16 v[140:143], a[172:175], v[48:51], v[140:143]\n"
      "ds_read_b64_tr_b16 a[172:173], v3 offset:37376\n"
      "v_add_f32 v33, v33, v40\n"
      "v_mfma_f32_16x16x32_bf16 v[36:39], a[128:131], a[64:67], 0\n"
      "ds_read_b64_tr_b16 a[174:175], v3 offset:39424\n"
      "v_mfma_f32_16x16x32_bf16 v[36:39], a[132:135], a[68:71], v[36:39]\n"
      "v_exp_f32 v41, v41\n"
      "v_mfma_f32_16x16x32_bf16 v[36:39], a[136:139], a[72:75], v[36:39]\n"
      "v_exp_f32 v42, v42\n"
      "v_mfma_f32_16x16x32_bf16 v[36:39], a[140:143], a[76:79], v[36:39]\n"
      "v_exp_f32 v43, v43\n"
      "v_mfma_f32_16x16x32_bf16 v[36:39], a[144:147], a[80:83], v[36:39]\n"
      "v_cvt_pk_bf16_f32 v68, v40, v41\n"
      "v_add_f32 v33, v33, v41\n"
      "v_mfma_f32_16x16x32_bf16 v[36:39], a[148:151], a[84:87], v[36:39]\n"
      "v_cvt_pk_bf16_f32 v69, v42, v43\n"
      "v_add_f32 v33, v33, v42\n"
      "v_mfma_f32_16x16x32_bf16 v[36:39], a[152:155], a[88:91], v[36:39]\n"
      "v_add_f32 v33, v33, v43\n"
      "v_mfma_f32_16x16x32_bf16 v[36:39], a[156:159], a[92:95], v[36:39]\n"
      "s_waitcnt vmcnt(12) lgkmcnt(0)\n"
      "s_barrier\n"
      "v_mfma_f32_16x16x32_bf16 v[144:147], a[160:163], v[48:51], v[144:147]\n"
      "s_add_u32 s52, s52, 0x4000\n"
      "ds_read_b128 a[128:131], v1 offset:18432\n"
      "s_nop 0\n"
      "v_mfma_f32_16x16x32_bf16 v[208:211], a[160:163], v[52:55], v[208:211]\n"
      "s_mov_b32 m0, s55\n"
      "ds_read_b128 a[132:135], v1 offset:18944\n"
      "v_fma_f32 v36, v36, s59, v30\n"
      "v_mfma_f32_16x16x32_bf16 v[96:99], a[160:163], v[56:59], v[96:99]\n"
      "s_add_u32 s53, s53, 0x4000\n"
      "ds_read_b128 a[136:139], v1 offset:22528\n"
      "v_fma_f32 v37, v37, s59, v30\n"
      "v_mfma_f32_16x16x32_bf16 v[100:103], a[164:167], v[56:59], v[100:103]\n"
      "s_add_u32 s90, s90, 0x4000\n"
      "ds_read_b128 a[140:143], v1 offset:23040\n"
      "v_fma_f32 v38, v38, s59, v30\n"
      "v_mfma_f32_16x16x32_bf16 v[212:215], a[164:167], v[52:55], v[212:215]\n"
      "s_add_u32 s96, s74, 0x800\n"
      "ds_read_b128 a[144:147], v1 offset:26624\n"
      "v_fma_f32 v39, v39, s59, v30\n"
      "v_mfma_f32_16x16x32_bf16 v[148:151], a[164:167], v[48:51], v[148:151]\n"
      "s_add_u32 s97, s75, 0x80\n"
      "ds_read_b128 a[148:151], v1 offset:27136\n"
      "v_mfma_f32_16x16x32_bf16 v[152:155], a[168:171], v[48:51], v[152:155]\n"
      "s_add_u32 s61, s61, 1\n"
      "ds_read_b128 a[152:155], v1 offset:30720\n"
      "v_mfma_f32_16x16x32_bf16 v[216:219], a[168:171], v[52:55], v[216:219]\n"
      "s_cmp_lt_u32 s61, 7\n"
      "ds_read_b128 a[156:159], v1 offset:31232\n"
      "v_mfma_f32_16x16x32_bf16 v[104:107], a[168:171], v[56:59], v[104:107]\n"
      "v_exp_f32 v36, v36\n"
      "v_mfma_f32_16x16x32_bf16 v[108:111], a[172:175], v[56:59], v[108:111]\n"
      "v_exp_f32 v37, v37\n"
      "v_mfma_f32_16x16x32_bf16 v[220:223], a[172:175], v[52:55], v[220:223]\n"
      "v_exp_f32 v38, v38\n"
      "v_mfma_f32_16x16x32_bf16 v[156:159], a[172:175], v[48:51], v[156:159]\n"
      "s_waitcnt lgkmcnt(7)\n"
      "v_exp_f32 v39, v39\n"
      "v_mfma_f32_16x16x32_bf16 v[40:43], a[128:131], a[0:3], 0\n"
      "s_waitcnt lgkmcnt(6)\n"
      "ds_read_b64_tr_b16 a[160:161], v2 offset:40960\n"
      "v_cvt_pk_bf16_f32 v72, v36, v37\n"
      "v_mfma_f32_16x16x32_bf16 v[40:43], a[132:135], a[4:7], v[40:43]\n"
      "s_waitcnt lgkmcnt(6)\n"
      "ds_read_b64_tr_b16 a[162:163], v2 offset:43008\n"
      "v_add_f32 v34, v34, v36\n"
      "v_mfma_f32_16x16x32_bf16 v[40:43], a[136:139], a[8:11], v[40:43]\n"
      "s_waitcnt lgkmcnt(6)\n"
      "ds_read_b64_tr_b16 a[164:165], v3 offset:40960\n"
      "v_add_f32 v34, v34, v37\n"
      "v_mfma_f32_16x16x32_bf16 v[40:43], a[140:143], a[12:15], v[40:43]\n"
      "s_waitcnt lgkmcnt(6)\n"
      "ds_read_b64_tr_b16 a[166:167], v3 offset:43008\n"
      "v_cvt_pk_bf16_f32 v73, v38, v39\n"
      "v_mfma_f32_16x16x32_bf16 v[40:43], a[144:147], a[16:19], v[40:43]\n"
      "s_waitcnt lgkmcnt(6)\n"
      "buffer_load_dwordx4 v4, s[36:39], s52 offen offset:0 sc1 lds\n"
      "ds_read_b64_tr_b16 a[168:169], v2 offset:41472\n"
      "v_mfma_f32_16x16x32_bf16 v[40:43], a[148:151], a[20:23], v[40:43]\n"
      "s_waitcnt lgkmcnt(6)\n"
      "ds_read_b64_tr_b16 a[170:171], v2 offset:43520\n"
      "v_add_f32 v34, v34, v38\n"
      "v_mfma_f32_16x16x32_bf16 v[40:43], a[152:155], a[24:27], v[40:43]\n"
      "s_waitcnt lgkmcnt(6)\n"
      "ds_read_b64_tr_b16 a[172:173], v3 offset:41472\n"
      "v_add_f32 v34, v34, v39\n"
      "v_mfma_f32_16x16x32_bf16 v[40:43], a[156:159], a[28:31], v[40:43]\n"
      "s_nop 0\n"
      "ds_read_b64_tr_b16 a[174:175], v3 offset:43520\n"
      "s_nop 0\n"
      "v_mfma_f32_16x16x32_bf16 v[36:39], a[156:159], a[60:63], 0\n"
      "s_nop 0\n"
      "buffer_load_dwordx4 v4, s[36:39], s52 offen offset:1024 sc1 lds\n"
      "s_nop 0\n"
      "v_mfma_f32_16x16x32_bf16 v[36:39], a[152:155], a[56:59], v[36:39]\n"
      "s_mov_b32 m0, s56\n"
      "v_fma_f32 v40, v40, s59, v28\n"
      "v_fma_f32 v41, v41, s59, v28\n"
      "v_mfma_f32_16x16x32_bf16 v[36:39], a[148:151], a[52:55], v[36:39]\n"
      "v_fma_f32 v42, v42, s59, v28\n"
      "v_fma_f32 v43, v43, s59, v28\n"
      "v_mfma_f32_16x16x32_bf16 v[36:39], a[144:147], a[48:51], v[36:39]\n"
      "v_exp_f32 v40, v40\n"
      "v_mfma_f32_16x16x32_bf16 v[36:39], a[140:143], a[44:47], v[36:39]\n"
      "buffer_load_dwordx4 v4, s[40:43], s53 offen offset:0 sc1 lds\n"
      "v_mfma_f32_16x16x32_bf16 v[36:39], a[136:139], a[40:43], v[36:39]\n"
      "v_exp_f32 v41, v41\n"
      "v_mfma_f32_16x16x32_bf16 v[36:39], a[132:135], a[36:39], v[36:39]\n"
      "v_exp_f32 v42, v42\n"
      "v_mfma_f32_16x16x32_bf16 v[36:39], a[128:131], a[32:35], v[36:39]\n"
      "s_waitcnt lgkmcnt(6)\n"
      "v_exp_f32 v43, v43\n"
      "v_mfma_f32_16x16x32_bf16 v[160:163], a[160:163], v[48:51], v[160:163]\n"
      "s_nop 0\n"
      "buffer_load_dwordx4 v4, s[40:43], s53 offen offset:1024 sc1 lds\n"
      "s_nop 0\n"
      "v_mfma_f32_16x16x32_bf16 v[224:227], a[160:163], v[52:55], v[224:227]\n"
      "s_mov_b32 m0, s96\n"
      "v_cvt_pk_bf16_f32 v66, v40, v41\n"
      "v_add_f32 v32, v32, v40\n"
      "v_mfma_f32_16x16x32_bf16 v[112:115], a[160:163], v[56:59], v[112:115]\n"
      "s_waitcnt lgkmcnt(4)\n"
      "ds_read_b64_tr_b16 a[160:161], v2 offset:45056\n"
      "v_add_f32 v32, v32, v41\n"
      "v_mfma_f32_16x16x32_bf16 v[116:119], a[164:167], v[56:59], v[116:119]\n"
      "ds_read_b64_tr_b16 a[162:163], v2 offset:47104\n"
      "v_cvt_pk_bf16_f32 v67, v42, v43\n"
      "v_mfma_f32_16x16x32_bf16 v[228:231], a[164:167], v[52:55], v[228:231]\n"
      "buffer_load_dwordx4 v5, s[44:47], s97 offen offset:0 nt lds\n"
      "v_mfma_f32_16x16x32_bf16 v[164:167], a[164:167], v[48:51], v[164:167]\n"
      "s_waitcnt lgkmcnt(4)\n"
      "ds_read_b64_tr_b16 a[164:165], v3 offset:45056\n"
      "v_add_f32 v32, v32, v42\n"
      "v_mfma_f32_16x16x32_bf16 v[168:171], a[168:171], v[48:51], v[168:171]\n"
      "ds_read_b64_tr_b16 a[166:167], v3 offset:47104\n"
      "v_add_f32 v32, v32, v43\n"
      "v_mfma_f32_16x16x32_bf16 v[232:235], a[168:171], v[52:55], v[232:235]\n"
      "v_fma_f32 v36, v36, s59, v29\n"
      "v_fma_f32 v37, v37, s59, v29\n"
      "v_mfma_f32_16x16x32_bf16 v[120:123], a[168:171], v[56:59], v[120:123]\n"
      "s_waitcnt lgkmcnt(4)\n"
      "ds_read_b64_tr_b16 a[168:169], v2 offset:45568\n"
      "v_fma_f32 v38, v38, s59, v29\n"
      "v_mfma_f32_16x16x32_bf16 v[124:127], a[172:175], v[56:59], v[124:127]\n"
      "ds_read_b64_tr_b16 a[170:171], v2 offset:47616\n"
      "v_fma_f32 v39, v39, s59, v29\n"
      "v_mfma_f32_16x16x32_bf16 v[236:239], a[172:175], v[52:55], v[236:239]\n"
      "v_exp_f32 v36, v36\n"
      "v_mfma_f32_16x16x32_bf16 v[172:175], a[172:175], v[48:51], v[172:175]\n"
      "ds_read_b64_tr_b16 a[172:173], v3 offset:45568\n"
      "v_add_f32 v33, v33, v36\n"
      "v_mfma_f32_16x16x32_bf16 v[40:43], a[128:131], a[64:67], 0\n"
      "ds_read_b64_tr_b16 a[174:175], v3 offset:47616\n"
      "v_mfma_f32_16x16x32_bf16 v[40:43], a[132:135], a[68:71], v[40:43]\n"
      "s_cbranch_scc0 LEX3a_%=\n"
      "v_exp_f32 v37, v37\n"
      "v_mfma_f32_16x16x32_bf16 v[40:43], a[136:139], a[72:75], v[40:43]\n"
      "v_exp_f32 v38, v38\n"
      "v_mfma_f32_16x16x32_bf16 v[40:43], a[140:143], a[76:79], v[40:43]\n"
      "v_exp_f32 v39, v39\n"
      "v_mfma_f32_16x16x32_bf16 v[40:43], a[144:147], a[80:83], v[40:43]\n"
      "v_cvt_pk_bf16_f32 v70, v36, v37\n"
      "v_add_f32 v33, v33, v37\n"
      "v_mfma_f32_16x16x32_bf16 v[40:43], a[148:151], a[84:87], v[40:43]\n"
      "v_cvt_pk_bf16_f32 v71, v38, v39\n"
      "v_add_f32 v33, v33, v38\n"
      "v_mfma_f32_16x16x32_bf16 v[40:43], a[152:155], a[88:91], v[40:43]\n"
      "v_add_f32 v33, v33, v39\n"
      "v_mfma_f32_16x16x32_bf16 v[40:43], a[156:159], a[92:95], v[40:43]\n"
      "s_waitcnt vmcnt(11) lgkmcnt(0)\n"
      "s_barrier\n"
      "v_mfma_f32_16x16x32_bf16 v[176:179], a[160:163], v[48:51], v[176:179]\n"
      "s_lshl_b32 s78, s61, 12\n"
      "ds_read_b128 v[16:19], v6 offset:0\n"
      "s_nop 0\n"
      "v_mfma_f32_16x16x32_bf16 v[240:243], a[160:163], v[52:55], v[240:243]\n"
      "s_add_u32 s76, s91, s78\n"
      "ds_read_b128 a[128:131], v1 offset:0\n"
      "v_fma_f32 v40, v40, s59, v30\n"
      "v_mfma_f32_16x16x32_bf16 a[176:179], a[160:163], v[56:59], a[176:179]\n"
      "s_add_u32 s74, s58, s78\n"
      "ds_read_b128 a[132:135], v1 offset:512\n"
      "v_fma_f32 v41, v41, s59, v30\n"
      "v_mfma_f32_16x16x32_bf16 a[180:183], a[164:167], v[56:59], a[180:183]\n"
      "s_lshr_b32 s78, s61, 1\n"
      "ds_read_b128 a[136:139], v1 offset:4096\n"
      "v_fma_f32 v42, v42, s59, v30\n"
      "v_mfma_f32_16x16x32_bf16 v[244:247], a[164:167], v[52:55], v[244:247]\n"
      "s_lshl_b32 s78, s78, 13\n"
      "ds_read_b128 a[140:143], v1 offset:4608\n"
      "v_fma_f32 v43, v43, s59, v30\n"
      "v_mfma_f32_16x16x32_bf16 v[180:183], a[164:167], v[48:51], v[180:183]\n"
      "s_and_b32 s79, s61, 1\n"
      "ds_read_b128 a[144:147], v1 offset:8192\n"
      "v_mfma_f32_16x16x32_bf16 v[184:187], a[168:171], v[48:51], v[184:187]\n"
      "s_lshl_b32 s79, s79, 8\n"
      "ds_read_b128 a[148:151], v1 offset:8704\n"
      "v_mfma_f32_16x16x32_bf16 v[248:251], a[168:171], v[52:55], v[248:251]\n"
      "s_add_u32 s75, s78, s79\n"
      "ds_read_b128 a[152:155], v1 offset:12288\n"
      "v_mfma_f32_16x16x32_bf16 a[184:187], a[168:171], v[56:59], a[184:187]\n"
      "s_add_u32 s75, s75, s92\n"
      "ds_read_b128 a[156:159], v1 offset:12800\n"
      "v_mfma_f32_16x16x32_bf16 a[188:191], a[172:175], v[56:59], a[188:191]\n"
      "s_cmp_lt_u32 s61, 5\n"
      "ds_read_b128 v[20:23], v6 offset:1024\n"
      "v_mfma_f32_16x16x32_bf16 v[252:255], a[172:175], v[52:55], v[252:255]\n"
      "s_cselect_b32 s77, 0x1000, 0\n"
      "v_exp_f32 v40, v40\n"
      "v_mfma_f32_16x16x32_bf16 v[188:191], a[172:175], v[48:51], v[188:191]\n"
      "s_branch LLOOP3a_%=\n"
      "LEX3a_%=:\n"
      "v_exp_f32 v37, v37\n"
      "v_mfma_f32_16x16x32_bf16 v[40:43], a[136:139], a[72:75], v[40:43]\n"
      "v_exp_f32 v38, v38\n"
      "v_mfma_f32_16x16x32_bf16 v[40:43], a[140:143], a[76:79], v[40:43]\n"
      "v_exp_f32 v39, v39\n"
      "v_mfma_f32_16x16x32_bf16 v[40:43], a[144:147], a[80:83], v[40:43]\n"
      "v_cvt_pk_bf16_f32 v70, v36, v37\n"
      "v_add_f32 v33, v33, v37\n"
      "v_mfma_f32_16x16x32_bf16 v[40:43], a[148:151], a[84:87], v[40:43]\n"
      "v_cvt_pk_bf16_f32 v71, v38, v39\n"
      "v_add_f32 v33, v33, v38\n"
      "v_mfma_f32_16x16x32_bf16 v[40:43], a[152:155], a[88:91], v[40:43]\n"
      "v_add_f32 v33, v33, v39\n"
      "v_mfma_f32_16x16x32_bf16 v[40:43], a[156:159], a[92:95], v[40:43]\n"
      "s_waitcnt vmcnt(11) lgkmcnt(0)\n"
      "s_barrier\n"
      "LD3_%=:\n"
      "s_cmp_eq_u32 s62, 0\n"
      "s_cbranch_scc1 LEND3_%=\n"
      "LX34_%=:\n"
      "v_mfma_f32_16x16x32_bf16 v[176:179], a[160:163], v[48:51], v[176:179]\n"
      "s_mov_b32 m0, s55\n"
      "ds_read_b128 a[0:3], v12 offset:0\n"
      "v_mov_b32 v24, v32\n"
      "v_mfma_f32_16x16x32_bf16 v[240:243], a[160:163], v[52:55], v[240:243]\n"
      "s_and_b32 s79, s63, 0xffffe000\n"
      "ds_read_b128 a[128:131], v1 offset:0\n"
      "v_fma_f32 v40, v40, s59, v30\n"
      "v_mfma_f32_16x16x32_bf16 a[176:179], a[160:163], v[56:59], a[176:179]\n"
      "s_add_u32 s48, s82, s79\n"
      "ds_read_b128 a[4:7], v12 offset:512\n"
      "v_fma_f32 v41, v41, s59, v30\n"
      "v_mfma_f32_16x16x32_bf16 a[180:183], a[164:167], v[56:59], a[180:183]\n"
      "s_addc_u32 s49, s83, 0\n"
      "ds_read_b128 a[132:135], v1 offset:512\n"
      "v_fma_f32 v42, v42, s59, v30\n"
      "v_mfma_f32_16x16x32_bf16 v[244:247], a[164:167], v[52:55], v[244:247]\n"
      "s_and_b32 s79, s63, 0xf8\n"
      "ds_read_b128 a[8:11], v12 offset:2048\n"
      "v_fma_f32 v43, v43, s59, v30\n"
      "v_mfma_f32_16x16x32_bf16 v[180:183], a[164:167], v[48:51], v[180:183]\n"
      "s_lshl_b32 s50, s79, 10\n"
      "ds_read_b128 a[136:139], v1 offset:4096\n"
      "v_mov_b32 v26, v33\n"
      "v_mfma_f32_16x16x32_bf16 v[184:187], a[168:171], v[48:51], v[184:187]\n"
      "s_and_b32 s79, s63, 7\n"
      "ds_read_b128 a[12:15], v12 offset:2560\n"
      "ds_read_b128 a[140:143], v1 offset:4608\n"
      "v_mfma_f32_16x16x32_bf16 v[248:251], a[168:171], v[52:55], v[248:251]\n"
      "s_mul_i32 s79, s79, s93\n"
      "ds_read_b128 a[16:19], v12 offset:4096\n"
      "ds_read_b128 a[144:147], v1 offset:8192\n"
      "v_mfma_f32_16x16x32_bf16 a[184:187], a[168:171], v[56:59], a[184:187]\n"
      "s_lshl_b32 s91, s79, 13\n"
      "ds_read_b128 a[20:23], v12 offset:4608\n"
      "ds_read_b128 a[148:151], v1 offset:8704\n"
      "v_mfma_f32_16x16x32_bf16 a[188:191], a[172:175], v[56:59], a[188:191]\n"
      "s_and_b32 s79, s65, 0xffffe000\n"
      "ds_read_b128 a[24:27], v12 offset:6144\n"
      "ds_read_b128 a[152:155], v1 offset:12288\n"
      "v_mfma_f32_16x16x32_bf16 v[252:255], a[172:175], v[52:55], v[252:255]\n"
      "s_add_u32 s44, s80, s79\n"
      "ds_read_b128 a[28:31], v12 offset:6656\n"
      "ds_read_b128 a[156:159], v1 offset:12800\n"
      "v_mfma_f32_16x16x32_bf16 v[188:191], a[172:175], v[48:51], v[188:191]\n"
      "s_waitcnt lgkmcnt(14)\n"
      "ds_read_b128 a[60:63], v12 offset:14848\n"
      "v_mfma_f32_16x16x32_bf16 v[36:39], a[128:131], a[0:3], 0\n"
      "s_waitcnt lgkmcnt(13)\n"
      "ds_read_b128 a[56:59], v12 offset:14336\n"
      "v_exp_f32 v40, v40\n"
      "v_mfma_f32_16x16x32_bf16 v[36:39], a[132:135], a[4:7], v[36:39]\n"
      "s_waitcnt lgkmcnt(12)\n"
      "ds_read_b128 a[52:55], v12 offset:12800\n"
      "v_exp_f32 v41, v41\n"
      "v_mfma_f32_16x16x32_bf16 v[36:39], a[136:139], a[8:11], v[36:39]\n"
      "s_waitcnt lgkmcnt(11)\n"
      "ds_read_b128 a[48:51], v12 offset:12288\n"
      "v_exp_f32 v42, v42\n"
      "v_mfma_f32_16x16x32_bf16 v[36:39], a[140:143], a[12:15], v[36:39]\n"
      "s_waitcnt lgkmcnt(10)\n"
      "ds_read_b128 a[44:47], v12 offset:10752\n"
      "v_exp_f32 v43, v43\n"
      "v_mfma_f32_16x16x32_bf16 v[36:39], a[144:147], a[16:19], v[36:39]\n"
      "s_waitcnt lgkmcnt(9)\n"
      "ds_read_b128 a[40:43], v12 offset:10240\n"
      "buffer_load_dwordx4 v4, s[36:39], s52 offen offset:2048 sc1 lds\n"
      "v_mfma_f32_16x16x32_bf16 v[36:39], a[148:151], a[20:23], v[36:39]\n"
      "s_waitcnt lgkmcnt(8)\n"
      "ds_read_b128 a[36:39], v12 offset:8704\n"
      "v_cvt_pk_bf16_f32 v74, v40, v41\n"
      "v_add_f32 v34, v34, v40\n"
      "v_add_f32 v34, v34, v41\n"
      "v_mfma_f32_16x16x32_bf16 v[36:39], a[152:155], a[24:27], v[36:39]\n"
      "s_waitcnt lgkmcnt(7)\n"
      "ds_read_b128 a[32:35], v12 offset:8192\n"
      "v_cvt_pk_bf16_f32 v75, v42, v43\n"
      "v_add_f32 v34, v34, v42\n"
      "v_mfma_f32_16x16x32_bf16 v[36:39], a[156:159], a[28:31], v[36:39]\n"
      "s_waitcnt lgkmcnt(7)\n"
      "ds_read_b64_tr_b16 a[160:161], v2 offset:49152\n"
      "v_add_f32 v34, v34, v43\n"
      "ds_read_b64_tr_b16 a[162:163], v2 offset:51200\n"
      "v_mfma_f32_16x16x32_bf16 v[40:43], a[156:159], a[60:63], 0\n"
      "s_waitcnt lgkmcnt(8)\n"
      "buffer_load_dwordx4 v4, s[36:39], s52 offen offset:3072 sc1 lds\n"
      "ds_read_b64_tr_b16 a[164:165], v3 offset:49152\n"
      "v_mfma_f32_16x16x32_bf16 v[40:43], a[152:155], a[56:59], v[40:43]\n"
      "s_waitcnt lgkmcnt(8)\n"
      "ds_read_b64_tr_b16 a[166:167], v3 offset:51200\n"
      "v_max3_f32 v28, v36, v37, v38\n"
      "v_mfma_f32_16x16x32_bf16 v[40:43], a[148:151], a[52:55], v[40:43]\n"
      "s_waitcnt lgkmcnt(8)\n"
      "ds_read_b64_tr_b16 a[168:169], v2 offset:49664\n"
      "v_max_f32 v28, v28, v39\n"
      "v_mfma_f32_16x16x32_bf16 v[40:43], a[144:147], a[48:51], v[40:43]\n"
      "s_waitcnt lgkmcnt(8)\n"
      "ds_read_b64_tr_b16 a[170:171], v2 offset:51712\n"
      "v_mov_b32 v0, v28\n"
      "v_mfma_f32_16x16x32_bf16 v[40:43], a[140:143], a[44:47], v[40:43]\n"
      "s_waitcnt lgkmcnt(8)\n"
      "s_addc_u32 s45, s81, 0\n"
      "ds_read_b64_tr_b16 a[172:173], v3 offset:49664\n"
      "v_mfma_f32_16x16x32_bf16 v[40:43], a[136:139], a[40:43], v[40:43]\n"
      "s_waitcnt lgkmcnt(8)\n"
      "s_and_b32 s79, s65, 0xf8\n"
      "ds_read_b64_tr_b16 a[174:175], v3 offset:51712\n"
      "v_mfma_f32_16x16x32_bf16 v[40:43], a[132:135], a[36:39], v[40:43]\n"
      "s_waitcnt lgkmcnt(8)\n"
      "s_lshl_b32 s46, s79, 10\n"
      "ds_read_b128 a[64:67], v12 offset:16384\n"
      "v_mfma_f32_16x16x32_bf16 v[40:43], a[128:131], a[32:35], v[40:43]\n"
      "s_waitcnt lgkmcnt(7)\n"
      "s_and_b32 s79, s65, 7\n"
      "ds_read_b128 a[68:71], v12 offset:16896\n"
      "v_mfma_f32_16x16x32_bf16 v[128:131], a[160:163], v[64:67], v[128:131]\n"
      "s_mul_i32 s79, s79, s93\n"
      "ds_read_b128 a[72:75], v12 offset:18432\n"
      "v_mov_b32 v46, v34\n"
      "v_mfma_f32_16x16x32_bf16 v[192:195], a[160:163], v[68:71], v[192:195]\n"
      "s_lshl_b32 s92, s79, 13\n"
      "ds_read_b128 a[76:79], v12 offset:18944\n"
      "v_max3_f32 v29, v40, v41, v42\n"
      "v_mfma_f32_16x16x32_bf16 v[80:83], a[160:163], v[72:75], v[80:83]\n"
      "s_waitcnt lgkmcnt(8)\n"
      "s_add_u32 s95, s91, 0x6000\n"
      "v_permlane32_swap_b32 v32, v24\n"
      "v_mfma_f32_16x16x32_bf16 v[84:87], a[164:167], v[72:75], v[84:87]\n"
      "s_sub_u32 s62, s62, 1\n"
      "v_permlane32_swap_b32 v28, v0\n"
      "v_add_f32 v32, v32, v24\n"
      "v_mov_b32 v24, v32\n"
      "v_mfma_f32_16x16x32_bf16 v[196:199], a[164:167], v[68:71], v[196:199]\n"
      "s_add_u32 m0, s56, 0x2000\n"
      "ds_read_b128 a[80:83], v12 offset:20480\n"
      "buffer_load_dwordx4 v4, s[40:43], s90 offen offset:0 sc1 lds\n"
      "buffer_load_dwordx4 v4, s[40:43], s90 offen offset:1024 sc1 lds\n"
      "v_mfma_f32_16x16x32_bf16 v[132:135], a[164:167], v[64:67], v[132:135]\n"
      "s_waitcnt lgkmcnt(7)\n"
      "ds_read_b128 a[84:87], v12 offset:20992\n"
      "v_max_f32 v28, v28, v0\n"
      "v_mov_b32 v0, v28\n"
      "v_permlane16_swap_b32 v32, v24\n"
      "v_mfma_f32_16x16x32_bf16 v[136:139], a[168:171], v[64:67], v[136:139]\n"
      "s_mov_b32 s96, 0x30000\n"
      "v_permlane16_swap_b32 v28, v0\n"
      "v_add_f32 v32, v32, v24\n"
      "v_max_f32 v28, v28, v0\n"
      "v_mul_f32 v28, s69, v28\n"
      "v_mfma_f32_16x16x32_bf16 v[200:203], a[168:171], v[68:71], v[200:203]\n"
      "s_mov_b32 s97, 0x10000000\n"
      "ds_read_b128 a[88:91], v12 offset:22528\n"
      "v_fma_f32 v36, v36, s59, v28\n"
      "v_fma_f32 v37, v37, s59, v28\n"
      "v_fma_f32 v38, v38, s59, v28\n"
      "v_fma_f32 v39, v39, s59, v28\n"
      "v_mfma_f32_16x16x32_bf16 v[88:91], a[168:171], v[72:75], v[88:91]\n"
      "s_waitcnt lgkmcnt(7)\n"
      "ds_read_b128 a[92:95], v12 offset:23040\n"
      "v_rcp_f32 v24, v32\n"
      "v_exp_f32 v36, v36\n"
      "v_mfma_f32_16x16x32_bf16 v[92:95], a[172:175], v[72:75], v[92:95]\n"
      "s_mov_b32 s63, s64\n"
      "v_exp_f32 v37, v37\n"
      "v_exp_f32 v38, v38\n"
      "v_mfma_f32_16x16x32_bf16 v[204:207], a[172:175], v[68:71], v[204:207]\n"
      "s_mov_b32 s64, s65\n"
      "v_exp_f32 v39, v39\n"
      "v_cmp_nle_f32 s[72:73], v32, s60\n"
      "v_cvt_pk_bf16_f32 v48, v36, v37\n"
      "v_mfma_f32_16x16x32_bf16 v[140:143], a[172:175], v[64:67], v[140:143]\n"
      "s_waitcnt lgkmcnt(7)\n"
      "ds_read_b64_tr_b16 a[160:161], v2 offset:53248\n"
      "v_add_f32 v32, v36, v37\n"
      "v_cvt_pk_bf16_f32 v49, v38, v39\n"
      "v_add_f32 v32, v32, v38\n"
      "v_add_f32 v32, v32, v39\n"
      "v_mfma_f32_16x16x32_bf16 v[36:39], a[128:131], a[64:67], 0\n"
      "s_waitcnt lgkmcnt(7)\n"
      "ds_read_b64_tr_b16 a[162:163], v2 offset:55296\n"
      "v_max_f32 v29, v29, v43\n"
      "v_mov_b32 v0, v29\n"
      "v_permlane32_swap_b32 v33, v26\n"
      "v_mfma_f32_16x16x32_bf16 v[36:39], a[132:135], a[68:71], v[36:39]\n"
      "s_waitcnt lgkmcnt(7)\n"
      "v_permlane32_swap_b32 v29, v0\n"
      "v_add_f32 v33, v33, v26\n"
      "v_mov_b32 v26, v33\n"
      "v_max_f32 v29, v29, v0\n"
      "v_mfma_f32_16x16x32_bf16 v[36:39], a[136:139], a[72:75], v[36:39]\n"
      "s_waitcnt lgkmcnt(6)\n"
      "ds_read_b64_tr_b16 a[164:165], v3 offset:53248\n"
      "v_mov_b32 v0, v29\n"
      "v_permlane16_swap_b32 v33, v26\n"
      "v_add_f32 v33, v33, v26\n"
      "v_mfma_f32_16x16x32_bf16 v[36:39], a[140:143], a[76:79], v[36:39]\n"
      "s_waitcnt lgkmcnt(6)\n"
      "v_permlane16_swap_b32 v29, v0\n"
      "v_max_f32 v29, v29, v0\n"
      "v_mul_f32 v29, s69, v29\n"
      "v_fma_f32 v40, v40, s59, v29\n"
      "v_mfma_f32_16x16x32_bf16 v[36:39], a[144:147], a[80:83], v[36:39]\n"
      "s_waitcnt lgkmcnt(5)\n"
      "ds_read_b64_tr_b16 a[166:167], v3 offset:55296\n"
      "v_fma_f32 v41, v41, s59, v29\n"
      "v_fma_f32 v42, v42, s59, v29\n"
      "v_fma_f32 v43, v43, s59, v29\n"
      "v_mov_b32 v25, v24\n"
      "v_mfma_f32_16x16x32_bf16 v[36:39], a[148:151], a[84:87], v[36:39]\n"
      "s_waitcnt lgkmcnt(5)\n"
      "v_rcp_f32 v26, v33\n"
      "v_exp_f32 v40, v40\n"
      "v_exp_f32 v41, v41\n"
      "v_pk_mul_f32 v[128:129], v[128:129], v[24:25]\n"
      "v_pk_mul_f32 v[130:131], v[130:131], v[24:25]\n"
      "v_pk_mul_f32 v[132:133], v[132:133], v[24:25]\n"
      "v_pk_mul_f32 v[134:135], v[134:135], v[24:25]\n"
      "v_pk_mul_f32 v[136:137], v[136:137], v[24:25]\n"
      "v_pk_mul_f32 v[138:139], v[138:139], v[24:25]\n"
      "v_pk_mul_f32 v[140:141], v[140:141], v[24:25]\n"
      "v_pk_mul_f32 v[142:143], v[142:143], v[24:25]\n"
      "v_cvt_pk_bf16_f32 v52, v40, v41\n"
      "v_mfma_f32_16x16x32_bf16 v[36:39], a[152:155], a[88:91], v[36:39]\n"
      "s_waitcnt lgkmcnt(4)\n"
      "v_exp_f32 v42, v42\n"
      "v_exp_f32 v43, v43\n"
      "v_mov_b32 v27, v26\n"
      "v_mfma_f32_16x16x32_bf16 v[36:39], a[156:159], a[92:95], v[36:39]\n"
      "s_or_b64 s[70:71], s[70:71], s[72:73]\n"
      "ds_read_b64_tr_b16 a[168:169], v2 offset:53760\n"
      "v_cmp_nle_f32 s[72:73], v33, s60\n"
      "v_add_f32 v33, v40, v41\n"
      "v_cvt_pk_bf16_f32 v53, v42, v43\n"
      "v_add_f32 v33, v33, v42\n"
      "v_pk_mul_f32 v[192:193], v[192:193], v[26:27]\n"
      "v_pk_mul_f32 v[194:195], v[194:195], v[26:27]\n"
      "v_pk_mul_f32 v[196:197], v[196:197], v[26:27]\n"
      "v_pk_mul_f32 v[198:199], v[198:199], v[26:27]\n"
      "v_pk_mul_f32 v[200:201], v[200:201], v[26:27]\n"
      "v_pk_mul_f32 v[202:203], v[202:203], v[26:27]\n"
      "v_pk_mul_f32 v[204:205], v[204:205], v[26:27]\n"
      "v_pk_mul_f32 v[206:207], v[206:207], v[26:27]\n"
      "v_add_f32 v33, v33, v43\n"
      "v_mfma_f32_16x16x32_bf16 v[40:43], a[156:159], a[124:127], 0\n"
      "s_or_b64 s[70:71], s[70:71], s[72:73]\n"
      "ds_read_b64_tr_b16 a[170:171], v2 offset:55808\n"
      "v_max3_f32 v30, v36, v37, v38\n"
      "v_mfma_f32_16x16x32_bf16 v[40:43], a[152:155], a[120:123], v[40:43]\n"
      "s_mov_b32 s65, s66\n"
      "ds_read_b64_tr_b16 a[172:173], v3 offset:53760\n"
      "v_max_f32 v30, v30, v39\n"
      "v_mfma_f32_16x16x32_bf16 v[40:43], a[148:151], a[116:119], v[40:43]\n"
      "s_mov_b32 s66, 0\n"
      "ds_read_b64_tr_b16 a[174:175], v3 offset:55808\n"
      "v_mov_b32 v0, v30\n"
      "v_mfma_f32_16x16x32_bf16 v[40:43], a[144:147], a[112:115], v[40:43]\n"
      "s_mov_b32 s61, 0\n"
      "v_permlane32_swap_b32 v34, v46\n"
      "v_mfma_f32_16x16x32_bf16 v[40:43], a[140:143], a[108:111], v[40:43]\n"
      "v_permlane32_swap_b32 v30, v0\n"
      "v_mfma_f32_16x16x32_bf16 v[40:43], a[136:139], a[104:107], v[40:43]\n"
      "v_add_f32 v34, v34, v46\n"
      "v_mov_b32 v46, v34\n"
      "v_mfma_f32_16x16x32_bf16 v[40:43], a[132:135], a[100:103], v[40:43]\n"
      "s_nop 0\n"
      "v_max_f32 v30, v30, v0\n"
      "v_mov_b32 v0, v30\n"
      "v_mfma_f32_16x16x32_bf16 v[40:43], a[128:131], a[96:99], v[40:43]\n"
      "s_waitcnt vmcnt(10) lgkmcnt(0)\n"
      "s_barrier\n"
      "v_mfma_f32_16x16x32_bf16 v[144:147], a[160:163], v[64:67], v[144:147]\n"
      "s_mov_b32 m0, s54\n"
      "ds_read_b128 a[128:131], v1 offset:2048\n"
      "v_cvt_pk_bf16_f32 v128, v128, v129\n"
      "v_mfma_f32_16x16x32_bf16 v[208:211], a[160:163], v[68:71], v[208:211]\n"
      "s_nop 0\n"
      "ds_read_b128 a[132:135], v1 offset:2560\n"
      "v_permlane16_swap_b32 v34, v46\n"
      "v_permlane16_swap_b32 v30, v0\n"
      "v_pk_mul_f32 v[144:145], v[144:145], v[24:25]\n"
      "v_pk_mul_f32 v[146:147], v[146:147], v[24:25]\n"
      "v_add_f32 v34, v34, v46\n"
      "v_mfma_f32_16x16x32_bf16 v[96:99], a[160:163], v[72:75], v[96:99]\n"
      "ds_read_b128 a[136:139], v1 offset:6144\n"
      "v_max_f32 v30, v30, v0\n"
      "v_mfma_f32_16x16x32_bf16 v[100:103], a[164:167], v[72:75], v[100:103]\n"
      "ds_read_b128 a[140:143], v1 offset:6656\n"
      "v_mul_f32 v30, s69, v30\n"
      "v_mfma_f32_16x16x32_bf16 v[212:215], a[164:167], v[68:71], v[212:215]\n"
      "s_nop 0\n"
      "ds_read_b128 a[144:147], v1 offset:10240\n"
      "v_fma_f32 v36, v36, s59, v30\n"
      "v_fma_f32 v37, v37, s59, v30\n"
      "v_fma_f32 v38, v38, s59, v30\n"
      "v_fma_f32 v39, v39, s59, v30\n"
      "v_pk_mul_f32 v[208:209], v[208:209], v[26:27]\n"
      "v_pk_mul_f32 v[210:211], v[210:211], v[26:27]\n"
      "v_pk_mul_f32 v[212:213], v[212:213], v[26:27]\n"
      "v_pk_mul_f32 v[214:215], v[214:215], v[26:27]\n"
      "v_cmp_nle_f32 s[72:73], v34, s60\n"
      "v_mfma_f32_16x16x32_bf16 v[148:151], a[164:167], v[64:67], v[148:151]\n"
      "s_or_b64 s[70:71], s[70:71], s[72:73]\n"
      "ds_read_b128 a[148:151], v1 offset:10752\n"
      "v_rcp_f32 v46, v34\n"
      "v_mfma_f32_16x16x32_bf16 v[152:155], a[168:171], v[64:67], v[152:155]\n"
      "s_cmp_lg_u64 s[70:71], 0\n"
      "ds_read_b128 a[152:155], v1 offset:14336\n"
      "v_exp_f32 v36, v36\n"
      "v_exp_f32 v37, v37\n"
      "v_pk_mul_f32 v[148:149], v[148:149], v[24:25]\n"
      "v_pk_mul_f32 v[150:151], v[150:151], v[24:25]\n"
      "v_cvt_pk_bf16_f32 v56, v36, v37\n"
      "v_mfma_f32_16x16x32_bf16 v[216:219], a[168:171], v[68:71], v[216:219]\n"
      "s_cselect_b32 s78, s68, 0\n"
      "v_exp_f32 v38, v38\n"
      "v_exp_f32 v39, v39\n"
      "v_add_f32 v34, v36, v37\n"
      "v_pk_mul_f32 v[152:153], v[152:153], v[24:25]\n"
      "v_pk_mul_f32 v[154:155], v[154:155], v[24:25]\n"
      "v_cvt_pk_bf16_f32 v57, v38, v39\n"
      "v_mfma_f32_16x16x32_bf16 v[104:107], a[168:171], v[72:75], v[104:107]\n"
      "s_or_b32 s67, s67, s78\n"
      "ds_read_b128 a[156:159], v1 offset:14848\n"
      "v_add_f32 v34, v34, v38\n"
      "v_mfma_f32_16x16x32_bf16 v[108:111], a[172:175], v[72:75], v[108:111]\n"
      "s_lshl_b32 s68, s68, 1\n"
      "ds_read_b64_tr_b16 a[160:161], v2 offset:57344\n"
      "v_add_f32 v34, v34, v39\n"
      "v_mfma_f32_16x16x32_bf16 v[220:223], a[172:175], v[68:71], v[220:223]\n"
      "s_add_u32 s52, s52, 0x4000\n"
      "ds_read_b64_tr_b16 a[162:163], v2 offset:59392\n"
      "v_max3_f32 v31, v40, v41, v42\n"
      "v_max_f32 v31, v31, v43\n"
      "v_mov_b32 v0, v31\n"
      "v_cvt_pk_bf16_f32 v129, v130, v131\n"
      "v_pk_mul_f32 v[216:217], v[216:217], v[26:27]\n"
      "v_pk_mul_f32 v[218:219], v[218:219], v[26:27]\n"
      "v_pk_mul_f32 v[220:221], v[220:221], v[26:27]\n"
      "v_pk_mul_f32 v[222:223], v[222:223], v[26:27]\n"
      "v_cvt_pk_bf16_f32 v192, v192, v193\n"
      "v_mfma_f32_16x16x32_bf16 v[156:159], a[172:175], v[64:67], v[156:159]\n"
      "s_waitcnt lgkmcnt(9)\n"
      "v_permlane32_swap_b32 v31, v0\n"
      "v_mfma_f32_16x16x32_bf16 v[36:39], a[128:131], a[0:3], 0\n"
      "s_waitcnt lgkmcnt(8)\n"
      "ds_read_b64_tr_b16 a[164:165], v3 offset:57344\n"
      "v_max_f32 v31, v31, v0\n"
      "v_mov_b32 v0, v31\n"
      "v_mfma_f32_16x16x32_bf16 v[36:39], a[132:135], a[4:7], v[36:39]\n"
      "s_waitcnt lgkmcnt(8)\n"
      "v_permlane16_swap_b32 v31, v0\n"
      "v_max_f32 v31, v31, v0\n"
      "v_mul_f32 v31, s69, v31\n"
      "v_mfma_f32_16x16x32_bf16 v[36:39], a[136:139], a[8:11], v[36:39]\n"
      "s_waitcnt lgkmcnt(7)\n"
      "ds_read_b64_tr_b16 a[166:167], v3 offset:59392\n"
      "v_fma_f32 v40, v40, s59, v31\n"
      "v_fma_f32 v41, v41, s59, v31\n"
      "v_mfma_f32_16x16x32_bf16 v[36:39], a[140:143], a[12:15], v[36:39]\n"
      "s_waitcnt lgkmcnt(7)\n"
      "ds_read_b64_tr_b16 a[168:169], v2 offset:57856\n"
      "v_fma_f32 v42, v42, s59, v31\n"
      "v_fma_f32 v43, v43, s59, v31\n"
      "v_cvt_pk_bf16_f32 v193, v194, v195\n"
      "v_mfma_f32_16x16x32_bf16 v[36:39], a[144:147], a[16:19], v[36:39]\n"
      "s_waitcnt lgkmcnt(7)\n"
      "buffer_load_dwordx4 v4, s[36:39], s52 offen offset:0 sc1 lds\n"
      "ds_read_b64_tr_b16 a[170:171], v2 offset:59904\n"
      "v_exp_f32 v40, v40\n"
      "v_mfma_f32_16x16x32_bf16 v[36:39], a[148:151], a[20:23], v[36:39]\n"
      "s_waitcnt lgkmcnt(7)\n"
      "v_exp_f32 v41, v41\n"
      "v_exp_f32 v42, v42\n"
      "v_mfma_f32_16x16x32_bf16 v[36:39], a[152:155], a[24:27], v[36:39]\n"
      "s_waitcnt lgkmcnt(6)\n"
      "v_exp_f32 v43, v43\n"
      "v_cvt_pk_bf16_f32 v60, v40, v41\n"
      "v_add_f32 v35, v40, v41\n"
      "v_mfma_f32_16x16x32_bf16 v[36:39], a[156:159], a[28:31], v[36:39]\n"
      "s_add_u32 s53, s53, 0x4000\n"
      "ds_read_b64_tr_b16 a[172:173], v3 offset:57856\n"
      "v_cvt_pk_bf16_f32 v61, v42, v43\n"
      "v_add_f32 v35, v35, v42\n"
      "v_add_f32 v35, v35, v43\n"
      "v_mfma_f32_16x16x32_bf16 v[40:43], a[156:159], a[60:63], 0\n"
      "s_add_u32 s90, s90, 0x4000\n"
      "buffer_load_dwordx4 v4, s[36:39], s52 offen offset:1024 sc1 lds\n"
      "ds_read_b64_tr_b16 a[174:175], v3 offset:59904\n"
      "v_mfma_f32_16x16x32_bf16 v[40:43], a[152:155], a[56:59], v[40:43]\n"
      "s_mov_b32 m0, s57\n"
      "v_fma_f32 v36, v36, s59, v28\n"
      "v_fma_f32 v37, v37, s59, v28\n"
      "v_mfma_f32_16x16x32_bf16 v[40:43], a[148:151], a[52:55], v[40:43]\n"
      "s_mov_b64 s[70:71], 0\n"
      "v_fma_f32 v38, v38, s59, v28\n"
      "v_fma_f32 v39, v39, s59, v28\n"
      "v_mfma_f32_16x16x32_bf16 v[40:43], a[144:147], a[48:51], v[40:43]\n"
      "v_exp_f32 v36, v36\n"
      "v_mfma_f32_16x16x32_bf16 v[40:43], a[140:143], a[44:47], v[40:43]\n"
      "buffer_load_dwordx4 v4, s[40:43], s53 offen offset:0 sc1 lds\n"
      "v_mfma_f32_16x16x32_bf16 v[40:43], a[136:139], a[40:43], v[40:43]\n"
      "v_exp_f32 v37, v37\n"
      "v_mfma_f32_16x16x32_bf16 v[40:43], a[132:135], a[36:39], v[40:43]\n"
      "v_exp_f32 v38, v38\n"
      "v_mfma_f32_16x16x32_bf16 v[40:43], a[128:131], a[32:35], v[40:43]\n"
      "s_waitcnt lgkmcnt(6)\n"
      "v_exp_f32 v39, v39\n"
      "v_mfma_f32_16x16x32_bf16 v[160:163], a[160:163], v[64:67], v[160:163]\n"
      "s_nop 0\n"
      "ds_write2st64_b64 v8, v[128:129], v[192:193] offset0:0 offset1:16\n"
      "v_pk_mul_f32 v[156:157], v[156:157], v[24:25]\n"
      "v_pk_mul_f32 v[158:159], v[158:159], v[24:25]\n"
      "v_cvt_pk_bf16_f32 v50, v36, v37\n"
      "v_mfma_f32_16x16x32_bf16 v[224:227], a[160:163], v[68:71], v[224:227]\n"
      "s_nop 0\n"
      "buffer_load_dwordx4 v4, s[40:43], s53 offen offset:1024 sc1 lds\n"
      "s_nop 0\n"
      "v_mfma_f32_16x16x32_bf16 v[112:115], a[160:163], v[72:75], v[112:115]\n"
      "s_waitcnt lgkmcnt(5)\n"
      "ds_read_b64_tr_b16 a[160:161], v2 offset:61440\n"
      "v_add_f32 v32, v32, v36\n"
      "v_add_f32 v32, v32, v37\n"
      "v_cvt_pk_bf16_f32 v51, v38, v39\n"
      "v_add_f32 v32, v32, v38\n"
      "v_pk_mul_f32 v[160:161], v[160:161], v[24:25]\n"
      "v_pk_mul_f32 v[162:163], v[162:163], v[24:25]\n"
      "v_pk_mul_f32 v[224:225], v[224:225], v[26:27]\n"
      "v_pk_mul_f32 v[226:227], v[226:227], v[26:27]\n"
      "v_add_f32 v32, v32, v39\n"
      "v_mfma_f32_16x16x32_bf16 v[116:119], a[164:167], v[72:75], v[116:119]\n"
      "ds_read_b64_tr_b16 a[162:163], v2 offset:63488\n"
      "v_fma_f32 v40, v40, s59, v29\n"
      "v_mfma_f32_16x16x32_bf16 v[228:231], a[164:167], v[68:71], v[228:231]\n"
      "s_nop 0\n"
      "v_fma_f32 v41, v41, s59, v29\n"
      "v_fma_f32 v42, v42, s59, v29\n"
      "v_mfma_f32_16x16x32_bf16 v[164:167], a[164:167], v[64:67], v[164:167]\n"
      "s_waitcnt lgkmcnt(5)\n"
      "ds_read_b64_tr_b16 a[164:165], v3 offset:61440\n"
      "v_fma_f32 v43, v43, s59, v29\n"
      "v_exp_f32 v40, v40\n"
      "v_mov_b32 v47, v46\n"
      "v_pk_mul_f32 v[80:81], v[80:81], v[46:47]\n"
      "v_pk_mul_f32 v[82:83], v[82:83], v[46:47]\n"
      "v_pk_mul_f32 v[96:97], v[96:97], v[46:47]\n"
      "v_pk_mul_f32 v[98:99], v[98:99], v[46:47]\n"
      "v_pk_mul_f32 v[84:85], v[84:85], v[46:47]\n"
      "v_pk_mul_f32 v[86:87], v[86:87], v[46:47]\n"
      "v_pk_mul_f32 v[100:101], v[100:101], v[46:47]\n"
      "v_pk_mul_f32 v[102:103], v[102:103], v[46:47]\n"
      "v_pk_mul_f32 v[88:89], v[88:89], v[46:47]\n"
      "v_pk_mul_f32 v[90:91], v[90:91], v[46:47]\n"
      "v_pk_mul_f32 v[104:105], v[104:105], v[46:47]\n"
      "v_pk_mul_f32 v[106:107], v[106:107], v[46:47]\n"
      "v_pk_mul_f32 v[92:93], v[92:93], v[46:47]\n"
      "v_pk_mul_f32 v[94:95], v[94:95], v[46:47]\n"
      "v_pk_mul_f32 v[108:109], v[108:109], v[46:47]\n"
      "v_pk_mul_f32 v[110:111], v[110:111], v[46:47]\n"
      "v_pk_mul_f32 v[112:113], v[112:113], v[46:47]\n"
      "v_pk_mul_f32 v[114:115], v[114:115], v[46:47]\n"
      "v_pk_mul_f32 v[116:117], v[116:117], v[46:47]\n"
      "v_pk_mul_f32 v[118:119], v[118:119], v[46:47]\n"
      "v_pk_mul_f32 v[164:165], v[164:165], v[24:25]\n"
      "v_pk_mul_f32 v[166:167], v[166:167], v[24:25]\n"
      "v_pk_mul_f32 v[228:229], v[228:229], v[26:27]\n"
      "v_pk_mul_f32 v[230:231], v[230:231], v[26:27]\n"
      "v_add_f32 v33, v33, v40\n"
      "v_mfma_f32_16x16x32_bf16 v[168:171], a[168:171], v[64:67], v[168:171]\n"
      "s_nop 0\n"
      "v_exp_f32 v41, v41\n"
      "v_mfma_f32_16x16x32_bf16 v[232:235], a[168:171], v[68:71], v[232:235]\n"
      "s_nop 0\n"
      "v_exp_f32 v42, v42\n"
      "v_exp_f32 v43, v43\n"
      "v_cvt_pk_bf16_f32 v54, v40, v41\n"
      "v_mfma_f32_16x16x32_bf16 v[120:123], a[168:171], v[72:75], v[120:123]\n"
      "s_waitcnt lgkmcnt(4)\n"
      "ds_read_b64_tr_b16 a[166:167], v3 offset:63488\n"
      "v_add_f32 v33, v33, v41\n"
      "v_cvt_pk_bf16_f32 v55, v42, v43\n"
      "v_add_f32 v33, v33, v42\n"
      "v_add_f32 v33, v33, v43\n"
      "v_pk_mul_f32 v[168:169], v[168:169], v[24:25]\n"
      "v_pk_mul_f32 v[170:171], v[170:171], v[24:25]\n"
      "v_pk_mul_f32 v[232:233], v[232:233], v[26:27]\n"
      "v_pk_mul_f32 v[234:235], v[234:235], v[26:27]\n"
      "v_pk_mul_f32 v[120:121], v[120:121], v[46:47]\n"
      "v_pk_mul_f32 v[122:123], v[122:123], v[46:47]\n"
      "v_cvt_pk_bf16_f32 v80, v80, v81\n"
      "v_mfma_f32_16x16x32_bf16 v[124:127], a[172:175], v[72:75], v[124:127]\n"
      "s_nop 0\n"
      "ds_read_b64_tr_b16 a[168:169], v2 offset:61952\n"
      "v_cvt_pk_bf16_f32 v81, v82, v83\n"
      "v_mfma_f32_16x16x32_bf16 v[236:239], a[172:175], v[68:71], v[236:239]\n"
      "s_nop 0\n"
      "ds_read_b64_tr_b16 a[170:171], v2 offset:64000\n"
      "v_cvt_pk_bf16_f32 v96, v96, v97\n"
      "v_cvt_pk_bf16_f32 v97, v98, v99\n"
      "v_cvt_pk_bf16_f32 v84, v84, v85\n"
      "v_cvt_pk_bf16_f32 v85, v86, v87\n"
      "v_pk_mul_f32 v[124:125], v[124:125], v[46:47]\n"
      "v_pk_mul_f32 v[126:127], v[126:127], v[46:47]\n"
      "v_pk_mul_f32 v[236:237], v[236:237], v[26:27]\n"
      "v_pk_mul_f32 v[238:239], v[238:239], v[26:27]\n"
      "v_cvt_pk_bf16_f32 v100, v100, v101\n"
      "v_mfma_f32_16x16x32_bf16 v[172:175], a[172:175], v[64:67], v[172:175]\n"
      "s_nop 0\n"
      "ds_read_b64_tr_b16 a[172:173], v3 offset:61952\n"
      "v_cvt_pk_bf16_f32 v101, v102, v103\n"
      "v_mfma_f32_16x16x32_bf16 v[36:39], a[128:131], a[64:67], 0\n"
      "s_nop 0\n"
      "ds_read_b64_tr_b16 a[174:175], v3 offset:64000\n"
      "v_cvt_pk_bf16_f32 v132, v132, v133\n"
      "v_mfma_f32_16x16x32_bf16 v[36:39], a[132:135], a[68:71], v[36:39]\n"
      "ds_write2st64_b64 v8, v[80:81], v[96:97] offset0:32 offset1:34\n"
      "v_mfma_f32_16x16x32_bf16 v[36:39], a[136:139], a[72:75], v[36:39]\n"
      "v_cvt_pk_bf16_f32 v133, v134, v135\n"
      "v_cvt_pk_bf16_f32 v196, v196, v197\n"
      "v_mfma_f32_16x16x32_bf16 v[36:39], a[140:143], a[76:79], v[36:39]\n"
      "v_cvt_pk_bf16_f32 v197, v198, v199\n"
      "v_cvt_pk_bf16_f32 v136, v136, v137\n"
      "v_mfma_f32_16x16x32_bf16 v[36:39], a[144:147], a[80:83], v[36:39]\n"
      "ds_write2st64_b64 v9, v[84:85], v[100:101] offset0:32 offset1:34\n"
      "v_mfma_f32_16x16x32_bf16 v[36:39], a[148:151], a[84:87], v[36:39]\n"
      "ds_write2st64_b64 v9, v[132:133], v[196:197] offset0:0 offset1:16\n"
      "v_mfma_f32_16x16x32_bf16 v[36:39], a[152:155], a[88:91], v[36:39]\n"
      "v_cvt_pk_bf16_f32 v137, v138, v139\n"
      "v_cvt_pk_bf16_f32 v200, v200, v201\n"
      "v_mfma_f32_16x16x32_bf16 v[36:39], a[156:159], a[92:95], v[36:39]\n"
      "s_nop 0\n"
      "v_cvt_pk_bf16_f32 v201, v202, v203\n"
      "v_cvt_pk_bf16_f32 v88, v88, v89\n"
      "v_mfma_f32_16x16x32_bf16 v[40:43], a[156:159], a[124:127], 0\n"
      "s_nop 0\n"
      "ds_write2st64_b64 v10, v[136:137], v[200:201] offset0:0 offset1:16\n"
      "v_mfma_f32_16x16x32_bf16 v[40:43], a[152:155], a[120:123], v[40:43]\n"
      "s_nop 0\n"
      "v_fma_f32 v36, v36, s59, v30\n"
      "v_fma_f32 v37, v37, s59, v30\n"
      "v_mfma_f32_16x16x32_bf16 v[40:43], a[148:151], a[116:119], v[40:43]\n"
      "v_fma_f32 v38, v38, s59, v30\n"
      "v_fma_f32 v39, v39, s59, v30\n"
      "v_mfma_f32_16x16x32_bf16 v[40:43], a[144:147], a[112:115], v[40:43]\n"
      "v_exp_f32 v36, v36\n"
      "v_mfma_f32_16x16x32_bf16 v[40:43], a[140:143], a[108:111], v[40:43]\n"
      "v_exp_f32 v37, v37\n"
      "v_mfma_f32_16x16x32_bf16 v[40:43], a[136:139], a[104:107], v[40:43]\n"
      "v_exp_f32 v38, v38\n"
      "v_mfma_f32_16x16x32_bf16 v[40:43], a[132:135], a[100:103], v[40:43]\n"
      "v_exp_f32 v39, v39\n"
      "v_mfma_f32_16x16x32_bf16 v[40:43], a[128:131], a[96:99], v[40:43]\n"
      "s_waitcnt vmcnt(9) lgkmcnt(0)\n"
      "s_barrier\n"
      "v_mfma_f32_16x16x32_bf16 v[176:179], a[160:163], v[64:67], v[176:179]\n"
      "s_mov_b32 m0, s54\n"
      "ds_read_b128 a[128:131], v1 offset:16384\n"
      "v_cvt_pk_bf16_f32 v58, v36, v37\n"
      "v_add_f32 v34, v34, v36\n"
      "v_add_f32 v34, v34, v37\n"
      "v_cvt_pk_bf16_f32 v59, v38, v39\n"
      "v_pk_mul_f32 v[172:173], v[172:173], v[24:25]\n"
      "v_pk_mul_f32 v[174:175], v[174:175], v[24:25]\n"
      "v_pk_mul_f32 v[176:177], v[176:177], v[24:25]\n"
      "v_pk_mul_f32 v[178:179], v[178:179], v[24:25]\n"
      "v_add_f32 v34, v34, v38\n"
      "v_mfma_f32_16x16x32_bf16 v[240:243], a[160:163], v[68:71], v[240:243]\n"
      "s_nop 0\n"
      "ds_read_b128 a[132:135], v1 offset:16896\n"
      "v_add_f32 v34, v34, v39\n"
      "v_mfma_f32_16x16x32_bf16 a[176:179], a[160:163], v[72:75], a[176:179]\n"
      "s_nop 0\n"
      "ds_read_b128 a[136:139], v1 offset:20480\n"
      "v_fma_f32 v40, v40, s59, v31\n"
      "v_mfma_f32_16x16x32_bf16 a[180:183], a[164:167], v[72:75], a[180:183]\n"
      "s_nop 0\n"
      "ds_read_b128 a[140:143], v1 offset:20992\n"
      "v_fma_f32 v41, v41, s59, v31\n"
      "v_mfma_f32_16x16x32_bf16 v[244:247], a[164:167], v[68:71], v[244:247]\n"
      "s_nop 0\n"
      "ds_read_b128 a[144:147], v1 offset:24576\n"
      "v_fma_f32 v42, v42, s59, v31\n"
      "v_fma_f32 v43, v43, s59, v31\n"
      "v_exp_f32 v40, v40\n"
      "v_pk_mul_f32 v[240:241], v[240:241], v[26:27]\n"
      "v_pk_mul_f32 v[242:243], v[242:243], v[26:27]\n"
      "v_add_f32 v35, v35, v40\n"
      "v_pk_mul_f32 v[244:245], v[244:245], v[26:27]\n"
      "v_pk_mul_f32 v[246:247], v[246:247], v[26:27]\n"
      "v_cvt_pk_bf16_f32 v89, v90, v91\n"
      "v_mfma_f32_16x16x32_bf16 v[180:183], a[164:167], v[64:67], v[180:183]\n"
      "s_nop 0\n"
      "ds_read_b128 a[148:151], v1 offset:25088\n"
      "v_cvt_pk_bf16_f32 v104, v104, v105\n"
      "v_mfma_f32_16x16x32_bf16 v[184:187], a[168:171], v[64:67], v[184:187]\n"
      "s_nop 0\n"
      "ds_read_b128 a[152:155], v1 offset:28672\n"
      "v_exp_f32 v41, v41\n"
      "v_exp_f32 v42, v42\n"
      "v_pk_mul_f32 v[180:181], v[180:181], v[24:25]\n"
      "v_pk_mul_f32 v[182:183], v[182:183], v[24:25]\n"
      "v_cvt_pk_bf16_f32 v62, v40, v41\n"
      "v_mfma_f32_16x16x32_bf16 v[248:251], a[168:171], v[68:71], v[248:251]\n"
      "s_nop 0\n"
      "ds_read_b128 a[156:159], v1 offset:29184\n"
      "v_exp_f32 v43, v43\n"
      "v_add_f32 v35, v35, v41\n"
      "v_cvt_pk_bf16_f32 v63, v42, v43\n"
      "v_pk_mul_f32 v[184:185], v[184:185], v[24:25]\n"
      "v_pk_mul_f32 v[186:187], v[186:187], v[24:25]\n"
      "v_add_f32 v35, v35, v42\n"
      "v_pk_mul_f32 v[248:249], v[248:249], v[26:27]\n"
      "v_pk_mul_f32 v[250:251], v[250:251], v[26:27]\n"
      "v_add_f32 v35, v35, v43\n"
      "v_mfma_f32_16x16x32_bf16 a[184:187], a[168:171], v[72:75], a[184:187]\n"
      "ds_read_b64_tr_b16 a[160:161], v2 offset:32768\n"
      "v_cvt_pk_bf16_f32 v105, v106, v107\n"
      "v_mfma_f32_16x16x32_bf16 a[188:191], a[172:175], v[72:75], a[188:191]\n"
      "ds_read_b64_tr_b16 a[162:163], v2 offset:34816\n"
      "v_cvt_pk_bf16_f32 v92, v92, v93\n"
      "v_mfma_f32_16x16x32_bf16 v[252:255], a[172:175], v[68:71], v[252:255]\n"
      "s_nop 0\n"
      "ds_read_b64_tr_b16 a[164:165], v3 offset:32768\n"
      "v_cvt_pk_bf16_f32 v93, v94, v95\n"
      "v_mfma_f32_16x16x32_bf16 v[188:191], a[172:175], v[64:67], v[188:191]\n"
      "s_waitcnt lgkmcnt(10)\n"
      "ds_write2st64_b64 v10, v[88:89], v[104:105] offset0:32 offset1:34\n"
      "v_mfma_f32_16x16x32_bf16 v[36:39], a[128:131], a[0:3], 0\n"
      "s_waitcnt lgkmcnt(10)\n"
      "ds_read_b64_tr_b16 a[166:167], v3 offset:34816\n"
      "v_cvt_pk_bf16_f32 v108, v108, v109\n"
      "v_cvt_pk_bf16_f32 v109, v110, v111\n"
      "v_cvt_pk_bf16_f32 v140, v140, v141\n"
      "v_cvt_pk_bf16_f32 v141, v142, v143\n"
      "v_pk_mul_f32 v[188:189], v[188:189], v[24:25]\n"
      "v_pk_mul_f32 v[190:191], v[190:191], v[24:25]\n"
      "v_pk_mul_f32 v[252:253], v[252:253], v[26:27]\n"
      "v_pk_mul_f32 v[254:255], v[254:255], v[26:27]\n"
      "v_cvt_pk_bf16_f32 v204, v204, v205\n"
      "v_mfma_f32_16x16x32_bf16 v[36:39], a[132:135], a[4:7], v[36:39]\n"
      "s_waitcnt lgkmcnt(10)\n"
      "ds_read_b64_tr_b16 a[168:169], v2 offset:33280\n"
      "v_cvt_pk_bf16_f32 v205, v206, v207\n"
      "v_mfma_f32_16x16x32_bf16 v[36:39], a[136:139], a[8:11], v[36:39]\n"
      "s_waitcnt lgkmcnt(10)\n"
      "ds_read_b64_tr_b16 a[170:171], v2 offset:35328\n"
      "v_cvt_pk_bf16_f32 v144, v144, v145\n"
      "v_mfma_f32_16x16x32_bf16 v[36:39], a[140:143], a[12:15], v[36:39]\n"
      "s_waitcnt lgkmcnt(10)\n"
      "ds_read_b64_tr_b16 a[172:173], v3 offset:33280\n"
      "v_cvt_pk_bf16_f32 v145, v146, v147\n"
      "v_mfma_f32_16x16x32_bf16 v[36:39], a[144:147], a[16:19], v[36:39]\n"
      "s_waitcnt lgkmcnt(10)\n"
      "buffer_load_dwordx4 v4, s[36:39], s52 offen offset:2048 sc1 lds\n"
      "ds_read_b64_tr_b16 a[174:175], v3 offset:35328\n"
      "v_mfma_f32_16x16x32_bf16 v[36:39], a[148:151], a[20:23], v[36:39]\n"
      "s_waitcnt lgkmcnt(10)\n"
      "ds_write2st64_b64 v11, v[92:93], v[108:109] offset0:32 offset1:34\n"
      "v_mfma_f32_16x16x32_bf16 v[36:39], a[152:155], a[24:27], v[36:39]\n"
      "s_waitcnt lgkmcnt(10)\n"
      "ds_write2st64_b64 v11, v[140:141], v[204:205] offset0:0 offset1:16\n"
      "v_mfma_f32_16x16x32_bf16 v[36:39], a[156:159], a[28:31], v[36:39]\n"
      "s_nop 0\n"
      "v_cvt_pk_bf16_f32 v208, v208, v209\n"
      "v_cvt_pk_bf16_f32 v209, v210, v211\n"
      "v_mfma_f32_16x16x32_bf16 v[40:43], a[156:159], a[60:63], 0\n"
      "s_nop 0\n"
      "buffer_load_dwordx4 v4, s[36:39], s52 offen offset:3072 sc1 lds\n"
      "s_nop 0\n"
      "v_mfma_f32_16x16x32_bf16 v[40:43], a[152:155], a[56:59], v[40:43]\n"
      "s_add_u32 m0, s57, 0x2000\n"
      "v_fma_f32 v36, v36, s59, v28\n"
      "v_fma_f32 v37, v37, s59, v28\n"
      "v_mfma_f32_16x16x32_bf16 v[40:43], a[148:151], a[52:55], v[40:43]\n"
      "v_fma_f32 v38, v38, s59, v28\n"
      "v_fma_f32 v39, v39, s59, v28\n"
      "v_mfma_f32_16x16x32_bf16 v[40:43], a[144:147], a[48:51], v[40:43]\n"
      "v_exp_f32 v36, v36\n"
      "v_mfma_f32_16x16x32_bf16 v[40:43], a[140:143], a[44:47], v[40:43]\n"
      "buffer_load_dwordx4 v4, s[40:43], s90 offen offset:0 sc1 lds\n"
      "v_mfma_f32_16x16x32_bf16 v[40:43], a[136:139], a[40:43], v[40:43]\n"
      "v_exp_f32 v37, v37\n"
      "v_mfma_f32_16x16x32_bf16 v[40:43], a[132:135], a[36:39], v[40:43]\n"
      "v_exp_f32 v38, v38\n"
      "v_mfma_f32_16x16x32_bf16 v[40:43], a[128:131], a[32:35], v[40:43]\n"
      "s_waitcnt lgkmcnt(9)\n"
      "v_exp_f32 v39, v39\n"
      "v_mfma_f32_16x16x32_bf16 v[128:131], a[160:163], v[48:51], 0\n"
      "s_nop 0\n"
      "buffer_load_dwordx4 v4, s[40:43], s90 offen offset:1024 sc1 lds\n"
      "s_nop 0\n"
      "v_mfma_f32_16x16x32_bf16 v[192:195], a[160:163], v[52:55], 0\n"
      "s_mov_b32 m0, s96\n"
      "v_cvt_pk_bf16_f32 v64, v36, v37\n"
      "v_add_f32 v32, v32, v36\n"
      "v_mfma_f32_16x16x32_bf16 v[80:83], a[160:163], v[56:59], 0\n"
      "v_add_f32 v32, v32, v37\n"
      "v_cvt_pk_bf16_f32 v65, v38, v39\n"
      "v_mfma_f32_16x16x32_bf16 a[192:195], a[160:163], v[60:63], 0\n"
      "s_waitcnt lgkmcnt(6)\n"
      "ds_read_b64_tr_b16 a[160:161], v2 offset:36864\n"
      "v_add_f32 v32, v32, v38\n"
      "v_mfma_f32_16x16x32_bf16 a[196:199], a[164:167], v[60:63], 0\n"
      "ds_read_b64_tr_b16 a[162:163], v2 offset:38912\n"
      "v_add_f32 v32, v32, v39\n"
      "v_mfma_f32_16x16x32_bf16 v[84:87], a[164:167], v[56:59], 0\n"
      "v_fma_f32 v40, v40, s59, v29\n"
      "v_fma_f32 v41, v41, s59, v29\n"
      "v_mfma_f32_16x16x32_bf16 v[196:199], a[164:167], v[52:55], 0\n"
      "v_fma_f32 v42, v42, s59, v29\n"
      "v_fma_f32 v43, v43, s59, v29\n"
      "v_mfma_f32_16x16x32_bf16 v[132:135], a[164:167], v[48:51], 0\n"
      "s_waitcnt lgkmcnt(6)\n"
      "v_exp_f32 v40, v40\n"
      "v_mfma_f32_16x16x32_bf16 v[136:139], a[168:171], v[48:51], 0\n"
      "v_exp_f32 v41, v41\n"
      "v_mfma_f32_16x16x32_bf16 v[200:203], a[168:171], v[52:55], 0\n"
      "v_exp_f32 v42, v42\n"
      "v_mfma_f32_16x16x32_bf16 v[88:91], a[168:171], v[56:59], 0\n"
      "v_exp_f32 v43, v43\n"
      "v_mfma_f32_16x16x32_bf16 a[200:203], a[168:171], v[60:63], 0\n"
      "s_waitcnt lgkmcnt(4)\n"
      "ds_read_b64_tr_b16 a[164:165], v3 offset:36864\n"
      "v_cvt_pk_bf16_f32 v68, v40, v41\n"
      "v_mfma_f32_16x16x32_bf16 a[204:207], a[172:175], v[60:63], 0\n"
      "ds_read_b64_tr_b16 a[166:167], v3 offset:38912\n"
      "v_add_f32 v33, v33, v40\n"
      "v_mfma_f32_16x16x32_bf16 v[92:95], a[172:175], v[56:59], 0\n"
      "ds_read_b64_tr_b16 a[168:169], v2 offset:37376\n"
      "v_add_f32 v33, v33, v41\n"
      "v_mfma_f32_16x16x32_bf16 v[204:207], a[172:175], v[52:55], 0\n"
      "ds_read_b64_tr_b16 a[170:171], v2 offset:39424\n"
      "v_cvt_pk_bf16_f32 v69, v42, v43\n"
      "v_mfma_f32_16x16x32_bf16 v[140:143], a[172:175], v[48:51], 0\n"
      "ds_read_b64_tr_b16 a[172:173], v3 offset:37376\n"
      "v_add_f32 v33, v33, v42\n"
      "v_mfma_f32_16x16x32_bf16 v[36:39], a[128:131], a[64:67], 0\n"
      "ds_read_b64_tr_b16 a[174:175], v3 offset:39424\n"
      "v_add_f32 v33, v33, v43\n"
      "v_mfma_f32_16x16x32_bf16 v[36:39], a[132:135], a[68:71], v[36:39]\n"
      "ds_write2st64_b64 v8, v[144:145], v[208:209] offset0:2 offset1:18\n"
      "v_mfma_f32_16x16x32_bf16 v[36:39], a[136:139], a[72:75], v[36:39]\n"
      "buffer_load_dwordx4 v5, s[44:47], s97 offen offset:0 nt lds\n"
      "v_mfma_f32_16x16x32_bf16 v[36:39], a[140:143], a[76:79], v[36:39]\n"
      "v_cvt_pk_bf16_f32 v148, v148, v149\n"
      "v_cvt_pk_bf16_f32 v149, v150, v151\n"
      "v_mfma_f32_16x16x32_bf16 v[36:39], a[144:147], a[80:83], v[36:39]\n"
      "v_cvt_pk_bf16_f32 v212, v212, v213\n"
      "v_cvt_pk_bf16_f32 v213, v214, v215\n"
      "v_mfma_f32_16x16x32_bf16 v[36:39], a[148:151], a[84:87], v[36:39]\n"
      "ds_write2st64_b64 v9, v[148:149], v[212:213] offset0:2 offset1:18\n"
      "v_mfma_f32_16x16x32_bf16 v[36:39], a[152:155], a[88:91], v[36:39]\n"
      "v_cvt_pk_bf16_f32 v152, v152, v153\n"
      "v_cvt_pk_bf16_f32 v153, v154, v155\n"
      "v_mfma_f32_16x16x32_bf16 v[36:39], a[156:159], a[92:95], v[36:39]\n"
      "s_nop 0\n"
      "v_cvt_pk_bf16_f32 v216, v216, v217\n"
      "v_cvt_pk_bf16_f32 v217, v218, v219\n"
      "v_mfma_f32_16x16x32_bf16 v[40:43], a[156:159], a[124:127], 0\n"
      "s_nop 0\n"
      "ds_write2st64_b64 v10, v[152:153], v[216:217] offset0:2 offset1:18\n"
      "v_mfma_f32_16x16x32_bf16 v[40:43], a[152:155], a[120:123], v[40:43]\n"
      "s_nop 0\n"
      "v_cvt_pk_bf16_f32 v156, v156, v157\n"
      "v_cvt_pk_bf16_f32 v157, v158, v159\n"
      "v_mfma_f32_16x16x32_bf16 v[40:43], a[148:151], a[116:119], v[40:43]\n"
      "v_cvt_pk_bf16_f32 v220, v220, v221\n"
      "v_cvt_pk_bf16_f32 v221, v222, v223\n"
      "v_mfma_f32_16x16x32_bf16 v[40:43], a[144:147], a[112:115], v[40:43]\n"
      "ds_write2st64_b64 v11, v[156:157], v[220:221] offset0:2 offset1:18\n"
      "v_mfma_f32_16x16x32_bf16 v[40:43], a[140:143], a[108:111], v[40:43]\n"
      "v_fma_f32 v36, v36, s59, v30\n"
      "v_fma_f32 v37, v37, s59, v30\n"
      "v_mfma_f32_16x16x32_bf16 v[40:43], a[136:139], a[104:107], v[40:43]\n"
      "v_fma_f32 v38, v38, s59, v30\n"
      "v_fma_f32 v39, v39, s59, v30\n"
      "v_mfma_f32_16x16x32_bf16 v[40:43], a[132:135], a[100:103], v[40:43]\n"
      "v_exp_f32 v36, v36\n"
      "v_mfma_f32_16x16x32_bf16 v[40:43], a[128:131], a[96:99], v[40:43]\n"
      "s_waitcnt vmcnt(9) lgkmcnt(0)\n"
      "s_barrier\n"
      "v_mfma_f32_16x16x32_bf16 v[144:147], a[160:163], v[48:51], 0\n"
      "s_add_u32 s52, s52, 0x4000\n"
      "ds_read_b128 a[128:131], v1 offset:18432\n"
      "v_add_f32 v34, v34, v36\n"
      "v_mfma_f32_16x16x32_bf16 v[208:211], a[160:163], v[52:55], 0\n"
      "s_mov_b32 m0, s55\n"
      "ds_read_b128 a[132:135], v1 offset:18944\n"
      "v_fma_f32 v40, v40, s59, v31\n"
      "v_mfma_f32_16x16x32_bf16 v[96:99], a[160:163], v[56:59], 0\n"
      "s_add_u32 s53, s53, 0x4000\n"
      "ds_read_b128 a[136:139], v1 offset:22528\n"
      "v_fma_f32 v41, v41, s59, v31\n"
      "v_mfma_f32_16x16x32_bf16 a[208:211], a[160:163], v[60:63], 0\n"
      "s_add_u32 s90, s90, 0x4000\n"
      "ds_read_b128 a[140:143], v1 offset:23040\n"
      "v_fma_f32 v42, v42, s59, v31\n"
      "v_mfma_f32_16x16x32_bf16 a[212:215], a[164:167], v[60:63], 0\n"
      "ds_read_b128 a[144:147], v1 offset:26624\n"
      "v_fma_f32 v43, v43, s59, v31\n"
      "v_mfma_f32_16x16x32_bf16 v[100:103], a[164:167], v[56:59], 0\n"
      "ds_read_b128 a[148:151], v1 offset:27136\n"
      "v_cvt_pk_bf16_f32 v160, v160, v161\n"
      "v_mfma_f32_16x16x32_bf16 v[212:215], a[164:167], v[52:55], 0\n"
      "ds_read_b128 a[152:155], v1 offset:30720\n"
      "v_cvt_pk_bf16_f32 v161, v162, v163\n"
      "v_mfma_f32_16x16x32_bf16 v[148:151], a[164:167], v[48:51], 0\n"
      "v_exp_f32 v37, v37\n"
      "v_mfma_f32_16x16x32_bf16 v[152:155], a[168:171], v[48:51], 0\n"
      "v_exp_f32 v38, v38\n"
      "v_mfma_f32_16x16x32_bf16 v[216:219], a[168:171], v[52:55], 0\n"
      "v_exp_f32 v39, v39\n"
      "v_mfma_f32_16x16x32_bf16 v[104:107], a[168:171], v[56:59], 0\n"
      "ds_read_b128 a[156:159], v1 offset:31232\n"
      "v_cvt_pk_bf16_f32 v72, v36, v37\n"
      "v_mfma_f32_16x16x32_bf16 a[216:219], a[168:171], v[60:63], 0\n"
      "ds_read_b64_tr_b16 a[160:161], v2 offset:40960\n"
      "v_add_f32 v34, v34, v37\n"
      "v_mfma_f32_16x16x32_bf16 a[220:223], a[172:175], v[60:63], 0\n"
      "ds_read_b64_tr_b16 a[162:163], v2 offset:43008\n"
      "v_cvt_pk_bf16_f32 v73, v38, v39\n"
      "v_mfma_f32_16x16x32_bf16 v[108:111], a[172:175], v[56:59], 0\n"
      "ds_read_b64_tr_b16 a[164:165], v3 offset:40960\n"
      "v_add_f32 v34, v34, v38\n"
      "v_mfma_f32_16x16x32_bf16 v[220:223], a[172:175], v[52:55], 0\n"
      "ds_read_b64_tr_b16 a[166:167], v3 offset:43008\n"
      "v_add_f32 v34, v34, v39\n"
      "v_mfma_f32_16x16x32_bf16 v[156:159], a[172:175], v[48:51], 0\n"
      "s_waitcnt lgkmcnt(11)\n"
      "v_exp_f32 v40, v40\n"
      "v_mfma_f32_16x16x32_bf16 v[36:39], a[128:131], a[0:3], 0\n"
      "s_waitcnt lgkmcnt(10)\n"
      "buffer_load_dwordx4 v4, s[36:39], s52 offen offset:0 sc1 lds\n"
      "ds_read_b64_tr_b16 a[168:169], v2 offset:41472\n"
      "v_mfma_f32_16x16x32_bf16 v[36:39], a[132:135], a[4:7], v[36:39]\n"
      "s_waitcnt lgkmcnt(10)\n"
      "v_exp_f32 v41, v41\n"
      "v_mfma_f32_16x16x32_bf16 v[36:39], a[136:139], a[8:11], v[36:39]\n"
      "s_waitcnt lgkmcnt(9)\n"
      "v_exp_f32 v42, v42\n"
      "v_mfma_f32_16x16x32_bf16 v[36:39], a[140:143], a[12:15], v[36:39]\n"
      "s_waitcnt lgkmcnt(8)\n"
      "v_exp_f32 v43, v43\n"
      "v_mfma_f32_16x16x32_bf16 v[36:39], a[144:147], a[16:19], v[36:39]\n"
      "s_waitcnt lgkmcnt(7)\n"
      "buffer_load_dwordx4 v4, s[36:39], s52 offen offset:1024 sc1 lds\n"
      "ds_read_b64_tr_b16 a[170:171], v2 offset:43520\n"
      "v_mfma_f32_16x16x32_bf16 v[36:39], a[148:151], a[20:23], v[36:39]\n"
      "s_waitcnt lgkmcnt(7)\n"
      "ds_read_b64_tr_b16 a[172:173], v3 offset:41472\n"
      "v_cvt_pk_bf16_f32 v76, v40, v41\n"
      "v_mfma_f32_16x16x32_bf16 v[36:39], a[152:155], a[24:27], v[36:39]\n"
      "s_waitcnt lgkmcnt(7)\n"
      "ds_read_b64_tr_b16 a[174:175], v3 offset:43520\n"
      "v_add_f32 v35, v35, v40\n"
      "v_add_f32 v35, v35, v41\n"
      "v_mfma_f32_16x16x32_bf16 v[36:39], a[156:159], a[28:31], v[36:39]\n"
      "s_mov_b32 m0, s56\n"
      "v_cvt_pk_bf16_f32 v77, v42, v43\n"
      "v_add_f32 v35, v35, v42\n"
      "v_add_f32 v35, v35, v43\n"
      "v_mfma_f32_16x16x32_bf16 v[40:43], a[156:159], a[60:63], 0\n"
      "s_nop 0\n"
      "buffer_load_dwordx4 v4, s[40:43], s53 offen offset:0 sc1 lds\n"
      "s_nop 0\n"
      "v_mfma_f32_16x16x32_bf16 v[40:43], a[152:155], a[56:59], v[40:43]\n"
      "v_cvt_pk_bf16_f32 v224, v224, v225\n"
      "v_cvt_pk_bf16_f32 v225, v226, v227\n"
      "v_mfma_f32_16x16x32_bf16 v[40:43], a[148:151], a[52:55], v[40:43]\n"
      "ds_write2st64_b64 v8, v[160:161], v[224:225] offset0:4 offset1:20\n"
      "v_mfma_f32_16x16x32_bf16 v[40:43], a[144:147], a[48:51], v[40:43]\n"
      "v_cvt_pk_bf16_f32 v112, v112, v113\n"
      "v_cvt_pk_bf16_f32 v113, v114, v115\n"
      "v_accvgpr_read_b32 v20, a176\n"
      "v_mfma_f32_16x16x32_bf16 v[40:43], a[140:143], a[44:47], v[40:43]\n"
      "buffer_load_dwordx4 v4, s[40:43], s53 offen offset:1024 sc1 lds\n"
      "v_mfma_f32_16x16x32_bf16 v[40:43], a[136:139], a[40:43], v[40:43]\n"
      "s_mov_b32 m0, s96\n"
      "v_accvgpr_read_b32 v21, a177\n"
      "v_accvgpr_read_b32 v22, a178\n"
      "v_accvgpr_read_b32 v23, a179\n"
      "v_cvt_pk_bf16_f32 v116, v116, v117\n"
      "v_mfma_f32_16x16x32_bf16 v[40:43], a[132:135], a[36:39], v[40:43]\n"
      "v_cvt_pk_bf16_f32 v117, v118, v119\n"
      "v_accvgpr_read_b32 v16, a180\n"
      "v_accvgpr_read_b32 v17, a181\n"
      "v_accvgpr_read_b32 v18, a182\n"
      "v_mfma_f32_16x16x32_bf16 v[40:43], a[128:131], a[32:35], v[40:43]\n"
      "s_waitcnt lgkmcnt(7)\n"
      "v_accvgpr_read_b32 v19, a183\n"
      "v_cvt_pk_bf16_f32 v164, v164, v165\n"
      "v_cvt_pk_bf16_f32 v165, v166, v167\n"
      "v_cvt_pk_bf16_f32 v228, v228, v229\n"
      "v_cvt_pk_bf16_f32 v229, v230, v231\n"
      "v_pk_mul_f32 v[20:21], v[20:21], v[46:47]\n"
      "v_pk_mul_f32 v[22:23], v[22:23], v[46:47]\n"
      "v_pk_mul_f32 v[16:17], v[16:17], v[46:47]\n"
      "v_pk_mul_f32 v[18:19], v[18:19], v[46:47]\n"
      "v_cvt_pk_bf16_f32 v20, v20, v21\n"
      "v_mfma_f32_16x16x32_bf16 v[160:163], a[160:163], v[48:51], 0\n"
      "v_cvt_pk_bf16_f32 v21, v22, v23\n"
      "v_cvt_pk_bf16_f32 v16, v16, v17\n"
      "v_cvt_pk_bf16_f32 v17, v18, v19\n"
      "v_cvt_pk_bf16_f32 v168, v168, v169\n"
      "v_mfma_f32_16x16x32_bf16 v[224:227], a[160:163], v[52:55], 0\n"
      "ds_write2st64_b64 v8, v[112:113], v[20:21] offset0:36 offset1:38\n"
      "v_mfma_f32_16x16x32_bf16 v[112:115], a[160:163], v[56:59], 0\n"
      "ds_write2st64_b64 v9, v[116:117], v[16:17] offset0:36 offset1:38\n"
      "v_mfma_f32_16x16x32_bf16 a[224:227], a[160:163], v[60:63], 0\n"
      "s_waitcnt lgkmcnt(7)\n"
      "ds_write2st64_b64 v9, v[164:165], v[228:229] offset0:4 offset1:20\n"
      "v_mfma_f32_16x16x32_bf16 a[228:231], a[164:167], v[60:63], 0\n"
      "ds_read_b64_tr_b16 a[160:161], v2 offset:45056\n"
      "v_cvt_pk_bf16_f32 v169, v170, v171\n"
      "v_cvt_pk_bf16_f32 v232, v232, v233\n"
      "v_cvt_pk_bf16_f32 v233, v234, v235\n"
      "v_mfma_f32_16x16x32_bf16 v[116:119], a[164:167], v[56:59], 0\n"
      "ds_write2st64_b64 v10, v[168:169], v[232:233] offset0:4 offset1:20\n"
      "v_mfma_f32_16x16x32_bf16 v[228:231], a[164:167], v[52:55], 0\n"
      "ds_read_b64_tr_b16 a[162:163], v2 offset:47104\n"
      "v_cvt_pk_bf16_f32 v120, v120, v121\n"
      "v_cvt_pk_bf16_f32 v121, v122, v123\n"
      "v_accvgpr_read_b32 v20, a184\n"
      "v_accvgpr_read_b32 v21, a185\n"
      "v_mfma_f32_16x16x32_bf16 v[164:167], a[164:167], v[48:51], 0\n"
      "s_waitcnt lgkmcnt(9)\n"
      "ds_read_b64_tr_b16 a[164:165], v3 offset:45056\n"
      "v_accvgpr_read_b32 v22, a186\n"
      "v_accvgpr_read_b32 v23, a187\n"
      "v_cvt_pk_bf16_f32 v124, v124, v125\n"
      "v_cvt_pk_bf16_f32 v125, v126, v127\n"
      "v_pk_mul_f32 v[20:21], v[20:21], v[46:47]\n"
      "v_pk_mul_f32 v[22:23], v[22:23], v[46:47]\n"
      "v_cvt_pk_bf16_f32 v20, v20, v21\n"
      "v_mfma_f32_16x16x32_bf16 v[168:171], a[168:171], v[48:51], 0\n"
      "ds_read_b64_tr_b16 a[166:167], v3 offset:47104\n"
      "v_cvt_pk_bf16_f32 v21, v22, v23\n"
      "v_accvgpr_read_b32 v16, a188\n"
      "v_accvgpr_read_b32 v17, a189\n"
      "v_accvgpr_read_b32 v18, a190\n"
      "v_accvgpr_read_b32 v19, a191\n"
      "v_mfma_f32_16x16x32_bf16 v[232:235], a[168:171], v[52:55], 0\n"
      "ds_write2st64_b64 v10, v[120:121], v[20:21] offset0:36 offset1:38\n"
      "v_pk_mul_f32 v[16:17], v[16:17], v[46:47]\n"
      "v_cvt_pk_bf16_f32 v16, v16, v17\n"
      "v_mfma_f32_16x16x32_bf16 v[120:123], a[168:171], v[56:59], 0\n"
      "v_cvt_pk_bf16_f32 v172, v172, v173\n"
      "v_cvt_pk_bf16_f32 v173, v174, v175\n"
      "v_cvt_pk_bf16_f32 v236, v236, v237\n"
      "v_cvt_pk_bf16_f32 v237, v238, v239\n"
      "v_fma_f32 v36, v36, s59, v28\n"
      "v_pk_mul_f32 v[18:19], v[18:19], v[46:47]\n"
      "v_cvt_pk_bf16_f32 v17, v18, v19\n"
      "v_mfma_f32_16x16x32_bf16 a[232:235], a[168:171], v[60:63], 0\n"
      "s_waitcnt lgkmcnt(10)\n"
      "ds_write2st64_b64 v11, v[124:125], v[16:17] offset0:36 offset1:38\n"
      "v_fma_f32 v37, v37, s59, v28\n"
      "v_mfma_f32_16x16x32_bf16 a[236:239], a[172:175], v[60:63], 0\n"
      "ds_write2st64_b64 v11, v[172:173], v[236:237] offset0:4 offset1:20\n"
      "v_fma_f32 v38, v38, s59, v28\n"
      "v_mfma_f32_16x16x32_bf16 v[124:127], a[172:175], v[56:59], 0\n"
      "ds_read_b64_tr_b16 a[168:169], v2 offset:45568\n"
      "v_fma_f32 v39, v39, s59, v28\n"
      "v_exp_f32 v36, v36\n"
      "v_fma_f32 v40, v40, s59, v29\n"
      "v_mfma_f32_16x16x32_bf16 v[236:239], a[172:175], v[52:55], 0\n"
      "v_exp_f32 v37, v37\n"
      "v_exp_f32 v38, v38\n"
      "v_exp_f32 v39, v39\n"
      "v_mfma_f32_16x16x32_bf16 v[172:175], a[172:175], v[48:51], 0\n"
      "ds_read_b64_tr_b16 a[170:171], v2 offset:47616\n"
      "v_cvt_pk_bf16_f32 v66, v36, v37\n"
      "v_add_f32 v32, v32, v36\n"
      "v_add_f32 v32, v32, v37\n"
      "v_cvt_pk_bf16_f32 v67, v38, v39\n"
      "v_add_f32 v32, v32, v38\n"
      "v_add_f32 v32, v32, v39\n"
      "v_mfma_f32_16x16x32_bf16 v[36:39], a[128:131], a[64:67], 0\n"
      "ds_read_b64_tr_b16 a[172:173], v3 offset:45568\n"
      "v_fma_f32 v41, v41, s59, v29\n"
      "v_fma_f32 v42, v42, s59, v29\n"
      "v_fma_f32 v43, v43, s59, v29\n"
      "v_mfma_f32_16x16x32_bf16 v[36:39], a[132:135], a[68:71], v[36:39]\n"
      "v_exp_f32 v40, v40\n"
      "v_exp_f32 v41, v41\n"
      "v_mfma_f32_16x16x32_bf16 v[36:39], a[136:139], a[72:75], v[36:39]\n"
      "v_exp_f32 v42, v42\n"
      "v_exp_f32 v43, v43\n"
      "v_mfma_f32_16x16x32_bf16 v[36:39], a[140:143], a[76:79], v[36:39]\n"
      "ds_read_b64_tr_b16 a[174:175], v3 offset:47616\n"
      "v_cvt_pk_bf16_f32 v70, v40, v41\n"
      "v_add_f32 v33, v33, v40\n"
      "v_add_f32 v33, v33, v41\n"
      "v_mfma_f32_16x16x32_bf16 v[36:39], a[144:147], a[80:83], v[36:39]\n"
      "v_cvt_pk_bf16_f32 v71, v42, v43\n"
      "v_add_f32 v33, v33, v42\n"
      "v_add_f32 v33, v33, v43\n"
      "v_cvt_pk_bf16_f32 v176, v176, v177\n"
      "v_mfma_f32_16x16x32_bf16 v[36:39], a[148:151], a[84:87], v[36:39]\n"
      "v_cvt_pk_bf16_f32 v177, v178, v179\n"
      "v_cvt_pk_bf16_f32 v240, v240, v241\n"
      "v_cvt_pk_bf16_f32 v241, v242, v243\n"
      "v_cvt_pk_bf16_f32 v180, v180, v181\n"
      "v_mfma_f32_16x16x32_bf16 v[36:39], a[152:155], a[88:91], v[36:39]\n"
      "ds_write2st64_b64 v8, v[176:177], v[240:241] offset0:6 offset1:22\n"
      "v_mfma_f32_16x16x32_bf16 v[36:39], a[156:159], a[92:95], v[36:39]\n"
      "s_nop 0\n"
      "v_cvt_pk_bf16_f32 v181, v182, v183\n"
      "v_cvt_pk_bf16_f32 v244, v244, v245\n"
      "v_cvt_pk_bf16_f32 v245, v246, v247\n"
      "v_cvt_pk_bf16_f32 v184, v184, v185\n"
      "v_mfma_f32_16x16x32_bf16 v[40:43], a[156:159], a[124:127], 0\n"
      "s_nop 0\n"
      "ds_write2st64_b64 v9, v[180:181], v[244:245] offset0:6 offset1:22\n"
      "v_mfma_f32_16x16x32_bf16 v[40:43], a[152:155], a[120:123], v[40:43]\n"
      "v_cvt_pk_bf16_f32 v185, v186, v187\n"
      "v_cvt_pk_bf16_f32 v248, v248, v249\n"
      "v_cvt_pk_bf16_f32 v249, v250, v251\n"
      "v_cvt_pk_bf16_f32 v188, v188, v189\n"
      "v_mfma_f32_16x16x32_bf16 v[40:43], a[148:151], a[116:119], v[40:43]\n"
      "ds_write2st64_b64 v10, v[184:185], v[248:249] offset0:6 offset1:22\n"
      "v_mfma_f32_16x16x32_bf16 v[40:43], a[144:147], a[112:115], v[40:43]\n"
      "v_cvt_pk_bf16_f32 v189, v190, v191\n"
      "v_cvt_pk_bf16_f32 v252, v252, v253\n"
      "v_cvt_pk_bf16_f32 v253, v254, v255\n"
      "v_fma_f32 v36, v36, s59, v30\n"
      "v_mfma_f32_16x16x32_bf16 v[40:43], a[140:143], a[108:111], v[40:43]\n"
      "v_fma_f32 v37, v37, s59, v30\n"
      "v_fma_f32 v38, v38, s59, v30\n"
      "v_fma_f32 v39, v39, s59, v30\n"
      "v_mfma_f32_16x16x32_bf16 v[40:43], a[136:139], a[104:107], v[40:43]\n"
      "ds_write2st64_b64 v11, v[188:189], v[252:253] offset0:6 offset1:22\n"
      "v_mfma_f32_16x16x32_bf16 v[40:43], a[132:135], a[100:103], v[40:43]\n"
      "v_exp_f32 v36, v36\n"
      "v_mfma_f32_16x16x32_bf16 v[40:43], a[128:131], a[96:99], v[40:43]\n"
      "v_exp_f32 v37, v37\n"
      "buffer_load_dwordx4 v5, s[44:47], s97 offen offset:0 nt lds\n"
      "s_waitcnt vmcnt(10) lgkmcnt(0)\n"
      "s_barrier\n"
      "v_mfma_f32_16x16x32_bf16 v[176:179], a[160:163], v[48:51], 0\n"
      "s_lshl_b32 s78, s61, 12\n"
      "ds_read_b128 v[16:19], v6 offset:0\n"
      "v_cvt_pk_bf16_f32 v74, v36, v37\n"
      "v_mfma_f32_16x16x32_bf16 v[240:243], a[160:163], v[52:55], 0\n"
      "s_add_u32 s76, s91, s78\n"
      "ds_read_b128 a[128:131], v1 offset:0\n"
      "v_add_f32 v34, v34, v36\n"
      "v_mfma_f32_16x16x32_bf16 a[176:179], a[160:163], v[56:59], 0\n"
      "s_add_u32 s74, s58, s78\n"
      "ds_read_b128 a[132:135], v1 offset:512\n"
      "v_add_f32 v34, v34, v37\n"
      "v_mfma_f32_16x16x32_bf16 a[240:243], a[160:163], v[60:63], 0\n"
      "s_lshr_b32 s78, s61, 1\n"
      "ds_read_b128 a[136:139], v1 offset:4096\n"
      "v_fma_f32 v40, v40, s59, v31\n"
      "v_mfma_f32_16x16x32_bf16 a[244:247], a[164:167], v[60:63], 0\n"
      "s_lshl_b32 s78, s78, 13\n"
      "ds_read_b128 a[140:143], v1 offset:4608\n"
      "v_fma_f32 v41, v41, s59, v31\n"
      "v_mfma_f32_16x16x32_bf16 a[180:183], a[164:167], v[56:59], 0\n"
      "s_and_b32 s79, s61, 1\n"
      "ds_read_b128 a[144:147], v1 offset:8192\n"
      "v_fma_f32 v42, v42, s59, v31\n"
      "v_mfma_f32_16x16x32_bf16 v[244:247], a[164:167], v[52:55], 0\n"
      "s_lshl_b32 s79, s79, 8\n"
      "ds_read_b128 a[148:151], v1 offset:8704\n"
      "v_fma_f32 v43, v43, s59, v31\n"
      "v_mfma_f32_16x16x32_bf16 v[180:183], a[164:167], v[48:51], 0\n"
      "s_add_u32 s75, s78, s79\n"
      "ds_read_b128 a[152:155], v1 offset:12288\n"
      "v_mfma_f32_16x16x32_bf16 v[184:187], a[168:171], v[48:51], 0\n"
      "s_add_u32 s75, s75, s92\n"
      "v_exp_f32 v38, v38\n"
      "v_mfma_f32_16x16x32_bf16 v[248:251], a[168:171], v[52:55], 0\n"
      "s_cmp_lt_u32 s61, 5\n"
      "v_exp_f32 v39, v39\n"
      "v_mfma_f32_16x16x32_bf16 a[184:187], a[168:171], v[56:59], 0\n"
      "s_cselect_b32 s77, 0x1000, 0\n"
      "ds_read_b128 a[156:159], v1 offset:12800\n"
      "v_cvt_pk_bf16_f32 v75, v38, v39\n"
      "v_mfma_f32_16x16x32_bf16 a[248:251], a[168:171], v[60:63], 0\n"
      "s_cmp_eq_u32 s61, 6\n"
      "ds_read_b128 v[20:23], v6 offset:1024\n"
      "v_add_f32 v34, v34, v38\n"
      "v_mfma_f32_16x16x32_bf16 a[252:255], a[172:175], v[60:63], 0\n"
      "s_cselect_b32 s76, 0x10000000, s76\n"
      "s_waitcnt lgkmcnt(9)\n"
      "buffer_store_dwordx4 v[16:19], v7, s[48:51], s76 offen offset:0 nt\n"
      "v_add_f32 v34, v34, v39\n"
      "v_exp_f32 v40, v40\n"
      "v_mfma_f32_16x16x32_bf16 a[188:191], a[172:175], v[56:59], 0\n"
      "s_mov_b32 m0, s55\n"
      "v_exp_f32 v41, v41\n"
      "v_mfma_f32_16x16x32_bf16 v[252:255], a[172:175], v[52:55], 0\n"
      "s_cselect_b32 s77, 0xffffb000, s77\n"
      "v_exp_f32 v42, v42\n"
      "v_mfma_f32_16x16x32_bf16 v[188:191], a[172:175], v[48:51], 0\n"
      "s_branch LLOOP4a_%=\n"
      "LLOOP0_%=:\n"
      "s_mov_b32 m0, s55\n"
      "s_nop 0\n"
      "buffer_load_dwordx4 v4, s[36:39], s52 offen offset:2048 sc1 lds\n"
      "buffer_load_dwordx4 v4, s[36:39], s52 offen offset:3072 sc1 lds\n"
      "s_lshl_b32 s78, s61, 12\n"
      "s_add_u32 s76, s91, s78\n"
      "s_add_u32 s74, s58, s78\n"
      "s_lshr_b32 s78, s61, 1\n"
      "s_lshl_b32 s78, s78, 13\n"
      "s_and_b32 s79, s61, 1\n"
      "s_lshl_b32 s79, s79, 8\n"
      "s_add_u32 s75, s78, s79\n"
      "s_add_u32 s75, s75, s92\n"
      "s_cmp_lt_u32 s61, 5\n"
      "s_cselect_b32 s77, 0x1000, 0\n"
      "s_cmp_eq_u32 s61, 6\n"
      "s_cselect_b32 s77, 0xffffb000, s77\n"
      "s_cselect_b32 s74, 0x30000, s74\n"
      "s_cselect_b32 s76, 0x10000000, s76\n"
      "s_cselect_b32 s75, 0x10000000, s75\n"
      "s_add_u32 m0, s56, 0x2000\n"
      "s_nop 0\n"
      "buffer_load_dwordx4 v4, s[40:43], s90 offen offset:0 sc1 lds\n"
      "buffer_load_dwordx4 v4, s[40:43], s90 offen offset:1024 sc1 lds\n"
      "s_mov_b32 m0, s96\n"
      "s_nop 0\n"
      "buffer_load_dwordx4 v5, s[44:47], s97 offen offset:1024 nt lds\n"
      "ds_read_b128 v[16:19], v6 offset:0\n"
      "s_waitcnt lgkmcnt(0)\n"
      "buffer_store_dwordx4 v[16:19], v7, s[48:51], s76 offen offset:0 nt\n"
      "ds_read_b128 v[20:23], v6 offset:1024\n"
      "s_waitcnt lgkmcnt(0)\n"
      "buffer_store_dwordx4 v[20:23], v7, s[48:51], s76 offen offset:128 nt\n"
      "ds_read_b128 v[16:19], v6 offset:2048\n"
      "s_waitcnt lgkmcnt(0)\n"
      "buffer_store_dwordx4 v[16:19], v7, s[48:51], s76 offen offset:256 nt\n"
      "ds_read_b128 v[20:23], v6 offset:3072\n"
      "s_waitcnt lgkmcnt(0)\n"
      "buffer_store_dwordx4 v[20:23], v7, s[48:51], s76 offen offset:384 nt\n"
      "s_waitcnt vmcnt(15) lgkmcnt(0)\n"
      "s_barrier\n"
      "s_add_u32 s52, s52, 0x4000\n"
      "s_add_u32 s53, s53, 0x4000\n"
      "s_add_u32 s90, s90, 0x4000\n"
      "s_mov_b32 m0, s54\n"
      "s_nop 0\n"
      "buffer_load_dwordx4 v4, s[36:39], s52 offen offset:0 sc1 lds\n"
      "buffer_load_dwordx4 v4, s[36:39], s52 offen offset:1024 sc1 lds\n"
      "s_mov_b32 m0, s57\n"
      "s_nop 0\n"
      "buffer_load_dwordx4 v4, s[40:43], s53 offen offset:0 sc1 lds\n"
      "buffer_load_dwordx4 v4, s[40:43], s53 offen offset:1024 sc1 lds\n"
      "s_mov_b32 m0, s74\n"
      "s_nop 0\n"
      "buffer_load_dwordx4 v5, s[44:47], s75 offen offset:0 nt lds\n"
      "s_waitcnt vmcnt(15) lgkmcnt(0)\n"
      "s_barrier\n"
      "s_mov_b32 m0, s54\n"
      "s_nop 0\n"
      "buffer_load_dwordx4 v4, s[36:39], s52 offen offset:2048 sc1 lds\n"
      "buffer_load_dwordx4 v4, s[36:39], s52 offen offset:3072 sc1 lds\n"
      "s_add_u32 m0, s57, 0x2000\n"
      "s_nop 0\n"
      "buffer_load_dwordx4 v4, s[40:43], s90 offen offset:0 sc1 lds\n"
      "buffer_load_dwordx4 v4, s[40:43], s90 offen offset:1024 sc1 lds\n"
      "s_mov_b32 m0, s74\n"
      "s_nop 0\n"
      "buffer_load_dwordx4 v5, s[44:47], s75 offen offset:1024 nt lds\n"
      "s_waitcnt vmcnt(15) lgkmcnt(0)\n"
      "s_barrier\n"
      "v_add_u32 v6, s77, v6\n"
      "s_add_u32 s52, s52, 0x4000\n"
      "s_add_u32 s53, s53, 0x4000\n"
      "s_add_u32 s90, s90, 0x4000\n"
      "s_mov_b32 m0, s55\n"
      "s_nop 0\n"
      "buffer_load_dwordx4 v4, s[36:39], s52 offen offset:0 sc1 lds\n"
      "buffer_load_dwordx4 v4, s[36:39], s52 offen offset:1024 sc1 lds\n"
      "s_mov_b32 m0, s56\n"
      "s_nop 0\n"
      "buffer_load_dwordx4 v4, s[40:43], s53 offen offset:0 sc1 lds\n"
      "buffer_load_dwordx4 v4, s[40:43], s53 offen offset:1024 sc1 lds\n"
      "s_add_u32 s96, s74, 0x800\n"
      "s_add_u32 s97, s75, 0x80\n"
      "s_mov_b32 m0, s96\n"
      "s_nop 0\n"
      "buffer_load_dwordx4 v5, s[44:47], s97 offen offset:0 nt lds\n"
      "s_add_u32 s61, s61, 1\n"
      "s_cmp_lt_u32 s61, 7\n"
      "s_waitcnt vmcnt(11) lgkmcnt(0)\n"
      "s_barrier\n"
      "s_cbranch_scc1 LLOOP0_%=\n"
      "s_cmp_eq_u32 s62, 0\n"
      "s_cbranch_scc0 LX34_%=\n"
      "s_waitcnt vmcnt(6) lgkmcnt(0)\n"
      "s_barrier\n"
      "s_branch LDONE_%=\n"
      "LSTART3_%=:\n"
      "s_waitcnt vmcnt(16)\n"
      "s_barrier\n"
      "ds_read_b128 a[128:131], v1 offset:0\n"
      "ds_read_b128 a[132:135], v1 offset:512\n"
      "ds_read_b128 a[136:139], v1 offset:4096\n"
      "ds_read_b128 a[140:143], v1 offset:4608\n"
      "ds_read_b128 a[144:147], v1 offset:8192\n"
      "ds_read_b128 a[148:151], v1 offset:8704\n"
      "ds_read_b128 a[152:155], v1 offset:12288\n"
      "ds_read_b128 a[156:159], v1 offset:12800\n"
      "ds_read_b128 a[192:195], v1 offset:2048\n"
      "ds_read_b128 a[196:199], v1 offset:2560\n"
      "ds_read_b128 a[200:203], v1 offset:6144\n"
      "ds_read_b128 a[204:207], v1 offset:6656\n"
      "ds_read_b128 a[208:211], v1 offset:10240\n"
      "ds_read_b128 a[212:215], v1 offset:10752\n"
      "ds_read_b128 a[216:219], v1 offset:14336\n"
      "ds_read_b128 a[220:223], v1 offset:14848\n"
      "s_waitcnt lgkmcnt(0)\n"
      "s_barrier\n"
      "s_add_u32 m0, s58, 16384\n"
      "s_add_u32 s75, s92, 16384\n"
      "buffer_load_dwordx4 v5, s[44:47], s75 offen offset:0 nt lds\n"
      "buffer_load_dwordx4 v5, s[44:47], s75 offen offset:1024 nt lds\n"
      "s_add_u32 m0, s58, 18432\n"
      "s_add_u32 s75, s92, 16512\n"
      "buffer_load_dwordx4 v5, s[44:47], s75 offen offset:0 nt lds\n"
      "buffer_load_dwordx4 v5, s[44:47], s75 offen offset:1024 nt lds\n"
      "s_add_u32 m0, s58, 20480\n"
      "s_add_u32 s75, s92, 16640\n"
      "buffer_load_dwordx4 v5, s[44:47], s75 offen offset:0 nt lds\n"
      "buffer_load_dwordx4 v5, s[44:47], s75 offen offset:1024 nt lds\n"
      "s_add_u32 m0, s58, 22528\n"
      "s_add_u32 s75, s92, 16768\n"
      "buffer_load_dwordx4 v5, s[44:47], s75 offen offset:0 nt lds\n"
      "buffer_load_dwordx4 v5, s[44:47], s75 offen offset:1024 nt lds\n"
      "s_nop 0\n"
      "s_waitcnt vmcnt(22)\n"
      "ds_read_b128 a[0:3], v12 offset:0\n"
      "s_waitcnt lgkmcnt(0)\n"
      "v_mfma_f32_16x16x32_bf16 v[36:39], a[128:131], a[0:3], 0\n"
      "s_mov_b32 s61, 0\n"
      "ds_read_b128 a[4:7], v12 offset:512\n"
      "s_waitcnt vmcnt(20)\n"
      "ds_read_b128 a[8:11], v12 offset:2048\n"
      "ds_read_b128 a[12:15], v12 offset:2560\n"
      "s_waitcnt vmcnt(18)\n"
      "ds_read_b128 a[16:19], v12 offset:4096\n"
      "ds_read_b128 a[20:23], v12 offset:4608\n"
      "s_waitcnt vmcnt(16)\n"
      "ds_read_b128 a[24:27], v12 offset:6144\n"
      "ds_read_b128 a[28:31], v12 offset:6656\n"
      "s_waitcnt lgkmcnt(6)\n"
      "v_mfma_f32_16x16x32_bf16 v[36:39], a[132:135], a[4:7], v[36:39]\n"
      "s_waitcnt lgkmcnt(5)\n"
      "v_mfma_f32_16x16x32_bf16 v[36:39], a[136:139], a[8:11], v[36:39]\n"
      "s_waitcnt lgkmcnt(4)\n"
      "v_mfma_f32_16x16x32_bf16 v[36:39], a[140:143], a[12:15], v[36:39]\n"
      "s_waitcnt lgkmcnt(3)\n"
      "v_mfma_f32_16x16x32_bf16 v[36:39], a[144:147], a[16:19], v[36:39]\n"
      "s_waitcnt lgkmcnt(2)\n"
      "v_mfma_f32_16x16x32_bf16 v[36:39], a[148:151], a[20:23], v[36:39]\n"
      "s_waitcnt lgkmcnt(1)\n"
      "v_mfma_f32_16x16x32_bf16 v[36:39], a[152:155], a[24:27], v[36:39]\n"
      "s_waitcnt lgkmcnt(0)\n"
      "v_mfma_f32_16x16x32_bf16 v[36:39], a[156:159], a[28:31], v[36:39]\n"
      "s_mov_b32 s96, 0x30000\n"
      "s_nop 0\n"
      "s_nop 0\n"
      "v_mfma_f32_16x16x32_bf16 v[40:43], a[192:195], a[0:3], 0\n"
      "s_mov_b32 s97, 0x10000000\n"
      "s_nop 0\n"
      "s_nop 0\n"
      "v_mfma_f32_16x16x32_bf16 v[40:43], a[196:199], a[4:7], v[40:43]\n"
      "v_max3_f32 v28, v36, v37, v38\n"
      "v_max_f32 v28, v28, v39\n"
      "v_mov_b32 v0, v28\n"
      "v_mfma_f32_16x16x32_bf16 v[40:43], a[200:203], a[8:11], v[40:43]\n"
      "s_nop 0\n"
      "v_permlane32_swap_b32 v28, v0\n"
      "v_max_f32 v28, v28, v0\n"
      "v_mov_b32 v0, v28\n"
      "v_mfma_f32_16x16x32_bf16 v[40:43], a[204:207], a[12:15], v[40:43]\n"
      "s_nop 0\n"
      "v_permlane16_swap_b32 v28, v0\n"
      "v_max_f32 v28, v28, v0\n"
      "v_mul_f32 v28, s69, v28\n"
      "v_fma_f32 v36, v36, s59, v28\n"
      "v_fma_f32 v37, v37, s59, v28\n"
      "v_fma_f32 v38, v38, s59, v28\n"
      "v_fma_f32 v39, v39, s59, v28\n"
      "v_exp_f32 v36, v36\n"
      "v_exp_f32 v37, v37\n"
      "v_exp_f32 v38, v38\n"
      "v_exp_f32 v39, v39\n"
      "v_cvt_pk_bf16_f32 v48, v36, v37\n"
      "v_add_f32 v32, v36, v37\n"
      "v_cvt_pk_bf16_f32 v49, v38, v39\n"
      "v_add_f32 v32, v32, v38\n"
      "v_add_f32 v32, v32, v39\n"
      "v_mfma_f32_16x16x32_bf16 v[40:43], a[208:211], a[16:19], v[40:43]\n"
      "v_mfma_f32_16x16x32_bf16 v[40:43], a[212:215], a[20:23], v[40:43]\n"
      "v_mfma_f32_16x16x32_bf16 v[40:43], a[216:219], a[24:27], v[40:43]\n"
      "v_mfma_f32_16x16x32_bf16 v[40:43], a[220:223], a[28:31], v[40:43]\n"
      "s_nop 0\n"
      "s_nop 0\n"
      "s_nop 0\n"
      "s_nop 4\n"
      "v_fma_f32 v40, v40, s59, v28\n"
      "v_exp_f32 v40, v40\n"
      "v_fma_f32 v41, v41, s59, v28\n"
      "v_exp_f32 v41, v41\n"
      "s_nop 0\n"
      "v_cvt_pk_bf16_f32 v50, v40, v41\n"
      "v_fma_f32 v42, v42, s59, v28\n"
      "v_exp_f32 v42, v42\n"
      "v_fma_f32 v43, v43, s59, v28\n"
      "v_exp_f32 v43, v43\n"
      "s_nop 0\n"
      "v_cvt_pk_bf16_f32 v51, v42, v43\n"
      "v_add_f32 v32, v32, v40\n"
      "v_add_f32 v32, v32, v41\n"
      "v_add_f32 v32, v32, v42\n"
      "v_add_f32 v32, v32, v43\n"
      "s_nop 0\n"
      "s_add_u32 s52, s52, 0x4000\n"
      "s_mov_b32 m0, s55\n"
      "s_nop 0\n"
      "buffer_load_dwordx4 v4, s[36:39], s52 offen offset:0 sc1 lds\n"
      "buffer_load_dwordx4 v4, s[36:39], s52 offen offset:1024 sc1 lds\n"
      "s_add_u32 m0, s56, 0x0\n"
      "s_nop 0\n"
      "buffer_load_dwordx4 v4, s[40:43], s53 offen offset:0 sc1 lds\n"
      "buffer_load_dwordx4 v4, s[40:43], s53 offen offset:1024 sc1 lds\n"
      "s_mov_b32 m0, s55\n"
      "s_nop 0\n"
      "buffer_load_dwordx4 v4, s[36:39], s52 offen offset:2048 sc1 lds\n"
      "buffer_load_dwordx4 v4, s[36:39], s52 offen offset:3072 sc1 lds\n"
      "s_add_u32 m0, s56, 0x2000\n"
      "s_nop 0\n"
      "buffer_load_dwordx4 v4, s[40:43], s90 offen offset:0 sc1 lds\n"
      "buffer_load_dwordx4 v4, s[40:43], s90 offen offset:1024 sc1 lds\n"
      "s_add_u32 s52, s52, 0x4000\n"
      "s_mov_b32 m0, s54\n"
      "s_nop 0\n"
      "buffer_load_dwordx4 v4, s[36:39], s52 offen offset:0 sc1 lds\n"
      "buffer_load_dwordx4 v4, s[36:39], s52 offen offset:1024 sc1 lds\n"
      "s_add_u32 s53, s53, 0x4000\n"
      "s_mov_b32 m0, s57\n"
      "s_nop 0\n"
      "buffer_load_dwordx4 v4, s[40:43], s53 offen offset:0 sc1 lds\n"
      "buffer_load_dwordx4 v4, s[40:43], s53 offen offset:1024 sc1 lds\n"
      "s_nop 0\n"
      "s_waitcnt vmcnt(26)\n"
      "ds_read_b128 a[32:35], v12 offset:8192\n"
      "s_waitcnt lgkmcnt(0)\n"
      "v_mfma_f32_16x16x32_bf16 v[36:39], a[128:131], a[32:35], 0\n"
      "s_add_u32 s90, s90, 0x4000\n"
      "ds_read_b128 a[36:39], v12 offset:8704\n"
      "s_waitcnt vmcnt(24)\n"
      "ds_read_b128 a[40:43], v12 offset:10240\n"
      "ds_read_b128 a[44:47], v12 offset:10752\n"
      "s_waitcnt vmcnt(22)\n"
      "ds_read_b128 a[48:51], v12 offset:12288\n"
      "ds_read_b128 a[52:55], v12 offset:12800\n"
      "s_waitcnt vmcnt(20)\n"
      "ds_read_b128 a[56:59], v12 offset:14336\n"
      "ds_read_b128 a[60:63], v12 offset:14848\n"
      "s_waitcnt lgkmcnt(6)\n"
      "v_mfma_f32_16x16x32_bf16 v[36:39], a[132:135], a[36:39], v[36:39]\n"
      "s_waitcnt lgkmcnt(5)\n"
      "s_waitcnt vmcnt(18)\n"
      "ds_read_b128 a[64:67], v12 offset:16384\n"
      "v_mfma_f32_16x16x32_bf16 v[36:39], a[136:139], a[40:43], v[36:39]\n"
      "s_waitcnt lgkmcnt(5)\n"
      "ds_read_b128 a[68:71], v12 offset:16896\n"
      "v_mfma_f32_16x16x32_bf16 v[36:39], a[140:143], a[44:47], v[36:39]\n"
      "s_waitcnt lgkmcnt(5)\n"
      "s_waitcnt vmcnt(16)\n"
      "ds_read_b128 a[72:75], v12 offset:18432\n"
      "v_mfma_f32_16x16x32_bf16 v[36:39], a[144:147], a[48:51], v[36:39]\n"
      "s_waitcnt lgkmcnt(5)\n"
      "ds_read_b128 a[76:79], v12 offset:18944\n"
      "v_mfma_f32_16x16x32_bf16 v[36:39], a[148:151], a[52:55], v[36:39]\n"
      "s_waitcnt lgkmcnt(5)\n"
      "s_waitcnt vmcnt(14)\n"
      "ds_read_b128 a[80:83], v12 offset:20480\n"
      "v_mfma_f32_16x16x32_bf16 v[36:39], a[152:155], a[56:59], v[36:39]\n"
      "s_waitcnt lgkmcnt(5)\n"
      "ds_read_b128 a[84:87], v12 offset:20992\n"
      "v_mfma_f32_16x16x32_bf16 v[36:39], a[156:159], a[60:63], v[36:39]\n"
      "s_and_b32 s79, s64, 0xffffe000\n"
      "s_waitcnt vmcnt(12)\n"
      "ds_read_b128 a[88:91], v12 offset:22528\n"
      "v_mfma_f32_16x16x32_bf16 v[40:43], a[192:195], a[32:35], 0\n"
      "s_add_u32 s44, s80, s79\n"
      "ds_read_b128 a[92:95], v12 offset:23040\n"
      "s_nop 0\n"
      "v_mfma_f32_16x16x32_bf16 v[40:43], a[196:199], a[36:39], v[40:43]\n"
      "s_addc_u32 s45, s81, 0\n"
      "v_max3_f32 v29, v36, v37, v38\n"
      "v_max_f32 v29, v29, v39\n"
      "v_mov_b32 v0, v29\n"
      "v_mfma_f32_16x16x32_bf16 v[40:43], a[200:203], a[40:43], v[40:43]\n"
      "s_and_b32 s79, s64, 0xf8\n"
      "v_permlane32_swap_b32 v29, v0\n"
      "v_max_f32 v29, v29, v0\n"
      "v_mov_b32 v0, v29\n"
      "v_mfma_f32_16x16x32_bf16 v[40:43], a[204:207], a[44:47], v[40:43]\n"
      "s_lshl_b32 s46, s79, 10\n"
      "v_permlane16_swap_b32 v29, v0\n"
      "v_max_f32 v29, v29, v0\n"
      "v_mul_f32 v29, s69, v29\n"
      "v_fma_f32 v36, v36, s59, v29\n"
      "v_mfma_f32_16x16x32_bf16 v[40:43], a[208:211], a[48:51], v[40:43]\n"
      "s_and_b32 s79, s64, 7\n"
      "v_fma_f32 v37, v37, s59, v29\n"
      "v_fma_f32 v38, v38, s59, v29\n"
      "v_fma_f32 v39, v39, s59, v29\n"
      "v_mfma_f32_16x16x32_bf16 v[40:43], a[212:215], a[52:55], v[40:43]\n"
      "s_mul_i32 s79, s79, s93\n"
      "v_exp_f32 v36, v36\n"
      "v_exp_f32 v37, v37\n"
      "v_mfma_f32_16x16x32_bf16 v[40:43], a[216:219], a[56:59], v[40:43]\n"
      "s_lshl_b32 s92, s79, 13\n"
      "v_exp_f32 v38, v38\n"
      "v_exp_f32 v39, v39\n"
      "v_cvt_pk_bf16_f32 v52, v36, v37\n"
      "v_mfma_f32_16x16x32_bf16 v[40:43], a[220:223], a[60:63], v[40:43]\n"
      "s_waitcnt lgkmcnt(7)\n"
      "v_add_f32 v33, v36, v37\n"
      "v_cvt_pk_bf16_f32 v53, v38, v39\n"
      "v_add_f32 v33, v33, v38\n"
      "v_add_f32 v33, v33, v39\n"
      "v_mfma_f32_16x16x32_bf16 v[36:39], a[128:131], a[64:67], 0\n"
      "s_waitcnt lgkmcnt(6)\n"
      "s_nop 0\n"
      "v_fma_f32 v40, v40, s59, v29\n"
      "v_fma_f32 v41, v41, s59, v29\n"
      "v_fma_f32 v42, v42, s59, v29\n"
      "v_mfma_f32_16x16x32_bf16 v[36:39], a[132:135], a[68:71], v[36:39]\n"
      "s_waitcnt lgkmcnt(5)\n"
      "v_fma_f32 v43, v43, s59, v29\n"
      "v_exp_f32 v40, v40\n"
      "v_mfma_f32_16x16x32_bf16 v[36:39], a[136:139], a[72:75], v[36:39]\n"
      "s_waitcnt lgkmcnt(4)\n"
      "v_exp_f32 v41, v41\n"
      "v_exp_f32 v42, v42\n"
      "v_mfma_f32_16x16x32_bf16 v[36:39], a[140:143], a[76:79], v[36:39]\n"
      "s_waitcnt lgkmcnt(3)\n"
      "v_exp_f32 v43, v43\n"
      "v_cvt_pk_bf16_f32 v54, v40, v41\n"
      "v_add_f32 v33, v33, v40\n"
      "v_mfma_f32_16x16x32_bf16 v[36:39], a[144:147], a[80:83], v[36:39]\n"
      "s_waitcnt lgkmcnt(2)\n"
      "v_add_f32 v33, v33, v41\n"
      "v_cvt_pk_bf16_f32 v55, v42, v43\n"
      "v_add_f32 v33, v33, v42\n"
      "v_add_f32 v33, v33, v43\n"
      "v_mfma_f32_16x16x32_bf16 v[36:39], a[148:151], a[84:87], v[36:39]\n"
      "s_waitcnt lgkmcnt(1)\n"
      "v_mfma_f32_16x16x32_bf16 v[36:39], a[152:155], a[88:91], v[36:39]\n"
      "s_waitcnt lgkmcnt(0)\n"
      "v_mfma_f32_16x16x32_bf16 v[36:39], a[156:159], a[92:95], v[36:39]\n"
      "s_nop 0\n"
      "s_nop 0\n"
      "s_nop 0\n"
      "v_mfma_f32_16x16x32_bf16 v[40:43], a[192:195], a[64:67], 0\n"
      "s_nop 0\n"
      "s_nop 0\n"
      "s_nop 0\n"
      "v_mfma_f32_16x16x32_bf16 v[40:43], a[196:199], a[68:71], v[40:43]\n"
      "v_max3_f32 v30, v36, v37, v38\n"
      "v_max_f32 v30, v30, v39\n"
      "v_mov_b32 v0, v30\n"
      "v_mfma_f32_16x16x32_bf16 v[40:43], a[200:203], a[72:75], v[40:43]\n"
      "s_nop 0\n"
      "v_permlane32_swap_b32 v30, v0\n"
      "v_max_f32 v30, v30, v0\n"
      "v_mov_b32 v0, v30\n"
      "v_mfma_f32_16x16x32_bf16 v[40:43], a[204:207], a[76:79], v[40:43]\n"
      "s_nop 0\n"
      "v_permlane16_swap_b32 v30, v0\n"
      "v_max_f32 v30, v30, v0\n"
      "v_mul_f32 v30, s69, v30\n"
      "v_fma_f32 v36, v36, s59, v30\n"
      "v_fma_f32 v37, v37, s59, v30\n"
      "v_mfma_f32_16x16x32_bf16 v[40:43], a[208:211], a[80:83], v[40:43]\n"
      "v_fma_f32 v38, v38, s59, v30\n"
      "v_fma_f32 v39, v39, s59, v30\n"
      "v_exp_f32 v36, v36\n"
      "v_exp_f32 v37, v37\n"
      "v_mfma_f32_16x16x32_bf16 v[40:43], a[212:215], a[84:87], v[40:43]\n"
      "v_exp_f32 v38, v38\n"
      "v_exp_f32 v39, v39\n"
      "v_cvt_pk_bf16_f32 v56, v36, v37\n"
      "v_add_f32 v34, v36, v37\n"
      "v_cvt_pk_bf16_f32 v57, v38, v39\n"
      "v_mfma_f32_16x16x32_bf16 v[40:43], a[216:219], a[88:91], v[40:43]\n"
      "v_add_f32 v34, v34, v38\n"
      "v_add_f32 v34, v34, v39\n"
      "v_mfma_f32_16x16x32_bf16 v[40:43], a[220:223], a[92:95], v[40:43]\n"
      "s_waitcnt vmcnt(8) lgkmcnt(0)\n"
      "s_barrier\n"
      "ds_read_b128 a[128:131], v1 offset:16384\n"
      "ds_read_b128 a[132:135], v1 offset:16896\n"
      "ds_read_b128 a[136:139], v1 offset:20480\n"
      "ds_read_b128 a[140:143], v1 offset:20992\n"
      "ds_read_b128 a[144:147], v1 offset:24576\n"
      "ds_read_b128 a[148:151], v1 offset:25088\n"
      "ds_read_b128 a[152:155], v1 offset:28672\n"
      "ds_read_b128 a[156:159], v1 offset:29184\n"
      "s_waitcnt lgkmcnt(7)\n"
      "v_mfma_f32_16x16x32_bf16 v[36:39], a[128:131], a[0:3], 0\n"
      "s_waitcnt lgkmcnt(6)\n"
      "ds_read_b64_tr_b16 a[160:161], v2 offset:32768\n"
      "v_fma_f32 v40, v40, s59, v30\n"
      "v_fma_f32 v41, v41, s59, v30\n"
      "v_mfma_f32_16x16x32_bf16 v[36:39], a[132:135], a[4:7], v[36:39]\n"
      "s_waitcnt lgkmcnt(6)\n"
      "ds_read_b64_tr_b16 a[162:163], v2 offset:34816\n"
      "v_fma_f32 v42, v42, s59, v30\n"
      "v_fma_f32 v43, v43, s59, v30\n"
      "v_mfma_f32_16x16x32_bf16 v[36:39], a[136:139], a[8:11], v[36:39]\n"
      "s_waitcnt lgkmcnt(6)\n"
      "v_exp_f32 v40, v40\n"
      "v_mfma_f32_16x16x32_bf16 v[36:39], a[140:143], a[12:15], v[36:39]\n"
      "s_waitcnt lgkmcnt(5)\n"
      "v_exp_f32 v41, v41\n"
      "v_add_f32 v34, v34, v40\n"
      "v_mfma_f32_16x16x32_bf16 v[36:39], a[144:147], a[16:19], v[36:39]\n"
      "s_waitcnt lgkmcnt(4)\n"
      "v_exp_f32 v42, v42\n"
      "v_cvt_pk_bf16_f32 v58, v40, v41\n"
      "v_mfma_f32_16x16x32_bf16 v[36:39], a[148:151], a[20:23], v[36:39]\n"
      "s_waitcnt lgkmcnt(3)\n"
      "v_exp_f32 v43, v43\n"
      "v_mfma_f32_16x16x32_bf16 v[36:39], a[152:155], a[24:27], v[36:39]\n"
      "s_waitcnt lgkmcnt(2)\n"
      "ds_read_b64_tr_b16 a[164:165], v3 offset:32768\n"
      "v_add_f32 v34, v34, v41\n"
      "v_mfma_f32_16x16x32_bf16 v[36:39], a[156:159], a[28:31], v[36:39]\n"
      "s_mov_b32 m0, s54\n"
      "ds_read_b64_tr_b16 a[166:167], v3 offset:34816\n"
      "v_cvt_pk_bf16_f32 v59, v42, v43\n"
      "v_add_f32 v34, v34, v42\n"
      "v_add_f32 v34, v34, v43\n"
      "v_mfma_f32_16x16x32_bf16 v[40:43], a[156:159], a[60:63], 0\n"
      "s_nop 0\n"
      "ds_read_b64_tr_b16 a[168:169], v2 offset:33280\n"
      "v_fma_f32 v36, v36, s59, v28\n"
      "v_mfma_f32_16x16x32_bf16 v[40:43], a[152:155], a[56:59], v[40:43]\n"
      "ds_read_b64_tr_b16 a[170:171], v2 offset:35328\n"
      "v_fma_f32 v37, v37, s59, v28\n"
      "v_mfma_f32_16x16x32_bf16 v[40:43], a[148:151], a[52:55], v[40:43]\n"
      "ds_read_b64_tr_b16 a[172:173], v3 offset:33280\n"
      "v_fma_f32 v38, v38, s59, v28\n"
      "v_mfma_f32_16x16x32_bf16 v[40:43], a[144:147], a[48:51], v[40:43]\n"
      "ds_read_b64_tr_b16 a[174:175], v3 offset:35328\n"
      "v_fma_f32 v39, v39, s59, v28\n"
      "v_mfma_f32_16x16x32_bf16 v[40:43], a[140:143], a[44:47], v[40:43]\n"
      "v_exp_f32 v36, v36\n"
      "v_mfma_f32_16x16x32_bf16 v[40:43], a[136:139], a[40:43], v[40:43]\n"
      "v_exp_f32 v37, v37\n"
      "v_mfma_f32_16x16x32_bf16 v[40:43], a[132:135], a[36:39], v[40:43]\n"
      "v_exp_f32 v38, v38\n"
      "v_mfma_f32_16x16x32_bf16 v[40:43], a[128:131], a[32:35], v[40:43]\n"
      "s_waitcnt lgkmcnt(6)\n"
      "v_exp_f32 v39, v39\n"
      "v_mfma_f32_16x16x32_bf16 v[128:131], a[160:163], v[48:51], 0\n"
      "s_nop 0\n"
      "buffer_load_dwordx4 v4, s[36:39], s52 offen offset:2048 sc1 lds\n"
      "s_nop 0\n"
      "v_mfma_f32_16x16x32_bf16 v[192:195], a[160:163], v[52:55], 0\n"
      "s_nop 0\n"
      "v_cvt_pk_bf16_f32 v64, v36, v37\n"
      "v_add_f32 v32, v32, v36\n"
      "v_mfma_f32_16x16x32_bf16 v[80:83], a[160:163], v[56:59], 0\n"
      "s_waitcnt lgkmcnt(4)\n"
      "ds_read_b64_tr_b16 a[160:161], v2 offset:36864\n"
      "v_add_f32 v32, v32, v37\n"
      "v_mfma_f32_16x16x32_bf16 v[84:87], a[164:167], v[56:59], 0\n"
      "ds_read_b64_tr_b16 a[162:163], v2 offset:38912\n"
      "v_cvt_pk_bf16_f32 v65, v38, v39\n"
      "v_mfma_f32_16x16x32_bf16 v[196:199], a[164:167], v[52:55], 0\n"
      "buffer_load_dwordx4 v4, s[36:39], s52 offen offset:3072 sc1 lds\n"
      "v_mfma_f32_16x16x32_bf16 v[132:135], a[164:167], v[48:51], 0\n"
      "s_waitcnt lgkmcnt(4)\n"
      "ds_read_b64_tr_b16 a[164:165], v3 offset:36864\n"
      "v_add_f32 v32, v32, v38\n"
      "v_mfma_f32_16x16x32_bf16 v[136:139], a[168:171], v[48:51], 0\n"
      "s_add_u32 m0, s57, 0x2000\n"
      "ds_read_b64_tr_b16 a[166:167], v3 offset:38912\n"
      "v_add_f32 v32, v32, v39\n"
      "v_mfma_f32_16x16x32_bf16 v[200:203], a[168:171], v[52:55], 0\n"
      "s_add_u32 s94, s92, 0x6000\n"
      "v_fma_f32 v40, v40, s59, v29\n"
      "v_fma_f32 v41, v41, s59, v29\n"
      "v_mfma_f32_16x16x32_bf16 v[88:91], a[168:171], v[56:59], 0\n"
      "s_waitcnt lgkmcnt(4)\n"
      "buffer_load_dwordx4 v4, s[40:43], s90 offen offset:0 sc1 lds\n"
      "ds_read_b64_tr_b16 a[168:169], v2 offset:37376\n"
      "v_mfma_f32_16x16x32_bf16 v[92:95], a[172:175], v[56:59], 0\n"
      "ds_read_b64_tr_b16 a[170:171], v2 offset:39424\n"
      "v_fma_f32 v42, v42, s59, v29\n"
      "v_mfma_f32_16x16x32_bf16 v[204:207], a[172:175], v[52:55], 0\n"
      "v_fma_f32 v43, v43, s59, v29\n"
      "v_mfma_f32_16x16x32_bf16 v[140:143], a[172:175], v[48:51], 0\n"
      "ds_read_b64_tr_b16 a[172:173], v3 offset:37376\n"
      "v_mfma_f32_16x16x32_bf16 v[36:39], a[128:131], a[64:67], 0\n"
      "buffer_load_dwordx4 v4, s[40:43], s90 offen offset:1024 sc1 lds\n"
      "ds_read_b64_tr_b16 a[174:175], v3 offset:39424\n"
      "v_mfma_f32_16x16x32_bf16 v[36:39], a[132:135], a[68:71], v[36:39]\n"
      "v_exp_f32 v40, v40\n"
      "v_mfma_f32_16x16x32_bf16 v[36:39], a[136:139], a[72:75], v[36:39]\n"
      "v_exp_f32 v41, v41\n"
      "v_mfma_f32_16x16x32_bf16 v[36:39], a[140:143], a[76:79], v[36:39]\n"
      "v_exp_f32 v42, v42\n"
      "v_mfma_f32_16x16x32_bf16 v[36:39], a[144:147], a[80:83], v[36:39]\n"
      "buffer_load_dwordx4 a[96:99], v13, s[44:47], s94 offen offset:0\n"
      "v_cvt_pk_bf16_f32 v68, v40, v41\n"
      "v_mfma_f32_16x16x32_bf16 v[36:39], a[148:151], a[84:87], v[36:39]\n"
      "v_exp_f32 v43, v43\n"
      "v_mfma_f32_16x16x32_bf16 v[36:39], a[152:155], a[88:91], v[36:39]\n"
      "v_add_f32 v33, v33, v40\n"
      "v_add_f32 v33, v33, v41\n"
      "v_mfma_f32_16x16x32_bf16 v[36:39], a[156:159], a[92:95], v[36:39]\n"
      "buffer_load_dwordx4 a[100:103], v13, s[44:47], s94 offen offset:64\n"
      "buffer_load_dwordx4 a[104:107], v13, s[44:47], s94 offen offset:128\n"
      "buffer_load_dwordx4 a[108:111], v13, s[44:47], s94 offen offset:192\n"
      "s_waitcnt vmcnt(12) lgkmcnt(0)\n"
      "s_barrier\n"
      "v_mfma_f32_16x16x32_bf16 v[144:147], a[160:163], v[48:51], 0\n"
      "s_add_u32 s52, s52, 0x4000\n"
      "ds_read_b128 a[128:131], v1 offset:18432\n"
      "v_cvt_pk_bf16_f32 v69, v42, v43\n"
      "v_mfma_f32_16x16x32_bf16 v[208:211], a[160:163], v[52:55], 0\n"
      "s_mov_b32 m0, s55\n"
      "ds_read_b128 a[132:135], v1 offset:18944\n"
      "v_add_f32 v33, v33, v42\n"
      "v_mfma_f32_16x16x32_bf16 v[96:99], a[160:163], v[56:59], 0\n"
      "s_add_u32 s53, s53, 0x4000\n"
      "ds_read_b128 a[136:139], v1 offset:22528\n"
      "v_add_f32 v33, v33, v43\n"
      "v_mfma_f32_16x16x32_bf16 v[100:103], a[164:167], v[56:59], 0\n"
      "s_add_u32 s90, s90, 0x4000\n"
      "ds_read_b128 a[140:143], v1 offset:23040\n"
      "v_fma_f32 v36, v36, s59, v30\n"
      "v_mfma_f32_16x16x32_bf16 v[212:215], a[164:167], v[52:55], 0\n"
      "ds_read_b128 a[144:147], v1 offset:26624\n"
      "v_fma_f32 v37, v37, s59, v30\n"
      "v_mfma_f32_16x16x32_bf16 v[148:151], a[164:167], v[48:51], 0\n"
      "ds_read_b128 a[148:151], v1 offset:27136\n"
      "v_fma_f32 v38, v38, s59, v30\n"
      "v_mfma_f32_16x16x32_bf16 v[152:155], a[168:171], v[48:51], 0\n"
      "ds_read_b128 a[152:155], v1 offset:30720\n"
      "v_fma_f32 v39, v39, s59, v30\n"
      "v_mfma_f32_16x16x32_bf16 v[216:219], a[168:171], v[52:55], 0\n"
      "ds_read_b128 a[156:159], v1 offset:31232\n"
      "v_mfma_f32_16x16x32_bf16 v[104:107], a[168:171], v[56:59], 0\n"
      "v_exp_f32 v36, v36\n"
      "v_mfma_f32_16x16x32_bf16 v[108:111], a[172:175], v[56:59], 0\n"
      "v_exp_f32 v37, v37\n"
      "v_mfma_f32_16x16x32_bf16 v[220:223], a[172:175], v[52:55], 0\n"
      "v_exp_f32 v38, v38\n"
      "v_mfma_f32_16x16x32_bf16 v[156:159], a[172:175], v[48:51], 0\n"
      "s_waitcnt lgkmcnt(7)\n"
      "v_exp_f32 v39, v39\n"
      "v_mfma_f32_16x16x32_bf16 v[40:43], a[128:131], a[0:3], 0\n"
      "s_waitcnt lgkmcnt(6)\n"
      "ds_read_b64_tr_b16 a[160:161], v2 offset:40960\n"
      "v_cvt_pk_bf16_f32 v72, v36, v37\n"
      "v_mfma_f32_16x16x32_bf16 v[40:43], a[132:135], a[4:7], v[40:43]\n"
      "s_waitcnt lgkmcnt(6)\n"
      "ds_read_b64_tr_b16 a[162:163], v2 offset:43008\n"
      "v_add_f32 v34, v34, v36\n"
      "v_mfma_f32_16x16x32_bf16 v[40:43], a[136:139], a[8:11], v[40:43]\n"
      "s_waitcnt lgkmcnt(6)\n"
      "ds_read_b64_tr_b16 a[164:165], v3 offset:40960\n"
      "v_add_f32 v34, v34, v37\n"
      "v_mfma_f32_16x16x32_bf16 v[40:43], a[140:143], a[12:15], v[40:43]\n"
      "s_waitcnt lgkmcnt(6)\n"
      "ds_read_b64_tr_b16 a[166:167], v3 offset:43008\n"
      "v_cvt_pk_bf16_f32 v73, v38, v39\n"
      "v_mfma_f32_16x16x32_bf16 v[40:43], a[144:147], a[16:19], v[40:43]\n"
      "s_waitcnt lgkmcnt(6)\n"
      "buffer_load_dwordx4 v4, s[36:39], s52 offen offset:0 sc1 lds\n"
      "ds_read_b64_tr_b16 a[168:169], v2 offset:41472\n"
      "v_mfma_f32_16x16x32_bf16 v[40:43], a[148:151], a[20:23], v[40:43]\n"
      "s_waitcnt lgkmcnt(6)\n"
      "ds_read_b64_tr_b16 a[170:171], v2 offset:43520\n"
      "v_add_f32 v34, v34, v38\n"
      "v_mfma_f32_16x16x32_bf16 v[40:43], a[152:155], a[24:27], v[40:43]\n"
      "s_waitcnt lgkmcnt(6)\n"
      "ds_read_b64_tr_b16 a[172:173], v3 offset:41472\n"
      "v_add_f32 v34, v34, v39\n"
      "v_mfma_f32_16x16x32_bf16 v[40:43], a[156:159], a[28:31], v[40:43]\n"
      "s_nop 0\n"
      "ds_read_b64_tr_b16 a[174:175], v3 offset:43520\n"
      "s_nop 0\n"
      "v_mfma_f32_16x16x32_bf16 v[36:39], a[156:159], a[60:63], 0\n"
      "s_nop 0\n"
      "buffer_load_dwordx4 v4, s[36:39], s52 offen offset:1024 sc1 lds\n"
      "s_nop 0\n"
      "v_mfma_f32_16x16x32_bf16 v[36:39], a[152:155], a[56:59], v[36:39]\n"
      "s_mov_b32 m0, s56\n"
      "v_fma_f32 v40, v40, s59, v28\n"
      "v_fma_f32 v41, v41, s59, v28\n"
      "v_mfma_f32_16x16x32_bf16 v[36:39], a[148:151], a[52:55], v[36:39]\n"
      "v_fma_f32 v42, v42, s59, v28\n"
      "v_fma_f32 v43, v43, s59, v28\n"
      "v_mfma_f32_16x16x32_bf16 v[36:39], a[144:147], a[48:51], v[36:39]\n"
      "v_exp_f32 v40, v40\n"
      "v_mfma_f32_16x16x32_bf16 v[36:39], a[140:143], a[44:47], v[36:39]\n"
      "buffer_load_dwordx4 v4, s[40:43], s53 offen offset:0 sc1 lds\n"
      "v_mfma_f32_16x16x32_bf16 v[36:39], a[136:139], a[40:43], v[36:39]\n"
      "v_exp_f32 v41, v41\n"
      "v_mfma_f32_16x16x32_bf16 v[36:39], a[132:135], a[36:39], v[36:39]\n"
      "v_exp_f32 v42, v42\n"
      "v_mfma_f32_16x16x32_bf16 v[36:39], a[128:131], a[32:35], v[36:39]\n"
      "s_waitcnt lgkmcnt(6)\n"
      "v_exp_f32 v43, v43\n"
      "v_mfma_f32_16x16x32_bf16 v[160:163], a[160:163], v[48:51], 0\n"
      "s_nop 0\n"
      "buffer_load_dwordx4 v4, s[40:43], s53 offen offset:1024 sc1 lds\n"
      "s_nop 0\n"
      "v_mfma_f32_16x16x32_bf16 v[224:227], a[160:163], v[52:55], 0\n"
      "s_nop 0\n"
      "v_cvt_pk_bf16_f32 v66, v40, v41\n"
      "v_add_f32 v32, v32, v40\n"
      "v_mfma_f32_16x16x32_bf16 v[112:115], a[160:163], v[56:59], 0\n"
      "s_waitcnt lgkmcnt(4)\n"
      "ds_read_b64_tr_b16 a[160:161], v2 offset:45056\n"
      "v_add_f32 v32, v32, v41\n"
      "v_mfma_f32_16x16x32_bf16 v[116:119], a[164:167], v[56:59], 0\n"
      "ds_read_b64_tr_b16 a[162:163], v2 offset:47104\n"
      "v_cvt_pk_bf16_f32 v67, v42, v43\n"
      "v_mfma_f32_16x16x32_bf16 v[228:231], a[164:167], v[52:55], 0\n"
      "v_add_f32 v32, v32, v42\n"
      "v_add_f32 v32, v32, v43\n"
      "v_mfma_f32_16x16x32_bf16 v[164:167], a[164:167], v[48:51], 0\n"
      "s_waitcnt lgkmcnt(4)\n"
      "ds_read_b64_tr_b16 a[164:165], v3 offset:45056\n"
      "v_fma_f32 v36, v36, s59, v29\n"
      "v_mfma_f32_16x16x32_bf16 v[168:171], a[168:171], v[48:51], 0\n"
      "ds_read_b64_tr_b16 a[166:167], v3 offset:47104\n"
      "v_fma_f32 v37, v37, s59, v29\n"
      "v_mfma_f32_16x16x32_bf16 v[232:235], a[168:171], v[52:55], 0\n"
      "v_fma_f32 v38, v38, s59, v29\n"
      "v_fma_f32 v39, v39, s59, v29\n"
      "v_mfma_f32_16x16x32_bf16 v[120:123], a[168:171], v[56:59], 0\n"
      "s_waitcnt lgkmcnt(4)\n"
      "ds_read_b64_tr_b16 a[168:169], v2 offset:45568\n"
      "buffer_load_dwordx4 a[112:115], v13, s[44:47], s94 offen offset:256\n"
      "v_mfma_f32_16x16x32_bf16 v[124:127], a[172:175], v[56:59], 0\n"
      "ds_read_b64_tr_b16 a[170:171], v2 offset:47616\n"
      "buffer_load_dwordx4 a[116:119], v13, s[44:47], s94 offen offset:320\n"
      "v_mfma_f32_16x16x32_bf16 v[236:239], a[172:175], v[52:55], 0\n"
      "v_exp_f32 v36, v36\n"
      "v_mfma_f32_16x16x32_bf16 v[172:175], a[172:175], v[48:51], 0\n"
      "ds_read_b64_tr_b16 a[172:173], v3 offset:45568\n"
      "v_add_f32 v33, v33, v36\n"
      "v_mfma_f32_16x16x32_bf16 v[40:43], a[128:131], a[64:67], 0\n"
      "ds_read_b64_tr_b16 a[174:175], v3 offset:47616\n"
      "buffer_load_dwordx4 a[120:123], v13, s[44:47], s94 offen offset:384\n"
      "v_mfma_f32_16x16x32_bf16 v[40:43], a[132:135], a[68:71], v[40:43]\n"
      "v_exp_f32 v37, v37\n"
      "v_mfma_f32_16x16x32_bf16 v[40:43], a[136:139], a[72:75], v[40:43]\n"
      "v_exp_f32 v38, v38\n"
      "v_mfma_f32_16x16x32_bf16 v[40:43], a[140:143], a[76:79], v[40:43]\n"
      "v_exp_f32 v39, v39\n"
      "v_mfma_f32_16x16x32_bf16 v[40:43], a[144:147], a[80:83], v[40:43]\n"
      "v_cvt_pk_bf16_f32 v70, v36, v37\n"
      "v_add_f32 v33, v33, v37\n"
      "v_mfma_f32_16x16x32_bf16 v[40:43], a[148:151], a[84:87], v[40:43]\n"
      "v_cvt_pk_bf16_f32 v71, v38, v39\n"
      "v_add_f32 v33, v33, v38\n"
      "v_mfma_f32_16x16x32_bf16 v[40:43], a[152:155], a[88:91], v[40:43]\n"
      "v_add_f32 v33, v33, v39\n"
      "v_mfma_f32_16x16x32_bf16 v[40:43], a[156:159], a[92:95], v[40:43]\n"
      "buffer_load_dwordx4 a[124:127], v13, s[44:47], s94 offen offset:448\n"
      "s_waitcnt vmcnt(16) lgkmcnt(0)\n"
      "s_barrier\n"
      "s_branch LP3_%=\n"
      "LEND4_%=:\n"
      "v_mfma_f32_16x16x32_bf16 v[176:179], a[160:163], v[48:51], v[176:179]\n"
      "s_and_b32 s79, s63, 0xffffe000\n"
      "v_exp_f32 v38, v38\n"
      "v_mfma_f32_16x16x32_bf16 v[240:243], a[160:163], v[52:55], v[240:243]\n"
      "s_add_u32 s48, s82, s79\n"
      "v_exp_f32 v39, v39\n"
      "v_mfma_f32_16x16x32_bf16 a[176:179], a[160:163], v[56:59], a[176:179]\n"
      "s_addc_u32 s49, s83, 0\n"
      "v_fma_f32 v40, v40, s59, v31\n"
      "v_fma_f32 v41, v41, s59, v31\n"
      "v_mfma_f32_16x16x32_bf16 a[240:243], a[160:163], v[60:63], a[240:243]\n"
      "s_and_b32 s79, s63, 0xf8\n"
      "ds_read_b64_tr_b16 a[160:161], v2 offset:49152\n"
      "v_fma_f32 v42, v42, s59, v31\n"
      "v_mfma_f32_16x16x32_bf16 a[244:247], a[164:167], v[60:63], a[244:247]\n"
      "s_lshl_b32 s50, s79, 10\n"
      "ds_read_b64_tr_b16 a[162:163], v2 offset:51200\n"
      "v_fma_f32 v43, v43, s59, v31\n"
      "v_mfma_f32_16x16x32_bf16 a[180:183], a[164:167], v[56:59], a[180:183]\n"
      "s_and_b32 s79, s63, 7\n"
      "v_cvt_pk_bf16_f32 v74, v36, v37\n"
      "v_cvt_pk_bf16_f32 v75, v38, v39\n"
      "v_mfma_f32_16x16x32_bf16 v[244:247], a[164:167], v[52:55], v[244:247]\n"
      "s_mul_i32 s79, s79, s93\n"
      "v_exp_f32 v40, v40\n"
      "v_mfma_f32_16x16x32_bf16 v[180:183], a[164:167], v[48:51], v[180:183]\n"
      "s_lshl_b32 s91, s79, 13\n"
      "ds_read_b64_tr_b16 a[164:165], v3 offset:49152\n"
      "v_mov_b32 v24, v32\n"
      "v_mfma_f32_16x16x32_bf16 v[184:187], a[168:171], v[48:51], v[184:187]\n"
      "s_add_u32 s95, s91, 0x6000\n"
      "ds_read_b64_tr_b16 a[166:167], v3 offset:51200\n"
      "v_mov_b32 v26, v33\n"
      "v_mfma_f32_16x16x32_bf16 v[248:251], a[168:171], v[52:55], v[248:251]\n"
      "s_add_u32 s84, s91, 0x0\n"
      "v_exp_f32 v41, v41\n"
      "v_mfma_f32_16x16x32_bf16 a[184:187], a[168:171], v[56:59], a[184:187]\n"
      "s_add_u32 s85, s91, 0x1000\n"
      "v_exp_f32 v42, v42\n"
      "v_mfma_f32_16x16x32_bf16 a[248:251], a[168:171], v[60:63], a[248:251]\n"
      "s_add_u32 s86, s91, 0x2000\n"
      "ds_read_b64_tr_b16 a[168:169], v2 offset:49664\n"
      "v_cvt_pk_bf16_f32 v78, v40, v41\n"
      "v_mfma_f32_16x16x32_bf16 a[252:255], a[172:175], v[60:63], a[252:255]\n"
      "s_add_u32 s87, s91, 0x3000\n"
      "ds_read_b64_tr_b16 a[170:171], v2 offset:51712\n"
      "v_add_f32 v34, v34, v36\n"
      "v_mfma_f32_16x16x32_bf16 a[188:191], a[172:175], v[56:59], a[188:191]\n"
      "s_add_u32 s88, s91, 0x4000\n"
      "v_exp_f32 v43, v43\n"
      "v_mfma_f32_16x16x32_bf16 v[252:255], a[172:175], v[52:55], v[252:255]\n"
      "s_add_u32 s89, s91, 0x5000\n"
      "v_cvt_pk_bf16_f32 v79, v42, v43\n"
      "v_add_f32 v34, v34, v37\n"
      "v_mfma_f32_16x16x32_bf16 v[188:191], a[172:175], v[48:51], v[188:191]\n"
      "s_waitcnt lgkmcnt(4)\n"
      "ds_read_b64_tr_b16 a[172:173], v3 offset:49664\n"
      "v_add_f32 v34, v34, v38\n"
      "v_mfma_f32_16x16x32_bf16 v[128:131], a[160:163], v[64:67], v[128:131]\n"
      "ds_read_b64_tr_b16 a[174:175], v3 offset:51712\n"
      "v_add_f32 v34, v34, v39\n"
      "v_mfma_f32_16x16x32_bf16 v[192:195], a[160:163], v[68:71], v[192:195]\n"
      "v_permlane32_swap_b32 v32, v24\n"
      "v_mfma_f32_16x16x32_bf16 v[80:83], a[160:163], v[72:75], v[80:83]\n"
      "v_permlane32_swap_b32 v33, v26\n"
      "v_mfma_f32_16x16x32_bf16 a[192:195], a[160:163], v[76:79], a[192:195]\n"
      "s_waitcnt lgkmcnt(4)\n"
      "ds_read_b64_tr_b16 a[160:161], v2 offset:53248\n"
      "v_add_f32 v32, v32, v24\n"
      "v_mfma_f32_16x16x32_bf16 a[196:199], a[164:167], v[76:79], a[196:199]\n"
      "s_nop 0\n"
      "ds_read_b64_tr_b16 a[162:163], v2 offset:55296\n"
      "v_mov_b32 v24, v32\n"
      "v_mfma_f32_16x16x32_bf16 v[84:87], a[164:167], v[72:75], v[84:87]\n"
      "s_nop 0\n"
      "v_add_f32 v33, v33, v26\n"
      "v_mov_b32 v26, v33\n"
      "v_mfma_f32_16x16x32_bf16 v[196:199], a[164:167], v[68:71], v[196:199]\n"
      "s_nop 0\n"
      "v_permlane16_swap_b32 v32, v24\n"
      "v_mfma_f32_16x16x32_bf16 v[132:135], a[164:167], v[64:67], v[132:135]\n"
      "s_waitcnt lgkmcnt(4)\n"
      "ds_read_b64_tr_b16 a[164:165], v3 offset:53248\n"
      "v_add_f32 v32, v32, v24\n"
      "v_mfma_f32_16x16x32_bf16 v[136:139], a[168:171], v[64:67], v[136:139]\n"
      "ds_read_b64_tr_b16 a[166:167], v3 offset:55296\n"
      "v_mov_b32 v46, v34\n"
      "v_mfma_f32_16x16x32_bf16 v[200:203], a[168:171], v[68:71], v[200:203]\n"
      "s_nop 0\n"
      "v_permlane16_swap_b32 v33, v26\n"
      "v_mfma_f32_16x16x32_bf16 v[88:91], a[168:171], v[72:75], v[88:91]\n"
      "v_add_f32 v33, v33, v26\n"
      "v_rcp_f32 v24, v32\n"
      "v_add_f32 v35, v35, v40\n"
      "v_mfma_f32_16x16x32_bf16 a[200:203], a[168:171], v[76:79], a[200:203]\n"
      "s_waitcnt lgkmcnt(4)\n"
      "ds_read_b64_tr_b16 a[168:169], v2 offset:53760\n"
      "ds_read_b64_tr_b16 a[170:171], v2 offset:55808\n"
      "v_rcp_f32 v26, v33\n"
      "v_mfma_f32_16x16x32_bf16 a[204:207], a[172:175], v[76:79], a[204:207]\n"
      "v_mov_b32 v25, v24\n"
      "v_mov_b32 v27, v26\n"
      "v_permlane32_swap_b32 v34, v46\n"
      "v_mfma_f32_16x16x32_bf16 v[92:95], a[172:175], v[72:75], v[92:95]\n"
      "v_add_f32 v34, v34, v46\n"
      "v_mov_b32 v46, v34\n"
      "v_add_f32 v35, v35, v41\n"
      "v_add_f32 v35, v35, v42\n"
      "v_permlane16_swap_b32 v34, v46\n"
      "v_pk_mul_f32 v[128:129], v[128:129], v[24:25]\n"
      "v_pk_mul_f32 v[130:131], v[130:131], v[24:25]\n"
      "v_pk_mul_f32 v[192:193], v[192:193], v[26:27]\n"
      "v_pk_mul_f32 v[194:195], v[194:195], v[26:27]\n"
      "v_pk_mul_f32 v[132:133], v[132:133], v[24:25]\n"
      "v_pk_mul_f32 v[134:135], v[134:135], v[24:25]\n"
      "v_pk_mul_f32 v[196:197], v[196:197], v[26:27]\n"
      "v_pk_mul_f32 v[198:199], v[198:199], v[26:27]\n"
      "v_pk_mul_f32 v[136:137], v[136:137], v[24:25]\n"
      "v_pk_mul_f32 v[138:139], v[138:139], v[24:25]\n"
      "v_pk_mul_f32 v[200:201], v[200:201], v[26:27]\n"
      "v_pk_mul_f32 v[202:203], v[202:203], v[26:27]\n"
      "v_cvt_pk_bf16_f32 v128, v128, v129\n"
      "v_mfma_f32_16x16x32_bf16 v[204:207], a[172:175], v[68:71], v[204:207]\n"
      "s_nop 0\n"
      "v_cvt_pk_bf16_f32 v129, v130, v131\n"
      "v_cvt_pk_bf16_f32 v192, v192, v193\n"
      "v_cvt_pk_bf16_f32 v193, v194, v195\n"
      "v_cvt_pk_bf16_f32 v132, v132, v133\n"
      "v_cvt_pk_bf16_f32 v133, v134, v135\n"
      "v_cvt_pk_bf16_f32 v196, v196, v197\n"
      "v_mfma_f32_16x16x32_bf16 v[140:143], a[172:175], v[64:67], v[140:143]\n"
      "ds_read_b64_tr_b16 a[172:173], v3 offset:53760\n"
      "ds_read_b64_tr_b16 a[174:175], v3 offset:55808\n"
      "s_waitcnt vmcnt(6) lgkmcnt(0)\n"
      "s_barrier\n"
      "v_mfma_f32_16x16x32_bf16 v[144:147], a[160:163], v[64:67], v[144:147]\n"
      "s_nop 0\n"
      "ds_write2st64_b64 v8, v[128:129], v[192:193] offset0:0 offset1:16\n"
      "v_pk_mul_f32 v[204:205], v[204:205], v[26:27]\n"
      "v_pk_mul_f32 v[140:141], v[140:141], v[24:25]\n"
      "v_pk_mul_f32 v[142:143], v[142:143], v[24:25]\n"
      "v_pk_mul_f32 v[206:207], v[206:207], v[26:27]\n"
      "v_cvt_pk_bf16_f32 v197, v198, v199\n"
      "v_cvt_pk_bf16_f32 v136, v136, v137\n"
      "v_pk_mul_f32 v[144:145], v[144:145], v[24:25]\n"
      "v_pk_mul_f32 v[146:147], v[146:147], v[24:25]\n"
      "v_cvt_pk_bf16_f32 v137, v138, v139\n"
      "v_mfma_f32_16x16x32_bf16 v[208:211], a[160:163], v[68:71], v[208:211]\n"
      "s_nop 0\n"
      "ds_write2st64_b64 v9, v[132:133], v[196:197] offset0:0 offset1:16\n"
      "v_mfma_f32_16x16x32_bf16 v[96:99], a[160:163], v[72:75], v[96:99]\n"
      "s_nop 0\n"
      "v_cvt_pk_bf16_f32 v200, v200, v201\n"
      "v_cvt_pk_bf16_f32 v201, v202, v203\n"
      "v_cvt_pk_bf16_f32 v140, v140, v141\n"
      "v_cvt_pk_bf16_f32 v141, v142, v143\n"
      "v_mfma_f32_16x16x32_bf16 a[208:211], a[160:163], v[76:79], a[208:211]\n"
      "ds_write2st64_b64 v10, v[136:137], v[200:201] offset0:0 offset1:16\n"
      "v_cvt_pk_bf16_f32 v204, v204, v205\n"
      "v_mfma_f32_16x16x32_bf16 a[212:215], a[164:167], v[76:79], a[212:215]\n"
      "ds_read_b64_tr_b16 a[160:161], v2 offset:57344\n"
      "v_cvt_pk_bf16_f32 v205, v206, v207\n"
      "v_cvt_pk_bf16_f32 v144, v144, v145\n"
      "v_cvt_pk_bf16_f32 v145, v146, v147\n"
      "v_add_f32 v34, v34, v46\n"
      "v_pk_mul_f32 v[208:209], v[208:209], v[26:27]\n"
      "v_pk_mul_f32 v[210:211], v[210:211], v[26:27]\n"
      "v_cvt_pk_bf16_f32 v208, v208, v209\n"
      "v_mfma_f32_16x16x32_bf16 v[100:103], a[164:167], v[72:75], v[100:103]\n"
      "ds_write2st64_b64 v11, v[140:141], v[204:205] offset0:0 offset1:16\n"
      "ds_read_b128 v[48:51], v6 offset:0\n"
      "v_cvt_pk_bf16_f32 v209, v210, v211\n"
      "v_add_f32 v35, v35, v43\n"
      "v_mfma_f32_16x16x32_bf16 v[212:215], a[164:167], v[68:71], v[212:215]\n"
      "s_nop 0\n"
      "ds_read_b128 v[52:55], v6 offset:4096\n"
      "ds_read_b128 v[56:59], v6 offset:8192\n"
      "ds_read_b128 v[60:63], v6 offset:12288\n"
      "ds_write2st64_b64 v8, v[144:145], v[208:209] offset0:2 offset1:18\n"
      "v_mov_b32 v14, v35\n"
      "v_mfma_f32_16x16x32_bf16 v[148:151], a[164:167], v[64:67], v[148:151]\n"
      "s_nop 0\n"
      "ds_read_b64_tr_b16 a[162:163], v2 offset:59392\n"
      "v_rcp_f32 v46, v34\n"
      "v_permlane32_swap_b32 v35, v14\n"
      "v_pk_mul_f32 v[212:213], v[212:213], v[26:27]\n"
      "v_pk_mul_f32 v[214:215], v[214:215], v[26:27]\n"
      "v_cvt_pk_bf16_f32 v212, v212, v213\n"
      "v_cvt_pk_bf16_f32 v213, v214, v215\n"
      "v_pk_mul_f32 v[148:149], v[148:149], v[24:25]\n"
      "v_pk_mul_f32 v[150:151], v[150:151], v[24:25]\n"
      "v_cvt_pk_bf16_f32 v148, v148, v149\n"
      "v_cvt_pk_bf16_f32 v149, v150, v151\n"
      "v_mov_b32 v47, v46\n"
      "v_pk_mul_f32 v[80:81], v[80:81], v[46:47]\n"
      "v_pk_mul_f32 v[82:83], v[82:83], v[46:47]\n"
      "v_pk_mul_f32 v[96:97], v[96:97], v[46:47]\n"
      "v_pk_mul_f32 v[98:99], v[98:99], v[46:47]\n"
      "v_pk_mul_f32 v[84:85], v[84:85], v[46:47]\n"
      "v_pk_mul_f32 v[86:87], v[86:87], v[46:47]\n"
      "v_pk_mul_f32 v[100:101], v[100:101], v[46:47]\n"
      "v_pk_mul_f32 v[102:103], v[102:103], v[46:47]\n"
      "v_pk_mul_f32 v[88:89], v[88:89], v[46:47]\n"
      "v_pk_mul_f32 v[90:91], v[90:91], v[46:47]\n"
      "v_pk_mul_f32 v[92:93], v[92:93], v[46:47]\n"
      "v_pk_mul_f32 v[94:95], v[94:95], v[46:47]\n"
      "v_cvt_pk_bf16_f32 v80, v80, v81\n"
      "v_mfma_f32_16x16x32_bf16 v[152:155], a[168:171], v[64:67], v[152:155]\n"
      "s_nop 0\n"
      "ds_write2st64_b64 v9, v[148:149], v[212:213] offset0:2 offset1:18\n"
      "ds_read_b64_tr_b16 a[164:165], v3 offset:57344\n"
      "v_cvt_pk_bf16_f32 v81, v82, v83\n"
      "v_cvt_pk_bf16_f32 v96, v96, v97\n"
      "v_cvt_pk_bf16_f32 v97, v98, v99\n"
      "v_mfma_f32_16x16x32_bf16 v[216:219], a[168:171], v[68:71], v[216:219]\n"
      "s_nop 0\n"
      "ds_write2st64_b64 v8, v[80:81], v[96:97] offset0:32 offset1:34\n"
      "ds_read_b64_tr_b16 a[166:167], v3 offset:59392\n"
      "v_pk_mul_f32 v[152:153], v[152:153], v[24:25]\n"
      "v_pk_mul_f32 v[154:155], v[154:155], v[24:25]\n"
      "v_cvt_pk_bf16_f32 v152, v152, v153\n"
      "v_cvt_pk_bf16_f32 v153, v154, v155\n"
      "v_cvt_pk_bf16_f32 v84, v84, v85\n"
      "v_pk_mul_f32 v[216:217], v[216:217], v[26:27]\n"
      "v_pk_mul_f32 v[218:219], v[218:219], v[26:27]\n"
      "v_cvt_pk_bf16_f32 v216, v216, v217\n"
      "v_cvt_pk_bf16_f32 v217, v218, v219\n"
      "v_mfma_f32_16x16x32_bf16 v[104:107], a[168:171], v[72:75], v[104:107]\n"
      "s_nop 0\n"
      "ds_write2st64_b64 v10, v[152:153], v[216:217] offset0:2 offset1:18\n"
      "v_cvt_pk_bf16_f32 v85, v86, v87\n"
      "v_cvt_pk_bf16_f32 v100, v100, v101\n"
      "v_cvt_pk_bf16_f32 v101, v102, v103\n"
      "v_cvt_pk_bf16_f32 v88, v88, v89\n"
      "v_mfma_f32_16x16x32_bf16 a[216:219], a[168:171], v[76:79], a[216:219]\n"
      "s_nop 0\n"
      "s_waitcnt lgkmcnt(10)\n"
      "buffer_store_dwordx4 v[48:51], v7, s[48:51], s84 offen offset:0 nt\n"
      "ds_read_b64_tr_b16 a[168:169], v2 offset:57856\n"
      "v_cvt_pk_bf16_f32 v89, v90, v91\n"
      "v_pk_mul_f32 v[104:105], v[104:105], v[46:47]\n"
      "v_pk_mul_f32 v[106:107], v[106:107], v[46:47]\n"
      "v_cvt_pk_bf16_f32 v104, v104, v105\n"
      "v_cvt_pk_bf16_f32 v105, v106, v107\n"
      "v_cvt_pk_bf16_f32 v92, v92, v93\n"
      "v_cvt_pk_bf16_f32 v93, v94, v95\n"
      "v_mfma_f32_16x16x32_bf16 a[220:223], a[172:175], v[76:79], a[220:223]\n"
      "s_waitcnt lgkmcnt(10)\n"
      "buffer_store_dwordx4 v[52:55], v7, s[48:51], s85 offen offset:0 nt\n"
      "s_waitcnt lgkmcnt(9)\n"
      "buffer_store_dwordx4 v[56:59], v7, s[48:51], s86 offen offset:0 nt\n"
      "s_waitcnt lgkmcnt(8)\n"
      "buffer_store_dwordx4 v[60:63], v7, s[48:51], s87 offen offset:0 nt\n"
      "ds_read_b64_tr_b16 a[170:171], v2 offset:59904\n"
      "ds_write2st64_b64 v9, v[84:85], v[100:101] offset0:32 offset1:34\n"
      "ds_write2st64_b64 v10, v[88:89], v[104:105] offset0:32 offset1:34\n"
      "v_mfma_f32_16x16x32_bf16 v[108:111], a[172:175], v[72:75], v[108:111]\n"
      "s_nop 0\n"
      "v_add_f32 v35, v35, v14\n"
      "v_mov_b32 v14, v35\n"
      "v_accvgpr_read_b32 v16, a192\n"
      "v_accvgpr_read_b32 v17, a193\n"
      "v_permlane16_swap_b32 v35, v14\n"
      "v_add_f32 v35, v35, v14\n"
      "v_rcp_f32 v14, v35\n"
      "v_pk_mul_f32 v[108:109], v[108:109], v[46:47]\n"
      "v_pk_mul_f32 v[110:111], v[110:111], v[46:47]\n"
      "v_cvt_pk_bf16_f32 v108, v108, v109\n"
      "v_cvt_pk_bf16_f32 v109, v110, v111\n"
      "v_mov_b32 v15, v14\n"
      "v_accvgpr_read_b32 v18, a194\n"
      "v_accvgpr_read_b32 v19, a195\n"
      "v_pk_mul_f32 v[16:17], v[16:17], v[14:15]\n"
      "v_cvt_pk_bf16_f32 v16, v16, v17\n"
      "v_mfma_f32_16x16x32_bf16 v[220:223], a[172:175], v[68:71], v[220:223]\n"
      "s_nop 0\n"
      "ds_write2st64_b64 v11, v[92:93], v[108:109] offset0:32 offset1:34\n"
      "v_pk_mul_f32 v[18:19], v[18:19], v[14:15]\n"
      "v_cvt_pk_bf16_f32 v17, v18, v19\n"
      "v_accvgpr_read_b32 v20, a196\n"
      "v_accvgpr_read_b32 v21, a197\n"
      "v_pk_mul_f32 v[20:21], v[20:21], v[14:15]\n"
      "v_accvgpr_read_b32 v22, a198\n"
      "v_pk_mul_f32 v[220:221], v[220:221], v[26:27]\n"
      "v_pk_mul_f32 v[222:223], v[222:223], v[26:27]\n"
      "v_cvt_pk_bf16_f32 v220, v220, v221\n"
      "v_cvt_pk_bf16_f32 v221, v222, v223\n"
      "v_accvgpr_read_b32 v23, a199\n"
      "v_pk_mul_f32 v[22:23], v[22:23], v[14:15]\n"
      "v_cvt_pk_bf16_f32 v18, v20, v21\n"
      "v_mfma_f32_16x16x32_bf16 v[156:159], a[172:175], v[64:67], v[156:159]\n"
      "s_waitcnt lgkmcnt(10)\n"
      "ds_read_b64_tr_b16 a[172:173], v3 offset:57856\n"
      "v_cvt_pk_bf16_f32 v19, v22, v23\n"
      "v_permlane16_swap_b32 v16, v18\n"
      "v_accvgpr_read_b32 v20, a204\n"
      "v_permlane16_swap_b32 v17, v19\n"
      "v_accvgpr_read_b32 v21, a205\n"
      "v_pk_mul_f32 v[20:21], v[20:21], v[14:15]\n"
      "v_pk_mul_f32 v[156:157], v[156:157], v[24:25]\n"
      "v_pk_mul_f32 v[158:159], v[158:159], v[24:25]\n"
      "v_cvt_pk_bf16_f32 v156, v156, v157\n"
      "v_cvt_pk_bf16_f32 v157, v158, v159\n"
      "ds_write2st64_b64 v11, v[156:157], v[220:221] offset0:2 offset1:18\n"
      "ds_read_b128 v[48:51], v6 offset:1024\n"
      "ds_read_b128 v[52:55], v6 offset:5120\n"
      "ds_read_b128 v[56:59], v6 offset:9216\n"
      "ds_read_b128 v[60:63], v6 offset:13312\n"
      "v_mfma_f32_16x16x32_bf16 v[160:163], a[160:163], v[64:67], v[160:163]\n"
      "s_nop 0\n"
      "ds_read_b64_tr_b16 a[174:175], v3 offset:59904\n"
      "v_accvgpr_read_b32 v22, a206\n"
      "v_accvgpr_read_b32 v23, a207\n"
      "v_mfma_f32_16x16x32_bf16 v[224:227], a[160:163], v[68:71], v[224:227]\n"
      "s_nop 0\n"
      "buffer_store_dwordx4 v[16:19], v44, s[48:51], s95 offen offset:0 nt\n"
      "v_cmp_nle_f32 s[72:73], v32, s60\n"
      "v_mfma_f32_16x16x32_bf16 v[112:115], a[160:163], v[72:75], v[112:115]\n"
      "s_or_b64 s[70:71], s[70:71], s[72:73]\n"
      "v_accvgpr_read_b32 v16, a200\n"
      "v_accvgpr_read_b32 v17, a201\n"
      "v_accvgpr_read_b32 v18, a202\n"
      "v_accvgpr_read_b32 v19, a203\n"
      "v_cmp_nle_f32 s[72:73], v33, s60\n"
      "v_pk_mul_f32 v[160:161], v[160:161], v[24:25]\n"
      "v_pk_mul_f32 v[162:163], v[162:163], v[24:25]\n"
      "v_pk_mul_f32 v[224:225], v[224:225], v[26:27]\n"
      "v_pk_mul_f32 v[226:227], v[226:227], v[26:27]\n"
      "v_pk_mul_f32 v[16:17], v[16:17], v[14:15]\n"
      "v_pk_mul_f32 v[18:19], v[18:19], v[14:15]\n"
      "v_pk_mul_f32 v[22:23], v[22:23], v[14:15]\n"
      "v_pk_mul_f32 v[112:113], v[112:113], v[46:47]\n"
      "v_pk_mul_f32 v[114:115], v[114:115], v[46:47]\n"
      "v_cvt_pk_bf16_f32 v160, v160, v161\n"
      "v_cvt_pk_bf16_f32 v161, v162, v163\n"
      "v_mfma_f32_16x16x32_bf16 a[224:227], a[160:163], v[76:79], a[224:227]\n"
      "s_waitcnt lgkmcnt(13)\n"
      "s_waitcnt lgkmcnt(4)\n"
      "buffer_store_dwordx4 v[48:51], v7, s[48:51], s84 offen offset:128 nt\n"
      "s_waitcnt lgkmcnt(3)\n"
      "buffer_store_dwordx4 v[52:55], v7, s[48:51], s85 offen offset:128 nt\n"
      "s_waitcnt lgkmcnt(2)\n"
      "buffer_store_dwordx4 v[56:59], v7, s[48:51], s86 offen offset:128 nt\n"
      "s_waitcnt lgkmcnt(1)\n"
      "buffer_store_dwordx4 v[60:63], v7, s[48:51], s87 offen offset:128 nt\n"
      "v_mfma_f32_16x16x32_bf16 a[228:231], a[164:167], v[76:79], a[228:231]\n"
      "s_or_b64 s[70:71], s[70:71], s[72:73]\n"
      "ds_read_b128 v[48:51], v6 offset:16384\n"
      "ds_read_b128 v[52:55], v6 offset:20480\n"
      "ds_read_b128 v[56:59], v6 offset:17408\n"
      "ds_read_b128 v[60:63], v6 offset:21504\n"
      "v_cvt_pk_bf16_f32 v224, v224, v225\n"
      "v_cvt_pk_bf16_f32 v225, v226, v227\n"
      "v_cvt_pk_bf16_f32 v16, v16, v17\n"
      "v_cvt_pk_bf16_f32 v17, v18, v19\n"
      "v_cvt_pk_bf16_f32 v18, v20, v21\n"
      "v_cvt_pk_bf16_f32 v19, v22, v23\n"
      "v_mfma_f32_16x16x32_bf16 v[116:119], a[164:167], v[72:75], v[116:119]\n"
      "s_nop 0\n"
      "ds_read_b64_tr_b16 a[160:161], v2 offset:61440\n"
      "ds_write2st64_b64 v8, v[160:161], v[224:225] offset0:4 offset1:20\n"
      "v_permlane16_swap_b32 v16, v18\n"
      "v_permlane16_swap_b32 v17, v19\n"
      "v_mfma_f32_16x16x32_bf16 v[228:231], a[164:167], v[68:71], v[228:231]\n"
      "s_nop 0\n"
      "ds_read_b64_tr_b16 a[162:163], v2 offset:63488\n"
      "v_accvgpr_read_b32 v20, a212\n"
      "v_accvgpr_read_b32 v21, a213\n"
      "v_accvgpr_read_b32 v22, a214\n"
      "v_accvgpr_read_b32 v23, a215\n"
      "v_pk_mul_f32 v[20:21], v[20:21], v[14:15]\n"
      "v_pk_mul_f32 v[22:23], v[22:23], v[14:15]\n"
      "v_pk_mul_f32 v[228:229], v[228:229], v[26:27]\n"
      "v_pk_mul_f32 v[230:231], v[230:231], v[26:27]\n"
      "v_pk_mul_f32 v[116:117], v[116:117], v[46:47]\n"
      "v_pk_mul_f32 v[118:119], v[118:119], v[46:47]\n"
      "v_cvt_pk_bf16_f32 v228, v228, v229\n"
      "v_cvt_pk_bf16_f32 v229, v230, v231\n"
      "v_cvt_pk_bf16_f32 v112, v112, v113\n"
      "v_cvt_pk_bf16_f32 v113, v114, v115\n"
      "v_cvt_pk_bf16_f32 v116, v116, v117\n"
      "v_cvt_pk_bf16_f32 v117, v118, v119\n"
      "v_mfma_f32_16x16x32_bf16 v[164:167], a[164:167], v[64:67], v[164:167]\n"
      "s_nop 0\n"
      "ds_read_b64_tr_b16 a[164:165], v3 offset:61440\n"
      "v_cmp_nle_f32 s[72:73], v34, s60\n"
      "v_mfma_f32_16x16x32_bf16 v[168:171], a[168:171], v[64:67], v[168:171]\n"
      "s_or_b64 s[70:71], s[70:71], s[72:73]\n"
      "ds_read_b64_tr_b16 a[166:167], v3 offset:63488\n"
      "v_cmp_nle_f32 s[72:73], v35, s60\n"
      "v_mfma_f32_16x16x32_bf16 v[232:235], a[168:171], v[68:71], v[232:235]\n"
      "s_or_b64 s[70:71], s[70:71], s[72:73]\n"
      "v_pk_mul_f32 v[164:165], v[164:165], v[24:25]\n"
      "v_pk_mul_f32 v[166:167], v[166:167], v[24:25]\n"
      "v_cvt_pk_bf16_f32 v164, v164, v165\n"
      "v_cvt_pk_bf16_f32 v165, v166, v167\n"
      "ds_write2st64_b64 v9, v[164:165], v[228:229] offset0:4 offset1:20\n"
      "v_pk_mul_f32 v[168:169], v[168:169], v[24:25]\n"
      "v_pk_mul_f32 v[170:171], v[170:171], v[24:25]\n"
      "v_pk_mul_f32 v[232:233], v[232:233], v[26:27]\n"
      "v_pk_mul_f32 v[234:235], v[234:235], v[26:27]\n"
      "v_cvt_pk_bf16_f32 v168, v168, v169\n"
      "v_cvt_pk_bf16_f32 v169, v170, v171\n"
      "v_cvt_pk_bf16_f32 v232, v232, v233\n"
      "v_cvt_pk_bf16_f32 v233, v234, v235\n"
      "ds_write2st64_b64 v10, v[168:169], v[232:233] offset0:4 offset1:20\n"
      "v_mfma_f32_16x16x32_bf16 v[120:123], a[168:171], v[72:75], v[120:123]\n"
      "s_cmp_lg_u64 s[70:71], 0\n"
      "s_waitcnt lgkmcnt(10)\n"
      "buffer_store_dwordx4 v[48:51], v7, s[48:51], s88 offen offset:0 nt\n"
      "s_waitcnt lgkmcnt(9)\n"
      "buffer_store_dwordx4 v[52:55], v7, s[48:51], s89 offen offset:0 nt\n"
      "s_waitcnt lgkmcnt(8)\n"
      "buffer_store_dwordx4 v[56:59], v7, s[48:51], s88 offen offset:128 nt\n"
      "s_waitcnt lgkmcnt(7)\n"
      "buffer_store_dwordx4 v[60:63], v7, s[48:51], s89 offen offset:128 nt\n"
      "v_pk_mul_f32 v[120:121], v[120:121], v[46:47]\n"
      "v_pk_mul_f32 v[122:123], v[122:123], v[46:47]\n"
      "v_cvt_pk_bf16_f32 v120, v120, v121\n"
      "v_cvt_pk_bf16_f32 v121, v122, v123\n"
      "v_mfma_f32_16x16x32_bf16 a[232:235], a[168:171], v[76:79], a[232:235]\n"
      "s_cselect_b32 s78, s68, 0\n"
      "buffer_store_dwordx4 v[16:19], v44, s[48:51], s95 offen offset:64 nt\n"
      "ds_read_b64_tr_b16 a[168:169], v2 offset:61952\n"
      "v_mfma_f32_16x16x32_bf16 a[236:239], a[172:175], v[76:79], a[236:239]\n"
      "s_or_b32 s67, s67, s78\n"
      "ds_read_b64_tr_b16 a[170:171], v2 offset:64000\n"
      "v_accvgpr_read_b32 v16, a208\n"
      "v_accvgpr_read_b32 v17, a209\n"
      "v_accvgpr_read_b32 v18, a210\n"
      "v_accvgpr_read_b32 v19, a211\n"
      "v_pk_mul_f32 v[16:17], v[16:17], v[14:15]\n"
      "v_pk_mul_f32 v[18:19], v[18:19], v[14:15]\n"
      "v_cvt_pk_bf16_f32 v16, v16, v17\n"
      "v_cvt_pk_bf16_f32 v17, v18, v19\n"
      "v_cvt_pk_bf16_f32 v18, v20, v21\n"
      "v_cvt_pk_bf16_f32 v19, v22, v23\n"
      "v_accvgpr_read_b32 v20, a220\n"
      "v_permlane16_swap_b32 v16, v18\n"
      "v_permlane16_swap_b32 v17, v19\n"
      "v_accvgpr_read_b32 v21, a221\n"
      "v_accvgpr_read_b32 v22, a222\n"
      "v_accvgpr_read_b32 v23, a223\n"
      "v_pk_mul_f32 v[20:21], v[20:21], v[14:15]\n"
      "v_pk_mul_f32 v[22:23], v[22:23], v[14:15]\n"
      "s_nop 0\n"
      "v_mfma_f32_16x16x32_bf16 v[124:127], a[172:175], v[72:75], v[124:127]\n"
      "s_lshl_b32 s68, s68, 1\n"
      "buffer_store_dwordx4 v[16:19], v44, s[48:51], s95 offen offset:128 nt\n"
      "v_mfma_f32_16x16x32_bf16 v[236:239], a[172:175], v[68:71], v[236:239]\n"
      "s_mov_b64 s[70:71], 0\n"
      "v_accvgpr_read_b32 v16, a216\n"
      "v_accvgpr_read_b32 v17, a217\n"
      "v_accvgpr_read_b32 v18, a218\n"
      "v_accvgpr_read_b32 v19, a219\n"
      "v_pk_mul_f32 v[16:17], v[16:17], v[14:15]\n"
      "v_pk_mul_f32 v[18:19], v[18:19], v[14:15]\n"
      "v_pk_mul_f32 v[124:125], v[124:125], v[46:47]\n"
      "v_pk_mul_f32 v[236:237], v[236:237], v[26:27]\n"
      "v_pk_mul_f32 v[238:239], v[238:239], v[26:27]\n"
      "v_pk_mul_f32 v[126:127], v[126:127], v[46:47]\n"
      "v_cvt_pk_bf16_f32 v236, v236, v237\n"
      "v_cvt_pk_bf16_f32 v237, v238, v239\n"
      "v_cvt_pk_bf16_f32 v16, v16, v17\n"
      "v_cvt_pk_bf16_f32 v17, v18, v19\n"
      "v_cvt_pk_bf16_f32 v18, v20, v21\n"
      "v_cvt_pk_bf16_f32 v19, v22, v23\n"
      "v_accvgpr_read_b32 v20, a228\n"
      "v_permlane16_swap_b32 v16, v18\n"
      "v_permlane16_swap_b32 v17, v19\n"
      "v_accvgpr_read_b32 v21, a229\n"
      "v_pk_mul_f32 v[20:21], v[20:21], v[14:15]\n"
      "v_accvgpr_read_b32 v22, a230\n"
      "v_accvgpr_read_b32 v23, a231\n"
      "v_pk_mul_f32 v[22:23], v[22:23], v[14:15]\n"
      "v_cvt_pk_bf16_f32 v124, v124, v125\n"
      "v_cvt_pk_bf16_f32 v125, v126, v127\n"
      "v_mfma_f32_16x16x32_bf16 v[172:175], a[172:175], v[64:67], v[172:175]\n"
      "s_waitcnt lgkmcnt(6)\n"
      "buffer_store_dwordx4 v[16:19], v44, s[48:51], s95 offen offset:192 nt\n"
      "ds_read_b64_tr_b16 a[172:173], v3 offset:61952\n"
      "v_mfma_f32_16x16x32_bf16 v[176:179], a[160:163], v[64:67], v[176:179]\n"
      "s_nop 0\n"
      "ds_read_b64_tr_b16 a[174:175], v3 offset:64000\n"
      "v_accvgpr_read_b32 v16, a224\n"
      "v_accvgpr_read_b32 v17, a225\n"
      "v_pk_mul_f32 v[172:173], v[172:173], v[24:25]\n"
      "v_pk_mul_f32 v[174:175], v[174:175], v[24:25]\n"
      "v_pk_mul_f32 v[16:17], v[16:17], v[14:15]\n"
      "v_cvt_pk_bf16_f32 v172, v172, v173\n"
      "v_pk_mul_f32 v[176:177], v[176:177], v[24:25]\n"
      "v_pk_mul_f32 v[178:179], v[178:179], v[24:25]\n"
      "v_cvt_pk_bf16_f32 v173, v174, v175\n"
      "ds_write2st64_b64 v11, v[172:173], v[236:237] offset0:4 offset1:20\n"
      "ds_read_b128 v[48:51], v6 offset:2048\n"
      "ds_read_b128 v[52:55], v6 offset:6144\n"
      "ds_read_b128 v[56:59], v6 offset:10240\n"
      "ds_read_b128 v[60:63], v6 offset:14336\n"
      "v_cvt_pk_bf16_f32 v176, v176, v177\n"
      "v_cvt_pk_bf16_f32 v177, v178, v179\n"
      "v_accvgpr_read_b32 v18, a226\n"
      "v_accvgpr_read_b32 v19, a227\n"
      "v_pk_mul_f32 v[18:19], v[18:19], v[14:15]\n"
      "v_cvt_pk_bf16_f32 v16, v16, v17\n"
      "v_cvt_pk_bf16_f32 v17, v18, v19\n"
      "v_cvt_pk_bf16_f32 v18, v20, v21\n"
      "v_cvt_pk_bf16_f32 v19, v22, v23\n"
      "v_accvgpr_read_b32 v20, a236\n"
      "v_permlane16_swap_b32 v16, v18\n"
      "v_permlane16_swap_b32 v17, v19\n"
      "v_accvgpr_read_b32 v21, a237\n"
      "v_pk_mul_f32 v[20:21], v[20:21], v[14:15]\n"
      "v_accvgpr_read_b32 v22, a238\n"
      "v_accvgpr_read_b32 v23, a239\n"
      "v_pk_mul_f32 v[22:23], v[22:23], v[14:15]\n"
      "s_nop 0\n"
      "v_mfma_f32_16x16x32_bf16 v[240:243], a[160:163], v[68:71], v[240:243]\n"
      "s_nop 0\n"
      "s_waitcnt lgkmcnt(3)\n"
      "buffer_store_dwordx4 v[48:51], v7, s[48:51], s84 offen offset:256 nt\n"
      "s_waitcnt lgkmcnt(2)\n"
      "buffer_store_dwordx4 v[52:55], v7, s[48:51], s85 offen offset:256 nt\n"
      "s_waitcnt lgkmcnt(1)\n"
      "buffer_store_dwordx4 v[56:59], v7, s[48:51], s86 offen offset:256 nt\n"
      "s_waitcnt lgkmcnt(0)\n"
      "buffer_store_dwordx4 v[60:63], v7, s[48:51], s87 offen offset:256 nt\n"
      "v_pk_mul_f32 v[240:241], v[240:241], v[26:27]\n"
      "v_pk_mul_f32 v[242:243], v[242:243], v[26:27]\n"
      "v_cvt_pk_bf16_f32 v240, v240, v241\n"
      "v_cvt_pk_bf16_f32 v241, v242, v243\n"
      "ds_write2st64_b64 v8, v[176:177], v[240:241] offset0:6 offset1:22\n"
      "v_mfma_f32_16x16x32_bf16 a[176:179], a[160:163], v[72:75], a[176:179]\n"
      "buffer_store_dwordx4 v[16:19], v44, s[48:51], s95 offen offset:256 nt\n"
      "v_mfma_f32_16x16x32_bf16 a[240:243], a[160:163], v[76:79], a[240:243]\n"
      "s_nop 0\n"
      "v_accvgpr_read_b32 v16, a232\n"
      "v_accvgpr_read_b32 v17, a233\n"
      "v_accvgpr_read_b32 v18, a234\n"
      "v_accvgpr_read_b32 v19, a235\n"
      "v_pk_mul_f32 v[16:17], v[16:17], v[14:15]\n"
      "v_pk_mul_f32 v[18:19], v[18:19], v[14:15]\n"
      "v_cvt_pk_bf16_f32 v16, v16, v17\n"
      "v_cvt_pk_bf16_f32 v17, v18, v19\n"
      "v_cvt_pk_bf16_f32 v18, v20, v21\n"
      "v_cvt_pk_bf16_f32 v19, v22, v23\n"
      "v_accvgpr_read_b32 v20, a176\n"
      "v_permlane16_swap_b32 v16, v18\n"
      "v_permlane16_swap_b32 v17, v19\n"
      "v_accvgpr_read_b32 v21, a177\n"
      "v_accvgpr_read_b32 v22, a178\n"
      "v_accvgpr_read_b32 v23, a179\n"
      "v_pk_mul_f32 v[20:21], v[20:21], v[46:47]\n"
      "v_pk_mul_f32 v[22:23], v[22:23], v[46:47]\n"
      "v_cvt_pk_bf16_f32 v20, v20, v21\n"
      "v_cvt_pk_bf16_f32 v21, v22, v23\n"
      "v_mfma_f32_16x16x32_bf16 a[244:247], a[164:167], v[76:79], a[244:247]\n"
      "s_nop 0\n"
      "buffer_store_dwordx4 v[16:19], v44, s[48:51], s95 offen offset:320 nt\n"
      "ds_write2st64_b64 v8, v[112:113], v[20:21] offset0:36 offset1:38\n"
      "v_mfma_f32_16x16x32_bf16 a[180:183], a[164:167], v[72:75], a[180:183]\n"
      "s_nop 0\n"
      "s_nop 0\n"
      "s_nop 0\n"
      "v_mfma_f32_16x16x32_bf16 v[244:247], a[164:167], v[68:71], v[244:247]\n"
      "s_nop 0\n"
      "v_accvgpr_read_b32 v20, a244\n"
      "v_accvgpr_read_b32 v21, a245\n"
      "v_accvgpr_read_b32 v22, a246\n"
      "v_accvgpr_read_b32 v16, a180\n"
      "v_accvgpr_read_b32 v17, a181\n"
      "v_pk_mul_f32 v[16:17], v[16:17], v[46:47]\n"
      "v_pk_mul_f32 v[20:21], v[20:21], v[14:15]\n"
      "v_pk_mul_f32 v[244:245], v[244:245], v[26:27]\n"
      "v_pk_mul_f32 v[246:247], v[246:247], v[26:27]\n"
      "v_cvt_pk_bf16_f32 v244, v244, v245\n"
      "v_cvt_pk_bf16_f32 v245, v246, v247\n"
      "v_accvgpr_read_b32 v18, a182\n"
      "v_accvgpr_read_b32 v19, a183\n"
      "v_pk_mul_f32 v[18:19], v[18:19], v[46:47]\n"
      "v_cvt_pk_bf16_f32 v16, v16, v17\n"
      "v_cvt_pk_bf16_f32 v17, v18, v19\n"
      "ds_write2st64_b64 v9, v[116:117], v[16:17] offset0:36 offset1:38\n"
      "v_accvgpr_read_b32 v16, a240\n"
      "v_accvgpr_read_b32 v17, a241\n"
      "v_pk_mul_f32 v[16:17], v[16:17], v[14:15]\n"
      "v_accvgpr_read_b32 v18, a242\n"
      "v_accvgpr_read_b32 v19, a243\n"
      "v_pk_mul_f32 v[18:19], v[18:19], v[14:15]\n"
      "v_cvt_pk_bf16_f32 v16, v16, v17\n"
      "v_cvt_pk_bf16_f32 v17, v18, v19\n"
      "v_accvgpr_read_b32 v23, a247\n"
      "v_pk_mul_f32 v[22:23], v[22:23], v[14:15]\n"
      "v_cvt_pk_bf16_f32 v18, v20, v21\n"
      "v_cvt_pk_bf16_f32 v19, v22, v23\n"
      "v_mfma_f32_16x16x32_bf16 v[180:183], a[164:167], v[64:67], v[180:183]\n"
      "s_nop 0\n"
      "v_permlane16_swap_b32 v16, v18\n"
      "v_permlane16_swap_b32 v17, v19\n"
      "buffer_store_dwordx4 v[16:19], v44, s[48:51], s95 offen offset:384 nt\n"
      "v_mfma_f32_16x16x32_bf16 v[184:187], a[168:171], v[64:67], v[184:187]\n"
      "s_nop 0\n"
      "s_nop 0\n"
      "s_nop 0\n"
      "v_pk_mul_f32 v[180:181], v[180:181], v[24:25]\n"
      "v_pk_mul_f32 v[182:183], v[182:183], v[24:25]\n"
      "v_cvt_pk_bf16_f32 v180, v180, v181\n"
      "v_cvt_pk_bf16_f32 v181, v182, v183\n"
      "ds_write2st64_b64 v9, v[180:181], v[244:245] offset0:6 offset1:22\n"
      "v_pk_mul_f32 v[184:185], v[184:185], v[24:25]\n"
      "v_pk_mul_f32 v[186:187], v[186:187], v[24:25]\n"
      "v_cvt_pk_bf16_f32 v184, v184, v185\n"
      "v_cvt_pk_bf16_f32 v185, v186, v187\n"
      "v_mfma_f32_16x16x32_bf16 v[248:251], a[168:171], v[68:71], v[248:251]\n"
      "s_nop 0\n"
      "s_nop 0\n"
      "s_nop 0\n"
      "v_mfma_f32_16x16x32_bf16 a[184:187], a[168:171], v[72:75], a[184:187]\n"
      "s_nop 0\n"
      "s_nop 0\n"
      "s_nop 0\n"
      "v_mfma_f32_16x16x32_bf16 a[248:251], a[168:171], v[76:79], a[248:251]\n"
      "s_nop 0\n"
      "v_pk_mul_f32 v[248:249], v[248:249], v[26:27]\n"
      "v_pk_mul_f32 v[250:251], v[250:251], v[26:27]\n"
      "v_cvt_pk_bf16_f32 v248, v248, v249\n"
      "v_cvt_pk_bf16_f32 v249, v250, v251\n"
      "ds_write2st64_b64 v10, v[184:185], v[248:249] offset0:6 offset1:22\n"
      "v_accvgpr_read_b32 v20, a184\n"
      "v_accvgpr_read_b32 v21, a185\n"
      "v_pk_mul_f32 v[20:21], v[20:21], v[46:47]\n"
      "v_accvgpr_read_b32 v22, a186\n"
      "v_accvgpr_read_b32 v23, a187\n"
      "v_pk_mul_f32 v[22:23], v[22:23], v[46:47]\n"
      "v_cvt_pk_bf16_f32 v20, v20, v21\n"
      "v_cvt_pk_bf16_f32 v21, v22, v23\n"
      "ds_write2st64_b64 v10, v[120:121], v[20:21] offset0:36 offset1:38\n"
      "v_mfma_f32_16x16x32_bf16 a[252:255], a[172:175], v[76:79], a[252:255]\n"
      "s_nop 0\n"
      "s_nop 0\n"
      "s_nop 0\n"
      "v_mfma_f32_16x16x32_bf16 a[188:191], a[172:175], v[72:75], a[188:191]\n"
      "s_nop 0\n"
      "s_nop 0\n"
      "s_nop 0\n"
      "v_mfma_f32_16x16x32_bf16 v[252:255], a[172:175], v[68:71], v[252:255]\n"
      "s_nop 0\n"
      "v_accvgpr_read_b32 v20, a252\n"
      "v_accvgpr_read_b32 v21, a253\n"
      "v_accvgpr_read_b32 v22, a254\n"
      "v_accvgpr_read_b32 v16, a188\n"
      "v_accvgpr_read_b32 v17, a189\n"
      "v_pk_mul_f32 v[16:17], v[16:17], v[46:47]\n"
      "v_pk_mul_f32 v[20:21], v[20:21], v[14:15]\n"
      "v_pk_mul_f32 v[252:253], v[252:253], v[26:27]\n"
      "v_pk_mul_f32 v[254:255], v[254:255], v[26:27]\n"
      "v_cvt_pk_bf16_f32 v252, v252, v253\n"
      "v_cvt_pk_bf16_f32 v253, v254, v255\n"
      "v_accvgpr_read_b32 v18, a190\n"
      "v_accvgpr_read_b32 v19, a191\n"
      "v_pk_mul_f32 v[18:19], v[18:19], v[46:47]\n"
      "v_cvt_pk_bf16_f32 v16, v16, v17\n"
      "v_cvt_pk_bf16_f32 v17, v18, v19\n"
      "ds_write2st64_b64 v11, v[124:125], v[16:17] offset0:36 offset1:38\n"
      "v_accvgpr_read_b32 v16, a248\n"
      "v_accvgpr_read_b32 v17, a249\n"
      "v_pk_mul_f32 v[16:17], v[16:17], v[14:15]\n"
      "v_accvgpr_read_b32 v18, a250\n"
      "v_accvgpr_read_b32 v19, a251\n"
      "v_pk_mul_f32 v[18:19], v[18:19], v[14:15]\n"
      "v_cvt_pk_bf16_f32 v16, v16, v17\n"
      "v_cvt_pk_bf16_f32 v17, v18, v19\n"
      "v_accvgpr_read_b32 v23, a255\n"
      "v_pk_mul_f32 v[22:23], v[22:23], v[14:15]\n"
      "v_cvt_pk_bf16_f32 v18, v20, v21\n"
      "v_cvt_pk_bf16_f32 v19, v22, v23\n"
      "v_mfma_f32_16x16x32_bf16 v[188:191], a[172:175], v[64:67], v[188:191]\n"
      "s_nop 0\n"
      "v_permlane16_swap_b32 v16, v18\n"
      "v_permlane16_swap_b32 v17, v19\n"
      "buffer_store_dwordx4 v[16:19], v44, s[48:51], s95 offen offset:448 nt\n"
      "s_nop 3\n"
      "v_pk_mul_f32 v[188:189], v[188:189], v[24:25]\n"
      "v_pk_mul_f32 v[190:191], v[190:191], v[24:25]\n"
      "v_cvt_pk_bf16_f32 v188, v188, v189\n"
      "v_cvt_pk_bf16_f32 v189, v190, v191\n"
      "ds_write2st64_b64 v11, v[188:189], v[252:253] offset0:6 offset1:22\n"
      "ds_read_b128 v[48:51], v6 offset:3072\n"
      "s_waitcnt lgkmcnt(0)\n"
      "buffer_store_dwordx4 v[48:51], v7, s[48:51], s84 offen offset:384 nt\n"
      "ds_read_b128 v[52:55], v6 offset:7168\n"
      "s_waitcnt lgkmcnt(0)\n"
      "buffer_store_dwordx4 v[52:55], v7, s[48:51], s85 offen offset:384 nt\n"
      "ds_read_b128 v[56:59], v6 offset:11264\n"
      "s_waitcnt lgkmcnt(0)\n"
      "buffer_store_dwordx4 v[56:59], v7, s[48:51], s86 offen offset:384 nt\n"
      "ds_read_b128 v[60:63], v6 offset:15360\n"
      "s_waitcnt lgkmcnt(0)\n"
      "buffer_store_dwordx4 v[60:63], v7, s[48:51], s87 offen offset:384 nt\n"
      "ds_read_b128 v[48:51], v6 offset:18432\n"
      "s_waitcnt lgkmcnt(0)\n"
      "buffer_store_dwordx4 v[48:51], v7, s[48:51], s88 offen offset:256 nt\n"
      "ds_read_b128 v[52:55], v6 offset:22528\n"
      "s_waitcnt lgkmcnt(0)\n"
      "buffer_store_dwordx4 v[52:55], v7, s[48:51], s89 offen offset:256 nt\n"
      "ds_read_b128 v[56:59], v6 offset:19456\n"
      "s_waitcnt lgkmcnt(0)\n"
      "buffer_store_dwordx4 v[56:59], v7, s[48:51], s88 offen offset:384 nt\n"
      "ds_read_b128 v[60:63], v6 offset:23552\n"
      "s_waitcnt lgkmcnt(0)\n"
      "buffer_store_dwordx4 v[60:63], v7, s[48:51], s89 offen offset:384 nt\n"
      "s_branch LDONE_%=\n"
      "LEND3_%=:\n"
      "v_mfma_f32_16x16x32_bf16 v[176:179], a[160:163], v[48:51], v[176:179]\n"
      "s_and_b32 s79, s63, 0xffffe000\n"
      "v_mov_b32 v24, v32\n"
      "v_mov_b32 v26, v33\n"
      "v_mfma_f32_16x16x32_bf16 v[240:243], a[160:163], v[52:55], v[240:243]\n"
      "s_add_u32 s48, s82, s79\n"
      "v_permlane32_swap_b32 v32, v24\n"
      "v_mfma_f32_16x16x32_bf16 a[176:179], a[160:163], v[56:59], a[176:179]\n"
      "s_addc_u32 s49, s83, 0\n"
      "ds_read_b64_tr_b16 a[160:161], v2 offset:49152\n"
      "v_add_f32 v32, v32, v24\n"
      "v_mfma_f32_16x16x32_bf16 a[180:183], a[164:167], v[56:59], a[180:183]\n"
      "s_and_b32 s79, s63, 0xf8\n"
      "ds_read_b64_tr_b16 a[162:163], v2 offset:51200\n"
      "v_mov_b32 v24, v32\n"
      "v_mfma_f32_16x16x32_bf16 v[244:247], a[164:167], v[52:55], v[244:247]\n"
      "s_lshl_b32 s50, s79, 10\n"
      "v_permlane32_swap_b32 v33, v26\n"
      "v_add_f32 v33, v33, v26\n"
      "v_mfma_f32_16x16x32_bf16 v[180:183], a[164:167], v[48:51], v[180:183]\n"
      "s_and_b32 s79, s63, 7\n"
      "ds_read_b64_tr_b16 a[164:165], v3 offset:49152\n"
      "v_mov_b32 v26, v33\n"
      "v_fma_f32 v40, v40, s59, v30\n"
      "v_fma_f32 v41, v41, s59, v30\n"
      "v_mfma_f32_16x16x32_bf16 v[184:187], a[168:171], v[48:51], v[184:187]\n"
      "s_mul_i32 s79, s79, s93\n"
      "ds_read_b64_tr_b16 a[166:167], v3 offset:51200\n"
      "v_fma_f32 v42, v42, s59, v30\n"
      "v_fma_f32 v43, v43, s59, v30\n"
      "v_mfma_f32_16x16x32_bf16 v[248:251], a[168:171], v[52:55], v[248:251]\n"
      "s_lshl_b32 s91, s79, 13\n"
      "v_permlane16_swap_b32 v32, v24\n"
      "v_permlane16_swap_b32 v33, v26\n"
      "v_add_f32 v32, v32, v24\n"
      "v_mfma_f32_16x16x32_bf16 a[184:187], a[168:171], v[56:59], a[184:187]\n"
      "s_add_u32 s95, s91, 0x6000\n"
      "ds_read_b64_tr_b16 a[168:169], v2 offset:49664\n"
      "v_exp_f32 v40, v40\n"
      "v_exp_f32 v41, v41\n"
      "v_add_f32 v33, v33, v26\n"
      "v_mfma_f32_16x16x32_bf16 a[188:191], a[172:175], v[56:59], a[188:191]\n"
      "s_add_u32 s84, s91, 0x0\n"
      "ds_read_b64_tr_b16 a[170:171], v2 offset:51712\n"
      "v_exp_f32 v42, v42\n"
      "v_exp_f32 v43, v43\n"
      "v_cvt_pk_bf16_f32 v74, v40, v41\n"
      "v_cvt_pk_bf16_f32 v75, v42, v43\n"
      "v_add_f32 v34, v34, v40\n"
      "v_mfma_f32_16x16x32_bf16 v[252:255], a[172:175], v[52:55], v[252:255]\n"
      "s_add_u32 s85, s91, 0x1000\n"
      "v_rcp_f32 v24, v32\n"
      "v_rcp_f32 v26, v33\n"
      "v_mov_b32 v25, v24\n"
      "v_mov_b32 v27, v26\n"
      "v_add_f32 v34, v34, v41\n"
      "v_add_f32 v34, v34, v42\n"
      "v_add_f32 v34, v34, v43\n"
      "v_mfma_f32_16x16x32_bf16 v[188:191], a[172:175], v[48:51], v[188:191]\n"
      "s_waitcnt lgkmcnt(4)\n"
      "ds_read_b64_tr_b16 a[172:173], v3 offset:49664\n"
      "v_mov_b32 v46, v34\n"
      "v_cmp_nle_f32 s[72:73], v32, s60\n"
      "v_mfma_f32_16x16x32_bf16 v[128:131], a[160:163], v[64:67], v[128:131]\n"
      "s_add_u32 s86, s91, 0x2000\n"
      "ds_read_b64_tr_b16 a[174:175], v3 offset:51712\n"
      "v_permlane32_swap_b32 v34, v46\n"
      "v_add_f32 v34, v34, v46\n"
      "v_mov_b32 v46, v34\n"
      "v_mfma_f32_16x16x32_bf16 v[192:195], a[160:163], v[68:71], v[192:195]\n"
      "s_add_u32 s87, s91, 0x3000\n"
      "v_permlane16_swap_b32 v34, v46\n"
      "v_pk_mul_f32 v[128:129], v[128:129], v[24:25]\n"
      "v_pk_mul_f32 v[130:131], v[130:131], v[24:25]\n"
      "v_cvt_pk_bf16_f32 v128, v128, v129\n"
      "v_cvt_pk_bf16_f32 v129, v130, v131\n"
      "v_add_f32 v34, v34, v46\n"
      "v_rcp_f32 v46, v34\n"
      "v_pk_mul_f32 v[192:193], v[192:193], v[26:27]\n"
      "v_pk_mul_f32 v[194:195], v[194:195], v[26:27]\n"
      "v_cvt_pk_bf16_f32 v192, v192, v193\n"
      "v_cvt_pk_bf16_f32 v193, v194, v195\n"
      "v_mfma_f32_16x16x32_bf16 v[80:83], a[160:163], v[72:75], v[80:83]\n"
      "s_waitcnt lgkmcnt(4)\n"
      "ds_write2st64_b64 v8, v[128:129], v[192:193] offset0:0 offset1:16\n"
      "ds_read_b64_tr_b16 a[160:161], v2 offset:53248\n"
      "v_mov_b32 v47, v46\n"
      "v_mfma_f32_16x16x32_bf16 v[84:87], a[164:167], v[72:75], v[84:87]\n"
      "s_add_u32 s88, s91, 0x4000\n"
      "ds_read_b64_tr_b16 a[162:163], v2 offset:55296\n"
      "s_nop 0\n"
      "v_mfma_f32_16x16x32_bf16 v[196:199], a[164:167], v[68:71], v[196:199]\n"
      "s_add_u32 s89, s91, 0x5000\n"
      "s_nop 0\n"
      "s_nop 0\n"
      "v_mfma_f32_16x16x32_bf16 v[132:135], a[164:167], v[64:67], v[132:135]\n"
      "s_waitcnt lgkmcnt(5)\n"
      "ds_read_b64_tr_b16 a[164:165], v3 offset:53248\n"
      "s_nop 0\n"
      "v_mfma_f32_16x16x32_bf16 v[136:139], a[168:171], v[64:67], v[136:139]\n"
      "s_or_b64 s[70:71], s[70:71], s[72:73]\n"
      "ds_read_b64_tr_b16 a[166:167], v3 offset:55296\n"
      "v_pk_mul_f32 v[196:197], v[196:197], v[26:27]\n"
      "v_pk_mul_f32 v[198:199], v[198:199], v[26:27]\n"
      "v_pk_mul_f32 v[132:133], v[132:133], v[24:25]\n"
      "v_pk_mul_f32 v[134:135], v[134:135], v[24:25]\n"
      "v_pk_mul_f32 v[80:81], v[80:81], v[46:47]\n"
      "v_pk_mul_f32 v[82:83], v[82:83], v[46:47]\n"
      "v_pk_mul_f32 v[136:137], v[136:137], v[24:25]\n"
      "v_pk_mul_f32 v[138:139], v[138:139], v[24:25]\n"
      "v_pk_mul_f32 v[84:85], v[84:85], v[46:47]\n"
      "v_pk_mul_f32 v[86:87], v[86:87], v[46:47]\n"
      "v_cvt_pk_bf16_f32 v132, v132, v133\n"
      "v_cvt_pk_bf16_f32 v133, v134, v135\n"
      "v_cvt_pk_bf16_f32 v196, v196, v197\n"
      "v_cvt_pk_bf16_f32 v197, v198, v199\n"
      "ds_write2st64_b64 v9, v[132:133], v[196:197] offset0:0 offset1:16\n"
      "v_cvt_pk_bf16_f32 v136, v136, v137\n"
      "v_cvt_pk_bf16_f32 v137, v138, v139\n"
      "v_cvt_pk_bf16_f32 v80, v80, v81\n"
      "v_cvt_pk_bf16_f32 v81, v82, v83\n"
      "v_cvt_pk_bf16_f32 v84, v84, v85\n"
      "v_cvt_pk_bf16_f32 v85, v86, v87\n"
      "v_cmp_nle_f32 s[72:73], v33, s60\n"
      "v_mfma_f32_16x16x32_bf16 v[200:203], a[168:171], v[68:71], v[200:203]\n"
      "s_or_b64 s[70:71], s[70:71], s[72:73]\n"
      "v_cmp_nle_f32 s[72:73], v34, s60\n"
      "s_nop 0\n"
      "v_mfma_f32_16x16x32_bf16 v[88:91], a[168:171], v[72:75], v[88:91]\n"
      "s_waitcnt lgkmcnt(6)\n"
      "ds_read_b64_tr_b16 a[168:169], v2 offset:53760\n"
      "ds_read_b64_tr_b16 a[170:171], v2 offset:55808\n"
      "v_mfma_f32_16x16x32_bf16 v[92:95], a[172:175], v[72:75], v[92:95]\n"
      "s_or_b64 s[70:71], s[70:71], s[72:73]\n"
      "v_pk_mul_f32 v[200:201], v[200:201], v[26:27]\n"
      "v_pk_mul_f32 v[202:203], v[202:203], v[26:27]\n"
      "v_cvt_pk_bf16_f32 v200, v200, v201\n"
      "v_cvt_pk_bf16_f32 v201, v202, v203\n"
      "ds_write2st64_b64 v10, v[136:137], v[200:201] offset0:0 offset1:16\n"
      "v_pk_mul_f32 v[88:89], v[88:89], v[46:47]\n"
      "v_pk_mul_f32 v[90:91], v[90:91], v[46:47]\n"
      "v_pk_mul_f32 v[92:93], v[92:93], v[46:47]\n"
      "v_pk_mul_f32 v[94:95], v[94:95], v[46:47]\n"
      "v_cvt_pk_bf16_f32 v88, v88, v89\n"
      "v_cvt_pk_bf16_f32 v89, v90, v91\n"
      "v_cvt_pk_bf16_f32 v92, v92, v93\n"
      "v_cvt_pk_bf16_f32 v93, v94, v95\n"
      "v_mfma_f32_16x16x32_bf16 v[204:207], a[172:175], v[68:71], v[204:207]\n"
      "s_cmp_lg_u64 s[70:71], 0\n"
      "s_nop 0\n"
      "s_nop 0\n"
      "v_mfma_f32_16x16x32_bf16 v[140:143], a[172:175], v[64:67], v[140:143]\n"
      "s_cselect_b32 s78, s68, 0\n"
      "ds_read_b64_tr_b16 a[172:173], v3 offset:53760\n"
      "ds_read_b64_tr_b16 a[174:175], v3 offset:55808\n"
      "s_waitcnt vmcnt(6) lgkmcnt(0)\n"
      "s_barrier\n"
      "v_mfma_f32_16x16x32_bf16 v[144:147], a[160:163], v[64:67], v[144:147]\n"
      "s_or_b32 s67, s67, s78\n"
      "v_pk_mul_f32 v[204:205], v[204:205], v[26:27]\n"
      "v_pk_mul_f32 v[140:141], v[140:141], v[24:25]\n"
      "v_pk_mul_f32 v[142:143], v[142:143], v[24:25]\n"
      "v_cvt_pk_bf16_f32 v140, v140, v141\n"
      "v_cvt_pk_bf16_f32 v141, v142, v143\n"
      "v_pk_mul_f32 v[206:207], v[206:207], v[26:27]\n"
      "v_cvt_pk_bf16_f32 v204, v204, v205\n"
      "v_pk_mul_f32 v[144:145], v[144:145], v[24:25]\n"
      "v_pk_mul_f32 v[146:147], v[146:147], v[24:25]\n"
      "v_cvt_pk_bf16_f32 v205, v206, v207\n"
      "ds_write2st64_b64 v11, v[140:141], v[204:205] offset0:0 offset1:16\n"
      "ds_read_b128 v[48:51], v6 offset:0\n"
      "ds_read_b128 v[52:55], v6 offset:4096\n"
      "ds_read_b128 v[56:59], v6 offset:8192\n"
      "ds_read_b128 v[60:63], v6 offset:12288\n"
      "v_cvt_pk_bf16_f32 v144, v144, v145\n"
      "v_cvt_pk_bf16_f32 v145, v146, v147\n"
      "s_waitcnt lgkmcnt(3)\n"
      "buffer_store_dwordx4 v[48:51], v7, s[48:51], s84 offen offset:0 nt\n"
      "s_waitcnt lgkmcnt(2)\n"
      "buffer_store_dwordx4 v[52:55], v7, s[48:51], s85 offen offset:0 nt\n"
      "s_waitcnt lgkmcnt(1)\n"
      "buffer_store_dwordx4 v[56:59], v7, s[48:51], s86 offen offset:0 nt\n"
      "s_waitcnt lgkmcnt(0)\n"
      "buffer_store_dwordx4 v[60:63], v7, s[48:51], s87 offen offset:0 nt\n"
      "v_mfma_f32_16x16x32_bf16 v[208:211], a[160:163], v[68:71], v[208:211]\n"
      "s_lshl_b32 s68, s68, 1\n"
      "s_nop 0\n"
      "s_nop 0\n"
      "v_mfma_f32_16x16x32_bf16 v[96:99], a[160:163], v[72:75], v[96:99]\n"
      "s_mov_b64 s[70:71], 0\n"
      "ds_read_b64_tr_b16 a[160:161], v2 offset:57344\n"
      "s_nop 0\n"
      "v_mfma_f32_16x16x32_bf16 v[100:103], a[164:167], v[72:75], v[100:103]\n"
      "s_nop 0\n"
      "ds_read_b64_tr_b16 a[162:163], v2 offset:59392\n"
      "v_pk_mul_f32 v[208:209], v[208:209], v[26:27]\n"
      "v_pk_mul_f32 v[210:211], v[210:211], v[26:27]\n"
      "v_cvt_pk_bf16_f32 v208, v208, v209\n"
      "v_cvt_pk_bf16_f32 v209, v210, v211\n"
      "v_pk_mul_f32 v[96:97], v[96:97], v[46:47]\n"
      "v_pk_mul_f32 v[98:99], v[98:99], v[46:47]\n"
      "v_pk_mul_f32 v[100:101], v[100:101], v[46:47]\n"
      "v_pk_mul_f32 v[102:103], v[102:103], v[46:47]\n"
      "ds_write2st64_b64 v8, v[144:145], v[208:209] offset0:2 offset1:18\n"
      "v_cvt_pk_bf16_f32 v96, v96, v97\n"
      "v_cvt_pk_bf16_f32 v97, v98, v99\n"
      "v_cvt_pk_bf16_f32 v100, v100, v101\n"
      "v_cvt_pk_bf16_f32 v101, v102, v103\n"
      "v_mfma_f32_16x16x32_bf16 v[212:215], a[164:167], v[68:71], v[212:215]\n"
      "s_nop 0\n"
      "ds_write2st64_b64 v8, v[80:81], v[96:97] offset0:32 offset1:34\n"
      "ds_write2st64_b64 v9, v[84:85], v[100:101] offset0:32 offset1:34\n"
      "v_mfma_f32_16x16x32_bf16 v[148:151], a[164:167], v[64:67], v[148:151]\n"
      "s_nop 0\n"
      "ds_read_b64_tr_b16 a[164:165], v3 offset:57344\n"
      "s_nop 0\n"
      "v_mfma_f32_16x16x32_bf16 v[152:155], a[168:171], v[64:67], v[152:155]\n"
      "s_nop 0\n"
      "ds_read_b64_tr_b16 a[166:167], v3 offset:59392\n"
      "v_pk_mul_f32 v[212:213], v[212:213], v[26:27]\n"
      "v_pk_mul_f32 v[214:215], v[214:215], v[26:27]\n"
      "v_pk_mul_f32 v[148:149], v[148:149], v[24:25]\n"
      "v_pk_mul_f32 v[150:151], v[150:151], v[24:25]\n"
      "v_cvt_pk_bf16_f32 v148, v148, v149\n"
      "v_cvt_pk_bf16_f32 v149, v150, v151\n"
      "v_pk_mul_f32 v[152:153], v[152:153], v[24:25]\n"
      "v_pk_mul_f32 v[154:155], v[154:155], v[24:25]\n"
      "v_cvt_pk_bf16_f32 v212, v212, v213\n"
      "v_cvt_pk_bf16_f32 v213, v214, v215\n"
      "ds_write2st64_b64 v9, v[148:149], v[212:213] offset0:2 offset1:18\n"
      "v_cvt_pk_bf16_f32 v152, v152, v153\n"
      "v_cvt_pk_bf16_f32 v153, v154, v155\n"
      "v_mfma_f32_16x16x32_bf16 v[216:219], a[168:171], v[68:71], v[216:219]\n"
      "s_nop 0\n"
      "s_nop 0\n"
      "s_nop 0\n"
      "v_mfma_f32_16x16x32_bf16 v[104:107], a[168:171], v[72:75], v[104:107]\n"
      "s_nop 0\n"
      "ds_read_b64_tr_b16 a[168:169], v2 offset:57856\n"
      "s_nop 0\n"
      "v_mfma_f32_16x16x32_bf16 v[108:111], a[172:175], v[72:75], v[108:111]\n"
      "s_nop 0\n"
      "ds_read_b64_tr_b16 a[170:171], v2 offset:59904\n"
      "v_pk_mul_f32 v[216:217], v[216:217], v[26:27]\n"
      "v_pk_mul_f32 v[218:219], v[218:219], v[26:27]\n"
      "v_cvt_pk_bf16_f32 v216, v216, v217\n"
      "v_cvt_pk_bf16_f32 v217, v218, v219\n"
      "v_pk_mul_f32 v[104:105], v[104:105], v[46:47]\n"
      "v_pk_mul_f32 v[106:107], v[106:107], v[46:47]\n"
      "v_pk_mul_f32 v[108:109], v[108:109], v[46:47]\n"
      "v_pk_mul_f32 v[110:111], v[110:111], v[46:47]\n"
      "ds_write2st64_b64 v10, v[152:153], v[216:217] offset0:2 offset1:18\n"
      "v_cvt_pk_bf16_f32 v104, v104, v105\n"
      "v_cvt_pk_bf16_f32 v105, v106, v107\n"
      "ds_write2st64_b64 v10, v[88:89], v[104:105] offset0:32 offset1:34\n"
      "v_cvt_pk_bf16_f32 v108, v108, v109\n"
      "v_cvt_pk_bf16_f32 v109, v110, v111\n"
      "ds_write2st64_b64 v11, v[92:93], v[108:109] offset0:32 offset1:34\n"
      "v_mfma_f32_16x16x32_bf16 v[220:223], a[172:175], v[68:71], v[220:223]\n"
      "s_nop 0\n"
      "s_nop 0\n"
      "s_nop 0\n"
      "v_mfma_f32_16x16x32_bf16 v[156:159], a[172:175], v[64:67], v[156:159]\n"
      "s_waitcnt lgkmcnt(11)\n"
      "ds_read_b64_tr_b16 a[172:173], v3 offset:57856\n"
      "s_nop 0\n"
      "v_mfma_f32_16x16x32_bf16 v[160:163], a[160:163], v[64:67], v[160:163]\n"
      "s_nop 0\n"
      "ds_read_b64_tr_b16 a[174:175], v3 offset:59904\n"
      "v_pk_mul_f32 v[220:221], v[220:221], v[26:27]\n"
      "v_pk_mul_f32 v[222:223], v[222:223], v[26:27]\n"
      "v_pk_mul_f32 v[156:157], v[156:157], v[24:25]\n"
      "v_pk_mul_f32 v[158:159], v[158:159], v[24:25]\n"
      "v_cvt_pk_bf16_f32 v156, v156, v157\n"
      "v_cvt_pk_bf16_f32 v157, v158, v159\n"
      "v_pk_mul_f32 v[160:161], v[160:161], v[24:25]\n"
      "v_pk_mul_f32 v[162:163], v[162:163], v[24:25]\n"
      "v_cvt_pk_bf16_f32 v220, v220, v221\n"
      "v_cvt_pk_bf16_f32 v221, v222, v223\n"
      "ds_write2st64_b64 v11, v[156:157], v[220:221] offset0:2 offset1:18\n"
      "ds_read_b128 v[48:51], v6 offset:1024\n"
      "ds_read_b128 v[52:55], v6 offset:5120\n"
      "ds_read_b128 v[56:59], v6 offset:9216\n"
      "ds_read_b128 v[60:63], v6 offset:13312\n"
      "s_waitcnt lgkmcnt(3)\n"
      "buffer_store_dwordx4 v[48:51], v7, s[48:51], s84 offen offset:128 nt\n"
      "s_waitcnt lgkmcnt(2)\n"
      "buffer_store_dwordx4 v[52:55], v7, s[48:51], s85 offen offset:128 nt\n"
      "s_waitcnt lgkmcnt(1)\n"
      "buffer_store_dwordx4 v[56:59], v7, s[48:51], s86 offen offset:128 nt\n"
      "s_waitcnt lgkmcnt(0)\n"
      "buffer_store_dwordx4 v[60:63], v7, s[48:51], s87 offen offset:128 nt\n"
      "ds_read_b128 v[48:51], v6 offset:16384\n"
      "ds_read_b128 v[52:55], v6 offset:20480\n"
      "ds_read_b128 v[56:59], v6 offset:17408\n"
      "ds_read_b128 v[60:63], v6 offset:21504\n"
      "v_cvt_pk_bf16_f32 v160, v160, v161\n"
      "v_cvt_pk_bf16_f32 v161, v162, v163\n"
      "v_mfma_f32_16x16x32_bf16 v[224:227], a[160:163], v[68:71], v[224:227]\n"
      "s_nop 0\n"
      "s_nop 0\n"
      "s_nop 0\n"
      "v_mfma_f32_16x16x32_bf16 v[112:115], a[160:163], v[72:75], v[112:115]\n"
      "s_nop 0\n"
      "s_waitcnt lgkmcnt(3)\n"
      "buffer_store_dwordx4 v[48:51], v7, s[48:51], s88 offen offset:0 nt\n"
      "s_waitcnt lgkmcnt(2)\n"
      "buffer_store_dwordx4 v[52:55], v7, s[48:51], s89 offen offset:0 nt\n"
      "ds_read_b64_tr_b16 a[160:161], v2 offset:61440\n"
      "v_pk_mul_f32 v[224:225], v[224:225], v[26:27]\n"
      "v_pk_mul_f32 v[226:227], v[226:227], v[26:27]\n"
      "v_pk_mul_f32 v[112:113], v[112:113], v[46:47]\n"
      "v_pk_mul_f32 v[114:115], v[114:115], v[46:47]\n"
      "v_cvt_pk_bf16_f32 v224, v224, v225\n"
      "v_cvt_pk_bf16_f32 v225, v226, v227\n"
      "ds_write2st64_b64 v8, v[160:161], v[224:225] offset0:4 offset1:20\n"
      "s_waitcnt lgkmcnt(3)\n"
      "buffer_store_dwordx4 v[56:59], v7, s[48:51], s88 offen offset:128 nt\n"
      "s_waitcnt lgkmcnt(2)\n"
      "buffer_store_dwordx4 v[60:63], v7, s[48:51], s89 offen offset:128 nt\n"
      "v_cvt_pk_bf16_f32 v112, v112, v113\n"
      "v_cvt_pk_bf16_f32 v113, v114, v115\n"
      "v_mfma_f32_16x16x32_bf16 v[116:119], a[164:167], v[72:75], v[116:119]\n"
      "s_nop 0\n"
      "ds_read_b64_tr_b16 a[162:163], v2 offset:63488\n"
      "s_nop 0\n"
      "v_mfma_f32_16x16x32_bf16 v[228:231], a[164:167], v[68:71], v[228:231]\n"
      "s_nop 0\n"
      "s_nop 0\n"
      "s_nop 0\n"
      "v_mfma_f32_16x16x32_bf16 v[164:167], a[164:167], v[64:67], v[164:167]\n"
      "s_nop 0\n"
      "ds_read_b64_tr_b16 a[164:165], v3 offset:61440\n"
      "s_nop 0\n"
      "v_mfma_f32_16x16x32_bf16 v[168:171], a[168:171], v[64:67], v[168:171]\n"
      "s_nop 0\n"
      "ds_read_b64_tr_b16 a[166:167], v3 offset:63488\n"
      "v_pk_mul_f32 v[228:229], v[228:229], v[26:27]\n"
      "v_pk_mul_f32 v[230:231], v[230:231], v[26:27]\n"
      "v_pk_mul_f32 v[164:165], v[164:165], v[24:25]\n"
      "v_pk_mul_f32 v[166:167], v[166:167], v[24:25]\n"
      "v_pk_mul_f32 v[116:117], v[116:117], v[46:47]\n"
      "v_pk_mul_f32 v[118:119], v[118:119], v[46:47]\n"
      "v_pk_mul_f32 v[168:169], v[168:169], v[24:25]\n"
      "v_pk_mul_f32 v[170:171], v[170:171], v[24:25]\n"
      "v_cvt_pk_bf16_f32 v164, v164, v165\n"
      "v_cvt_pk_bf16_f32 v165, v166, v167\n"
      "v_cvt_pk_bf16_f32 v228, v228, v229\n"
      "v_cvt_pk_bf16_f32 v229, v230, v231\n"
      "ds_write2st64_b64 v9, v[164:165], v[228:229] offset0:4 offset1:20\n"
      "v_cvt_pk_bf16_f32 v168, v168, v169\n"
      "v_cvt_pk_bf16_f32 v169, v170, v171\n"
      "v_cvt_pk_bf16_f32 v116, v116, v117\n"
      "v_cvt_pk_bf16_f32 v117, v118, v119\n"
      "v_mfma_f32_16x16x32_bf16 v[232:235], a[168:171], v[68:71], v[232:235]\n"
      "s_nop 0\n"
      "s_nop 0\n"
      "s_nop 0\n"
      "v_mfma_f32_16x16x32_bf16 v[120:123], a[168:171], v[72:75], v[120:123]\n"
      "s_nop 0\n"
      "ds_read_b64_tr_b16 a[168:169], v2 offset:61952\n"
      "s_nop 0\n"
      "v_mfma_f32_16x16x32_bf16 v[124:127], a[172:175], v[72:75], v[124:127]\n"
      "s_nop 0\n"
      "ds_read_b64_tr_b16 a[170:171], v2 offset:64000\n"
      "v_pk_mul_f32 v[232:233], v[232:233], v[26:27]\n"
      "v_pk_mul_f32 v[234:235], v[234:235], v[26:27]\n"
      "v_cvt_pk_bf16_f32 v232, v232, v233\n"
      "v_cvt_pk_bf16_f32 v233, v234, v235\n"
      "v_pk_mul_f32 v[120:121], v[120:121], v[46:47]\n"
      "v_pk_mul_f32 v[122:123], v[122:123], v[46:47]\n"
      "v_pk_mul_f32 v[124:125], v[124:125], v[46:47]\n"
      "v_pk_mul_f32 v[126:127], v[126:127], v[46:47]\n"
      "ds_write2st64_b64 v10, v[168:169], v[232:233] offset0:4 offset1:20\n"
      "v_cvt_pk_bf16_f32 v120, v120, v121\n"
      "v_cvt_pk_bf16_f32 v121, v122, v123\n"
      "v_cvt_pk_bf16_f32 v124, v124, v125\n"
      "v_cvt_pk_bf16_f32 v125, v126, v127\n"
      "v_mfma_f32_16x16x32_bf16 v[236:239], a[172:175], v[68:71], v[236:239]\n"
      "s_nop 0\n"
      "s_nop 0\n"
      "s_nop 0\n"
      "v_mfma_f32_16x16x32_bf16 v[172:175], a[172:175], v[64:67], v[172:175]\n"
      "s_waitcnt lgkmcnt(6)\n"
      "ds_read_b64_tr_b16 a[172:173], v3 offset:61952\n"
      "s_nop 0\n"
      "v_mfma_f32_16x16x32_bf16 v[176:179], a[160:163], v[64:67], v[176:179]\n"
      "s_nop 0\n"
      "ds_read_b64_tr_b16 a[174:175], v3 offset:64000\n"
      "v_pk_mul_f32 v[236:237], v[236:237], v[26:27]\n"
      "v_pk_mul_f32 v[238:239], v[238:239], v[26:27]\n"
      "v_pk_mul_f32 v[172:173], v[172:173], v[24:25]\n"
      "v_pk_mul_f32 v[174:175], v[174:175], v[24:25]\n"
      "v_cvt_pk_bf16_f32 v172, v172, v173\n"
      "v_cvt_pk_bf16_f32 v173, v174, v175\n"
      "v_pk_mul_f32 v[176:177], v[176:177], v[24:25]\n"
      "v_pk_mul_f32 v[178:179], v[178:179], v[24:25]\n"
      "v_cvt_pk_bf16_f32 v236, v236, v237\n"
      "v_cvt_pk_bf16_f32 v237, v238, v239\n"
      "ds_write2st64_b64 v11, v[172:173], v[236:237] offset0:4 offset1:20\n"
      "ds_read_b128 v[48:51], v6 offset:2048\n"
      "ds_read_b128 v[52:55], v6 offset:6144\n"
      "ds_read_b128 v[56:59], v6 offset:10240\n"
      "ds_read_b128 v[60:63], v6 offset:14336\n"
      "v_cvt_pk_bf16_f32 v176, v176, v177\n"
      "v_cvt_pk_bf16_f32 v177, v178, v179\n"
      "s_waitcnt lgkmcnt(3)\n"
      "buffer_store_dwordx4 v[48:51], v7, s[48:51], s84 offen offset:256 nt\n"
      "s_waitcnt lgkmcnt(2)\n"
      "buffer_store_dwordx4 v[52:55], v7, s[48:51], s85 offen offset:256 nt\n"
      "s_waitcnt lgkmcnt(1)\n"
      "buffer_store_dwordx4 v[56:59], v7, s[48:51], s86 offen offset:256 nt\n"
      "s_waitcnt lgkmcnt(0)\n"
      "buffer_store_dwordx4 v[60:63], v7, s[48:51], s87 offen offset:256 nt\n"
      "v_mfma_f32_16x16x32_bf16 v[240:243], a[160:163], v[68:71], v[240:243]\n"
      "s_nop 0\n"
      "s_nop 0\n"
      "s_nop 0\n"
      "v_mfma_f32_16x16x32_bf16 a[176:179], a[160:163], v[72:75], a[176:179]\n"
      "s_nop 0\n"
      "s_nop 0\n"
      "s_nop 0\n"
      "v_mfma_f32_16x16x32_bf16 a[180:183], a[164:167], v[72:75], a[180:183]\n"
      "s_nop 0\n"
      "v_pk_mul_f32 v[240:241], v[240:241], v[26:27]\n"
      "v_pk_mul_f32 v[242:243], v[242:243], v[26:27]\n"
      "v_cvt_pk_bf16_f32 v240, v240, v241\n"
      "v_cvt_pk_bf16_f32 v241, v242, v243\n"
      "ds_write2st64_b64 v8, v[176:177], v[240:241] offset0:6 offset1:22\n"
      "v_accvgpr_read_b32 v20, a176\n"
      "v_accvgpr_read_b32 v21, a177\n"
      "v_pk_mul_f32 v[20:21], v[20:21], v[46:47]\n"
      "v_accvgpr_read_b32 v22, a178\n"
      "v_accvgpr_read_b32 v23, a179\n"
      "v_pk_mul_f32 v[22:23], v[22:23], v[46:47]\n"
      "v_cvt_pk_bf16_f32 v20, v20, v21\n"
      "v_cvt_pk_bf16_f32 v21, v22, v23\n"
      "ds_write2st64_b64 v8, v[112:113], v[20:21] offset0:36 offset1:38\n"
      "v_accvgpr_read_b32 v16, a180\n"
      "v_accvgpr_read_b32 v17, a181\n"
      "v_pk_mul_f32 v[16:17], v[16:17], v[46:47]\n"
      "v_accvgpr_read_b32 v18, a182\n"
      "v_accvgpr_read_b32 v19, a183\n"
      "v_pk_mul_f32 v[18:19], v[18:19], v[46:47]\n"
      "v_cvt_pk_bf16_f32 v16, v16, v17\n"
      "v_cvt_pk_bf16_f32 v17, v18, v19\n"
      "ds_write2st64_b64 v9, v[116:117], v[16:17] offset0:36 offset1:38\n"
      "v_mfma_f32_16x16x32_bf16 v[244:247], a[164:167], v[68:71], v[244:247]\n"
      "s_nop 0\n"
      "s_nop 0\n"
      "s_nop 0\n"
      "v_mfma_f32_16x16x32_bf16 v[180:183], a[164:167], v[64:67], v[180:183]\n"
      "s_nop 0\n"
      "s_nop 0\n"
      "s_nop 0\n"
      "v_mfma_f32_16x16x32_bf16 v[184:187], a[168:171], v[64:67], v[184:187]\n"
      "s_nop 0\n"
      "v_pk_mul_f32 v[244:245], v[244:245], v[26:27]\n"
      "v_pk_mul_f32 v[246:247], v[246:247], v[26:27]\n"
      "v_cvt_pk_bf16_f32 v244, v244, v245\n"
      "v_pk_mul_f32 v[180:181], v[180:181], v[24:25]\n"
      "v_pk_mul_f32 v[182:183], v[182:183], v[24:25]\n"
      "v_cvt_pk_bf16_f32 v180, v180, v181\n"
      "v_cvt_pk_bf16_f32 v181, v182, v183\n"
      "v_pk_mul_f32 v[184:185], v[184:185], v[24:25]\n"
      "v_pk_mul_f32 v[186:187], v[186:187], v[24:25]\n"
      "v_cvt_pk_bf16_f32 v245, v246, v247\n"
      "ds_write2st64_b64 v9, v[180:181], v[244:245] offset0:6 offset1:22\n"
      "v_cvt_pk_bf16_f32 v184, v184, v185\n"
      "v_cvt_pk_bf16_f32 v185, v186, v187\n"
      "v_mfma_f32_16x16x32_bf16 v[248:251], a[168:171], v[68:71], v[248:251]\n"
      "s_nop 0\n"
      "s_nop 0\n"
      "s_nop 0\n"
      "v_mfma_f32_16x16x32_bf16 a[184:187], a[168:171], v[72:75], a[184:187]\n"
      "s_nop 0\n"
      "s_nop 0\n"
      "s_nop 0\n"
      "v_mfma_f32_16x16x32_bf16 a[188:191], a[172:175], v[72:75], a[188:191]\n"
      "s_nop 0\n"
      "v_pk_mul_f32 v[248:249], v[248:249], v[26:27]\n"
      "v_pk_mul_f32 v[250:251], v[250:251], v[26:27]\n"
      "v_cvt_pk_bf16_f32 v248, v248, v249\n"
      "v_cvt_pk_bf16_f32 v249, v250, v251\n"
      "ds_write2st64_b64 v10, v[184:185], v[248:249] offset0:6 offset1:22\n"
      "v_accvgpr_read_b32 v20, a184\n"
      "v_accvgpr_read_b32 v21, a185\n"
      "v_pk_mul_f32 v[20:21], v[20:21], v[46:47]\n"
      "v_accvgpr_read_b32 v22, a186\n"
      "v_accvgpr_read_b32 v23, a187\n"
      "v_pk_mul_f32 v[22:23], v[22:23], v[46:47]\n"
      "v_cvt_pk_bf16_f32 v20, v20, v21\n"
      "v_cvt_pk_bf16_f32 v21, v22, v23\n"
      "ds_write2st64_b64 v10, v[120:121], v[20:21] offset0:36 offset1:38\n"
      "v_accvgpr_read_b32 v16, a188\n"
      "v_accvgpr_read_b32 v17, a189\n"
      "v_pk_mul_f32 v[16:17], v[16:17], v[46:47]\n"
      "v_accvgpr_read_b32 v18, a190\n"
      "v_accvgpr_read_b32 v19, a191\n"
      "v_pk_mul_f32 v[18:19], v[18:19], v[46:47]\n"
      "v_cvt_pk_bf16_f32 v16, v16, v17\n"
      "v_cvt_pk_bf16_f32 v17, v18, v19\n"
      "ds_write2st64_b64 v11, v[124:125], v[16:17] offset0:36 offset1:38\n"
      "v_mfma_f32_16x16x32_bf16 v[252:255], a[172:175], v[68:71], v[252:255]\n"
      "s_nop 0\n"
      "s_nop 0\n"
      "s_nop 0\n"
      "v_mfma_f32_16x16x32_bf16 v[188:191], a[172:175], v[64:67], v[188:191]\n"
      "s_nop 0\n"
      "s_nop 0\n"
      "s_nop 0\n"
      "s_nop 4\n"
      "v_pk_mul_f32 v[188:189], v[188:189], v[24:25]\n"
      "v_pk_mul_f32 v[190:191], v[190:191], v[24:25]\n"
      "v_cvt_pk_bf16_f32 v188, v188, v189\n"
      "v_cvt_pk_bf16_f32 v189, v190, v191\n"
      "v_pk_mul_f32 v[252:253], v[252:253], v[26:27]\n"
      "v_pk_mul_f32 v[254:255], v[254:255], v[26:27]\n"
      "v_cvt_pk_bf16_f32 v252, v252, v253\n"
      "v_cvt_pk_bf16_f32 v253, v254, v255\n"
      "ds_write2st64_b64 v11, v[188:189], v[252:253] offset0:6 offset1:22\n"
      "ds_read_b128 v[48:51], v6 offset:3072\n"
      "s_waitcnt lgkmcnt(0)\n"
      "buffer_store_dwordx4 v[48:51], v7, s[48:51], s84 offen offset:384 nt\n"
      "ds_read_b128 v[52:55], v6 offset:7168\n"
      "s_waitcnt lgkmcnt(0)\n"
      "buffer_store_dwordx4 v[52:55], v7, s[48:51], s85 offen offset:384 nt\n"
      "ds_read_b128 v[56:59], v6 offset:11264\n"
      "s_waitcnt lgkmcnt(0)\n"
      "buffer_store_dwordx4 v[56:59], v7, s[48:51], s86 offen offset:384 nt\n"
      "ds_read_b128 v[60:63], v6 offset:15360\n"
      "s_waitcnt lgkmcnt(0)\n"
      "buffer_store_dwordx4 v[60:63], v7, s[48:51], s87 offen offset:384 nt\n"
      "ds_read_b128 v[48:51], v6 offset:18432\n"
      "s_waitcnt lgkmcnt(0)\n"
      "buffer_store_dwordx4 v[48:51], v7, s[48:51], s88 offen offset:256 nt\n"
      "ds_read_b128 v[52:55], v6 offset:22528\n"
      "s_waitcnt lgkmcnt(0)\n"
      "buffer_store_dwordx4 v[52:55], v7, s[48:51], s89 offen offset:256 nt\n"
      "ds_read_b128 v[56:59], v6 offset:19456\n"
      "s_waitcnt lgkmcnt(0)\n"
      "buffer_store_dwordx4 v[56:59], v7, s[48:51], s88 offen offset:384 nt\n"
      "ds_read_b128 v[60:63], v6 offset:23552\n"
      "s_waitcnt lgkmcnt(0)\n"
      "buffer_store_dwordx4 v[60:63], v7, s[48:51], s89 offen offset:384 nt\n"
      "LDONE_%=:\n"
      "s_waitcnt vmcnt(0) lgkmcnt(0)\n"
      "s_lshl_b32 s78, s93, 4\n"
      "s_or_b32 s78, s78, s67\n"
      "v_mov_b32 %0, s78\n"
      : "+v"(x)
      : "s"((unsigned long long)__builtin_amdgcn_kernarg_segment_ptr()), "s"(wg), "s"((unsigned)(size_t)lds)
      : "memory", "scc", "vcc", "v1", "v2", "v3", "v4", "v5", "v6", "v7", "v8", "v9", "v10", "v11", "v12", "v13", "v14", "v15", "v16", "v17", "v18", "v19", "v20", "v21", "v22", "v23", "v24", "v25", "v26", "v27", "v28", "v29", "v30", "v31", "v32", "v33", "v34", "v35", "v36", "v37", "v38", "v39", "v40", "v41", "v42", "v43", "v44", "v45", "v46", "v47", "v48", "v49", "v50", "v51", "v52", "v53", "v54", "v55", "v56", "v57", "v58", "v59", "v60", "v61", "v62", "v63", "v64", "v65", "v66", "v67", "v68", "v69", "v70", "v71", "v72", "v73", "v74", "v75", "v76", "v77", "v78", "v79", "v80", "v81", "v82", "v83", "v84", "v85", "v86", "v87", "v88", "v89", "v90", "v91", "v92", "v93", "v94", "v95", "v96", "v97", "v98", "v99", "v100", "v101", "v102", "v103", "v104", "v105", "v106", "v107", "v108", "v109", "v110", "v111", "v112", "v113", "v114", "v115", "v116", "v117", "v118", "v119", "v120", "v121", "v122", "v123", "v124", "v125", "v126", "v127", "v128", "v129", "v130", "v131", "v132", "v133", "v134", "v135", "v136", "v137", "v138", "v139", "v140", "v141", "v142", "v143", "v144", "v145", "v146", "v147", "v148", "v149", "v150", "v151", "v152", "v153", "v154", "v155", "v156", "v157", "v158", "v159", "v160", "v161", "v162", "v163", "v164", "v165", "v166", "v167", "v168", "v169", "v170", "v171", "v172", "v173", "v174", "v175", "v176", "v177", "v178", "v179", "v180", "v181", "v182", "v183", "v184", "v185", "v186", "v187", "v188", "v189", "v190", "v191", "v192", "v193", "v194", "v195", "v196", "v197", "v198", "v199", "v200", "v201", "v202", "v203", "v204", "v205", "v206", "v207", "v208", "v209", "v210", "v211", "v212", "v213", "v214", "v215", "v216", "v217", "v218", "v219", "v220", "v221", "v222", "v223", "v224", "v225", "v226", "v227", "v228", "v229", "v230", "v231", "v232", "v233", "v234", "v235", "v236", "v237", "v238", "v239", "v240", "v241", "v242", "v243", "v244", "v245", "v246", "v247", "v248", "v249", "v250", "v251", "v252", "v253", "v254", "v255", "a0", "a1", "a2", "a3", "a4", "a5", "a6", "a7", "a8", "a9", "a10", "a11", "a12", "a13", "a14", "a15", "a16", "a17", "a18", "a19", "a20", "a21", "a22", "a23", "a24", "a25", "a26", "a27", "a28", "a29", "a30", "a31", "a32", "a33", "a34", "a35", "a36", "a37", "a38", "a39", "a40", "a41", "a42", "a43", "a44", "a45", "a46", "a47", "a48", "a49", "a50", "a51", "a52", "a53", "a54", "a55", "a56", "a57", "a58", "a59", "a60", "a61", "a62", "a63", "a64", "a65", "a66", "a67", "a68", "a69", "a70", "a71", "a72", "a73", "a74", "a75", "a76", "a77", "a78", "a79", "a80", "a81", "a82", "a83", "a84", "a85", "a86", "a87", "a88", "a89", "a90", "a91", "a92", "a93", "a94", "a95", "a96", "a97", "a98", "a99", "a100", "a101", "a102", "a103", "a104", "a105", "a106", "a107", "a108", "a109", "a110", "a111", "a112", "a113", "a114", "a115", "a116", "a117", "a118", "a119", "a120", "a121", "a122", "a123", "a124", "a125", "a126", "a127", "a128", "a129", "a130", "a131", "a132", "a133", "a134", "a135", "a136", "a137", "a138", "a139", "a140", "a141", "a142", "a143", "a144", "a145", "a146", "a147", "a148", "a149", "a150", "a151", "a152", "a153", "a154", "a155", "a156", "a157", "a158", "a159", "a160", "a161", "a162", "a163", "a164", "a165", "a166", "a167", "a168", "a169", "a170", "a171", "a172", "a173", "a174", "a175", "a176", "a177", "a178", "a179", "a180", "a181", "a182", "a183", "a184", "a185", "a186", "a187", "a188", "a189", "a190", "a191", "a192", "a193", "a194", "a195", "a196", "a197", "a198", "a199", "a200", "a201", "a202", "a203", "a204", "a205", "a206", "a207", "a208", "a209", "a210", "a211", "a212", "a213", "a214", "a215", "a216", "a217", "a218", "a219", "a220", "a221", "a222", "a223", "a224", "a225", "a226", "a227", "a228", "a229", "a230", "a231", "a232", "a233", "a234", "a235", "a236", "a237", "a238", "a239", "a240", "a241", "a242", "a243", "a244", "a245", "a246", "a247", "a248", "a249", "a250", "a251", "a252", "a253", "a254", "a255", "s36", "s37", "s38", "s39", "s40", "s41", "s42", "s43", "s44", "s45", "s46", "s47", "s48", "s49", "s50", "s51", "s52", "s53", "s54", "s55", "s56", "s57", "s58", "s59", "s60", "s61", "s62", "s63", "s64", "s65", "s66", "s67", "s68", "s69", "s70", "s71", "s72", "s73", "s74", "s75", "s76", "s77", "s78", "s79", "s80", "s81", "s82", "s83", "s84", "s85", "s86", "s87", "s88", "s89", "s90", "s91", "s92", "s93", "s94", "s95", "s96", "s97", "s98", "s99");
  const unsigned bad = __builtin_amdgcn_readfirstlane(x);
  if (bad & 15u) {
    const unsigned j = wg >> 3, r = j & 3, head0 = (wg & 7) * 32 + (j >> 2) * 4, w = bad >> 4;
    const int lane = __builtin_amdgcn_mbcnt_hi(~0u, __builtin_amdgcn_mbcnt_lo(~0u, 0u));
    for (unsigned p = 0; p < 4; ++p) {
      if (!((bad >> p) & 1u)) continue;
      const unsigned d = pass_desc(head0, r, p), T = d & 7u, nt = (d >> 3) & 31u;
      const size_t e0 = (size_t)(d & ~8191u) / 2, kv = (size_t)(head0 + p) * SK * DD;
      for (unsigned t = w * T; t < w * T + T && t < nt; ++t)
        safe_tile16(Q + e0 + (size_t)t * 16 * DD, K + kv, V + kv, O + e0 + (size_t)t * 16 * DD, lane);
    }
  }
}

extern "C" void kernel_launch(void* const* d_in, const int* in_sizes, int n_in, void* d_out, int out_size, void* d_ws, size_t ws_size,
                              hipStream_t stream) {
  mha_fwd<<<NB * NH, 256, 0, stream>>>((const unsigned short*)d_in[0], (const unsigned short*)d_in[1], (const unsigned short*)d_in[2],
                                       (unsigned short*)d_out);
}
```
